```python
import torch

def _softmax(x, axis=-1):
    return torch.softmax(x, dim=axis)
def _gelu(x):
    return torch.nn.functional.gelu(x, approximate="tanh")
def _silu(x):
    return torch.nn.functional.silu(x)
def _sigmoid(x):
    return torch.sigmoid(x)
def _layernorm(x, axis=-1, eps=1e-5):
    m = x.mean(dim=axis, keepdim=True)
    v = x.var(dim=axis, keepdim=True, unbiased=False)
    return (x - m) / torch.sqrt(v + eps)
def _scatter_add(tgt, idx, src, axis=0):
    return tgt.clone().index_add_(axis, idx, src)
def _segment_sum(src, seg, n):
    out = torch.zeros((n,) + src.shape[1:], dtype=src.dtype)
    return out.index_add_(0, seg, src)
def _pad(x, pw):
    flat = tuple(v for pair in reversed(pw) for v in pair)
    return torch.nn.functional.pad(x, flat)

kernel_name = 'gated_mix__ssm_conv_h3__sliding_local_dilated__prompt'

def setup_inputs():

    _w = (lambda a: (lambda t: t.to(torch.bfloat16) if t.is_floating_point() else t)(torch.as_tensor(a)))
    def _A_setup():
        B, S, D = 1, 2048, 640
        ks = 4
        return {
            "q": _w((lambda *s: torch.randn(*s, dtype=torch.bfloat16) * 0.05)(B, S, D)),
            "k": _w((lambda *s: torch.randn(*s, dtype=torch.bfloat16) * 0.8)(B, S, D)),
            "v": _w((lambda *s: torch.randn(*s, dtype=torch.bfloat16) * 0.8)(B, S, D)),
            "shift_k": _w((lambda *s: torch.randn(*s, dtype=torch.bfloat16) * 0.8)(D, ks) * 0.5),
            "ssm_k": _w((lambda *s: torch.randn(*s, dtype=torch.bfloat16) * 3.2)(D, S) * (1.0 / S ** 0.5)),
            "ssm_D": _w((lambda *s: torch.randn(*s, dtype=torch.bfloat16) * 0.8)(D)),
        }
    _A_in = _A_setup()

    def _B_setup():
        B, S, D = 1, 2048, 640
        return {
            "x": _w((lambda *s: torch.randn(*s, dtype=torch.bfloat16) * 0.05)(B, S, D)),
            "wqkv": _w((lambda *s: torch.randn(*s, dtype=torch.bfloat16) * 0.8)(D, 3 * D)),
            "wo": _w((lambda *s: torch.randn(*s, dtype=torch.bfloat16) * 0.05)(D, D)),
        }
    _B_in = _B_setup()

    return {
        "A_q": _A_in["q"],
        "A_k": _A_in["k"],
        "A_v": _A_in["v"],
        "A_shift_k": _A_in["shift_k"],
        "A_ssm_k": _A_in["ssm_k"],
        "A_ssm_D": _A_in["ssm_D"],
        "B_x": _B_in["x"],
        "B_wqkv": _B_in["wqkv"],
        "B_wo": _B_in["wo"],
        "gate_w": _w((lambda *s: torch.randn(*s, dtype=torch.bfloat16) * 0.05)(640) * 16.0),
    }

def reference(A_q, A_k, A_v, A_shift_k, A_ssm_k, A_ssm_D, B_x, B_wqkv, B_wo, gate_w):
    def _A_ref(q, k, v, shift_k, ssm_k, ssm_D):
        B, S, D = q.shape
        ks = shift_k.shape[-1]
        k_pad = _pad(k, ((0, 0), (ks - 1, 0), (0, 0)))
        k_shift = torch.zeros_like(k)
        for t in range(ks):
            k_shift = k_shift + k_pad[:, t:t + S, :] * shift_k[:, ks - 1 - t][None, None, :]
        kv = k_shift * v
        L = 2 * S
        kv_pad = _pad(kv, ((0, 0), (0, S), (0, 0)))
        kf_pad = _pad(ssm_k, ((0, 0), (0, S)))
        kv_f = (lambda x, axes=(-2,-1): torch.fft.rfft2(x.float(), dim=axes))(kv_pad[:, None, :, :], axes=(1, 2))
        kf_f = (lambda x, axes=(-2,-1): torch.fft.rfft2(x.float(), dim=axes))(kf_pad[:, None, :], axes=(1, 2))
        kf_f = (lambda x, *d: x.permute(*d))(kf_f, 1, 2, 0)
        y_f = kv_f * kf_f[None, :, :, :]
        y = (lambda x, s, axes=(-2,-1): torch.fft.irfft2(x, s=s, dim=axes).to(torch.bfloat16))(y_f, (1, L), axes=(1, 2))[:, 0, :S, :]
        y = y + kv * ssm_D[None, None, :]
        return q * y

    def _B_ref(x, wqkv, wo):
        B, S, D = x.shape
        H = 32; Dh = D // H
        W, dil = 8, 2
        qkv = (lambda x, *d: x.permute(*d))((x @ wqkv).reshape(B, S, 3, H, Dh), 2, 0, 3, 1, 4)
        q, k, v = qkv[0], qkv[1], qkv[2]
        scale = Dh ** -0.5
        pos = torch.arange(S)
        off = torch.arange(W) * dil
        idx = pos[:, None] - off[None, :]
        mask = idx >= 0
        idx_c = torch.where(mask, idx, 0).reshape(-1)
        kg = k[:, :, idx_c, :].reshape(B, H, S, W, Dh)
        vg = v[:, :, idx_c, :].reshape(B, H, S, W, Dh)
        logits = torch.einsum("bhsd,bhswd->bhsw", q, kg) * scale
        logits = torch.where(mask[None, None, :, :], logits, -1e9)
        attn = _softmax(logits, axis=-1)
        o = torch.einsum("bhsw,bhswd->bhsd", attn, vg)
        return (lambda x, *d: x.permute(*d))(o, 0, 2, 1, 3).reshape(B, S, D) @ wo

    x = A_q
    g = _sigmoid((x * gate_w).sum(-1, keepdims=True))
    a = _A_ref(x, A_k, A_v, A_shift_k, A_ssm_k, A_ssm_D)
    b = _B_ref(x, B_wqkv, B_wo)
    return g * a + (1 - g) * b
```

```cpp
#define RETOUCH 1
#define POLL_DELAY 16
#define CODE_LINES 382
#define V_DELAY 6
#define MAP1 1
#define HINT_AT 9
#define VY_DELAY 16
#define GY_DELAY 12
#include <hip/hip_runtime.h>
#include <hip/hip_bf16.h>
#include <stdint.h>

#define S_LEN 2048
#define D_DIM 640
#define NQKV 1920

typedef unsigned short u16;
typedef unsigned long long u64;
typedef __attribute__((ext_vector_type(8))) short bf16x8;
typedef __attribute__((ext_vector_type(4))) short s16x4;
typedef __attribute__((ext_vector_type(4))) float f32x4;
typedef __attribute__((ext_vector_type(2))) float f32x2;
typedef __attribute__((ext_vector_type(4))) unsigned u32x4;
typedef __attribute__((ext_vector_type(2))) unsigned u32x2;
typedef __attribute__((ext_vector_type(2))) __bf16 bf16x2_t;
typedef __attribute__((address_space(3))) s16x4 lds_s16x4;
typedef __attribute__((address_space(3))) const s16x4 lds_cs16x4;
typedef __attribute__((address_space(3))) const bf16x8 lds_cbf16x8;
typedef __attribute__((address_space(3))) char lds_char;

#define __float_as_uint(f) __builtin_bit_cast(unsigned, (float)(f))
#define __uint_as_float(u) __builtin_bit_cast(float, (unsigned)(u))
#define __any(c) (__builtin_amdgcn_ballot_w64(c) != 0)
#define __all(c) (__builtin_amdgcn_ballot_w64(!(c)) == 0)
extern "C" __device__ unsigned long long llvm_dispatch_id() __asm("llvm.amdgcn.dispatch.id");

__device__ __forceinline__ float bf2f(u16 x) { return __uint_as_float(((unsigned)x) << 16); }
__device__ __forceinline__ u16 f2bf(float f) {
    __hip_bfloat16 b = __float2bfloat16(f);
    return *reinterpret_cast<u16*>(&b);
}
__device__ __forceinline__ float rbf(float f) { return bf2f(f2bf(f)); }
__device__ __forceinline__ float lo_f(unsigned u) { return __uint_as_float(u << 16); }
__device__ __forceinline__ float hi_f(unsigned u) { return __uint_as_float(u & 0xffff0000u); }
__device__ __forceinline__ unsigned pack2(float a, float b) {
    return __builtin_bit_cast(unsigned, __builtin_convertvector((f32x2){a, b}, bf16x2_t));
}
__device__ __forceinline__ float dot2(unsigned a, unsigned b, float c) {
    return __builtin_amdgcn_fdot2_f32_bf16(__builtin_bit_cast(bf16x2_t, a), __builtin_bit_cast(bf16x2_t, b), c, false);
}

#define SB() __builtin_amdgcn_sched_barrier(0)
#define WAIT_VM(n) asm volatile("s_waitcnt vmcnt(" #n ")" ::: "memory")
#define RAW_BARRIER() do { asm volatile("s_waitcnt lgkmcnt(0)" ::: "memory"); __builtin_amdgcn_s_barrier(); } while (0)

#define KV_PITCH 4224
#define WS_KV 4096
#define WS_O (WS_KV + D_DIM * KV_PITCH)
#define WS_Y (WS_O + D_DIM * S_LEN * 2)
#define WS_G (WS_Y + D_DIM * S_LEN * 2)
#define WS_XF (WS_G + S_LEN * 4)
#define WS_SF (WS_XF + 128 * 128)
#define WS_HF (WS_SF + 128 * 128)
#define WS_END (WS_HF + 128 * 128)
#define WS_DBG (WS_END + 4096)

#define L_A 0
#define A_SLOT 18432
#define L_W 55296
#define W_SLOT 16384
#define W_PITCH 512
#define NW 6
#define G_XCH 71680
#define G_Y 0
#define G_Q (14 * 10240)
#define G_VX 153600
#define G_HX 158208
#define G_VXD (G_HX + 4608)
#define KV_BYTES 4352
#define V_F (4 * KV_BYTES)
#define XC_ROW 288
#define XC_B 12
#define XC_COPY 4944
#define XC_BYTES (XC_COPY + 17 * XC_ROW)
#define V_KV4 (V_F + 5 * XC_BYTES)
#define V_KV(c) ((c) < 4 ? (c) * KV_BYTES : V_KV4)
#define V_WO (V_KV4 + KV_BYTES - 10240)
#define V_KST (V_WO + 10240 + 1312)
#define V_VST (V_KST + 20608)
#define V_STG 113536
#define V_QS (V_WO + 10240)
#define V_SCR 130560
#define V_XCH 0
#define V_Q 20480
#define V_Y (V_Q + 10240)
#define Y_PITCH 160
#define L_TOUCH 163328
#define L_CTRL 163584
#define LDS_TOTAL 163840
static_assert(V_VST + 20608 <= V_STG && V_STG % 128 == 0 && V_STG + 20480 <= L_TOUCH && V_WO >= V_F + 4 * XC_BYTES && V_WO + 10 * 10240 <= L_TOUCH &&
              V_WO + 6 * 10240 <= V_SCR && V_SCR + 32768 <= L_TOUCH && V_Y + 80 * Y_PITCH <= V_WO && V_WO % 16 == 0, "lds");
static_assert(L_W + NW * W_SLOT <= G_VX && G_VX + 4608 <= G_HX && G_VXD + 520 <= L_CTRL && G_Y + 80 * Y_PITCH <= A_SLOT, "lds");
#define C_FREE (L_CTRL + 16)
#define C_OST (L_CTRL + 48)
#define C_SENT (L_CTRL + 52)
#define C_WO (L_CTRL + 56)
#define C_KV (L_CTRL + 60)
#define C_KVST (L_CTRL + 64)
#define C_CONVN (L_CTRL + 68)
#define C_Y (L_CTRL + 72)
#define C_KVR (L_CTRL + 76)
#define C_XD (L_CTRL + 80)
#define C_RED (L_CTRL + 84)
#define C_ORDY (L_CTRL + 88)
#define C_QR (L_CTRL + 92)
#define C_XCH (L_CTRL + 96)
#define C_PRE (L_CTRL + 112)
#define C_SFC (L_CTRL + 136)
#define C_GO (L_CTRL + 140)
#define C_HR (L_CTRL + 144)
#define C_RED2 (L_CTRL + 148)
#define C_KVD (L_CTRL + 152)
#define C_WT (L_CTRL + 156)
#define C_HX (L_CTRL + 160)

#define KNOB(n, v)
#define SPIN_CAP 4000000
#define GH 0
#define WO_PRIO 0
#define T_PRIO 3
#define C0_PRIO 2
#define C1_PRIO 1
#define L_PRIO 3
#define O_SLEEP 4
#define O_DELAY 0
#define POLL_SLEEP 2
#define Y_SLEEP 8
#define W_SLEEP 4
#define X_SLEEP 2
#define X_DELAY 0
#define CONV_PRIO 2
#define NBUF 4
#define WATCH_RT 2
#define JL 17
#define RING_E0 70
#define RING_F 105
#define WOT_SPLIT 0
#define GO_W C_KVD
#define GO_N 11
#define SENT_PACE 8
#define GATE_LATE 0
#define WOT_PACE 3
#define SENT_W C_KVD
#define SENT_N 11
#define O_AHEAD 4
#define PASS_PRIO 1
#define G_OST 8
#define V_ODELAY 0
#define W_LATE 1
#define Y_SL0 0
#define SF_DELAY 8
#define WO_MALL 1
#define T4_EARLY 0
#define LATE_AT C_LOOP
#define LATE_N 4
#define CQ_X 2
#define KROT ((0x27057250u >> (xcd * 4)) & 15)

#define STAMP(id)
#define FLUSH()
#define CSTAMP(i)

#define TS(i)
#define TSFLUSH()
__device__ __forceinline__ unsigned lds_ld32(unsigned a) {
    unsigned v;
    asm volatile("ds_read_b32 %0, %1\n\ts_waitcnt lgkmcnt(0)" : "=v"(v) : "v"(a) : "memory");
    return v;
}
__device__ __forceinline__ unsigned lds_min4(unsigned a) {
    u32x4 v;
    asm volatile("ds_read_b128 %0, %1\n\ts_waitcnt lgkmcnt(0)" : "=v"(v) : "v"(a) : "memory");
    return min(min(v[0], v[1]), min(v[2], v[3]));
}
__device__ __forceinline__ unsigned lds_min8(unsigned a) {
    u32x4 v, u;
    asm volatile("ds_read_b128 %0, %2\n\tds_read_b128 %1, %2 offset:16\n\ts_waitcnt lgkmcnt(0)" : "=&v"(v), "=&v"(u) : "v"(a) : "memory");
    return min(min(min(v[0], v[1]), min(v[2], v[3])), min(min(u[0], u[1]), min(u[2], u[3])));
}
__device__ __forceinline__ void lds_st32(unsigned a, unsigned v) {
    asm volatile("ds_write_b32 %0, %1" ::"v"(a), "v"(v) : "memory");
}
__device__ __forceinline__ unsigned lds_add_rtn(unsigned a, int lane) {
    unsigned old = 0;
    if (lane == 0) {
        unsigned one = 1;
        asm volatile("ds_add_rtn_u32 %0, %1, %2\n\ts_waitcnt lgkmcnt(0)" : "=v"(old) : "v"(a), "v"(one) : "memory");
    }
    return __builtin_amdgcn_readfirstlane(old);
}
__device__ __forceinline__ void lds_add(unsigned a, int lane, unsigned n = 1) {
    if (lane == 0) asm volatile("ds_add_u32 %0, %1" ::"v"(a), "v"(n) : "memory");
}
__device__ __forceinline__ void spin_min4(unsigned a, unsigned target) {
#pragma nounroll
    for (int i = 0; i < SPIN_CAP; ++i) {
        if (__builtin_amdgcn_readfirstlane(lds_min4(a)) >= target) break;
        __builtin_amdgcn_s_sleep(1);
    }
}
__device__ __forceinline__ void spin_min8(unsigned a, unsigned target) {
#pragma nounroll
    for (int i = 0; i < SPIN_CAP; ++i) {
        if (__builtin_amdgcn_readfirstlane(lds_min8(a)) >= target) break;
        __builtin_amdgcn_s_sleep(1);
    }
}
__device__ __forceinline__ void spin_ge(unsigned a, unsigned target) {
#pragma nounroll
    for (int i = 0; i < SPIN_CAP; ++i) {
        if (__builtin_amdgcn_readfirstlane(lds_ld32(a)) >= target) break;
        __builtin_amdgcn_s_sleep(1);
    }
}
typedef __attribute__((address_space(3))) volatile u32x4 lds_vu32x4;
typedef __attribute__((address_space(3))) volatile unsigned lds_vu32_t;
__device__ __forceinline__ u32x4 lds_v128(unsigned a) { return *(lds_vu32x4*)(size_t)a; }
__device__ __forceinline__ unsigned lds_v32(unsigned a) { return *(lds_vu32_t*)(size_t)a; }
__device__ __forceinline__ float fmax_raw(float a, float b) { return __builtin_elementwise_maximum(a, b); }
__device__ __forceinline__ float fmax3_raw(float a, float b, float c) { return __builtin_elementwise_maximum(__builtin_elementwise_maximum(a, b), c); }
__device__ __forceinline__ unsigned opaque(unsigned v) { asm volatile("" : "+v"(v)); return v; }
__device__ __forceinline__ bf16x8 cat8(s16x4 a, s16x4 b) { return __builtin_shufflevector(a, b, 0, 1, 2, 3, 4, 5, 6, 7); }

__device__ __forceinline__ void poll_flags(__amdgpu_buffer_rsrc_t rs, unsigned off, unsigned tlo, unsigned thi) {
#pragma nounroll
    for (int i = 0; i < SPIN_CAP; ++i) {
        asm volatile("" ::: "memory");
        const u32x2 v = __builtin_amdgcn_raw_buffer_load_b64(rs, off, 0, 16);
        if (__all(v[0] == tlo && v[1] == thi)) break;
        __builtin_amdgcn_s_sleep(POLL_SLEEP);
    }
    __builtin_amdgcn_fence(__ATOMIC_ACQUIRE, "wavefront");
}

__device__ __forceinline__ void spin_slow(unsigned a, unsigned target) {
#pragma nounroll
    for (int i = 0; i < SPIN_CAP; ++i) {
        if (__builtin_amdgcn_readfirstlane(lds_ld32(a)) >= target) break;
        __builtin_amdgcn_s_sleep(6);
    }
}
__device__ __forceinline__ void spin_mid(unsigned a, unsigned target) {
#pragma nounroll
    for (int i = 0; i < SPIN_CAP; ++i) {
        if (__builtin_amdgcn_readfirstlane(lds_ld32(a)) >= target) break;
        __builtin_amdgcn_s_sleep(3);
    }
}
__device__ __forceinline__ void spin_all(unsigned a, unsigned target) {
#pragma nounroll
    for (int i = 0; i < SPIN_CAP; ++i) {
        if (__all(lds_ld32(a) >= target)) break;
        __builtin_amdgcn_s_sleep(1);
    }
}

template <int N> __device__ __forceinline__ void waitvm() { asm volatile("s_waitcnt vmcnt(%0)" ::"n"(N) : "memory"); }
#define BARRIER() asm volatile("s_barrier" ::: "memory")
#define GLS(voff, sbase, lw, ldsoff) asm volatile("s_add_u32 m0, %2, %3\n\ts_nop 0\n\tglobal_load_lds_dwordx4 %0, %1" ::"v"(voff), "s"(sbase), "s"(lw), "n"(ldsoff) : "memory", "scc")
#define GLT(voff, sbase) asm volatile("s_mov_b32 m0, %2\n\ts_nop 0\n\tglobal_load_lds_dword %0, %1" ::"v"(voff), "s"(sbase), "s"(lb + L_TOUCH) : "memory")
#define TRRD(p) __builtin_amdgcn_ds_read_tr16_b64_v4i16((lds_s16x4*)(p))

constexpr int ring_total(int NA) { return 10 * (8 + NA); }
constexpr int ring_need(int J, int NA) { return J < 0 ? NA + 8 : (J & 1) ? (NA + 8) * ((J + 3) / 2) : (NA + 8) * ((J + 2) / 2) + 4 + NA; }
constexpr int ring_isA(int p, int NA) { return p < NA ? 1 : p < NA + 8 ? 0 : (p % (8 + NA) >= 4 && p % (8 + NA) < 4 + NA); }
constexpr int ring_tile(int p, int NA) { return p < NA ? 0 : p < NA + 8 ? (p - NA) / 4 : ring_isA(p, NA) ? p / (8 + NA) : 2 * (p / (8 + NA)) + (p % (8 + NA) >= 4); }
constexpr int ring_sub(int p, int NA) { return p < NA ? p : p < NA + 8 ? (p - NA) % 4 : p % (8 + NA) < 4 ? p % (8 + NA) : p % (8 + NA) < 4 + NA ? p % (8 + NA) - 4 : p % (8 + NA) - 4 - NA; }
constexpr int ring_cum(int J, int NA) {
    int c = J >= 16 ? ring_total(NA) : NA + 8 + (8 + NA) * (2 * RING_E0 + RING_F * (J + 1)) / 200;
    if (c > ring_total(NA)) c = ring_total(NA);
    if (c < ring_need(J, NA)) c = ring_need(J, NA);
    while (ring_isA(c - 1, NA) ? 2 * (ring_tile(c - 1, NA) - 3) + 1 > J - 1 : ring_tile(c - 1, NA) - NW > J - 1) --c;
    return c;
}
static_assert(ring_cum(16, 5) == ring_total(5) && ring_cum(16, 4) == ring_total(4), "ring");
template <int NA, int P0, int P1>
__device__ __forceinline__ void ring_issue(const unsigned (&ev)[9], const void* xp, const void* wp, int kr, unsigned lw) {
    if constexpr (P0 < P1) {
        constexpr int t = ring_tile(P0, NA), sub = ring_sub(P0, NA);
        if constexpr (ring_isA(P0, NA)) {
            const int c = __builtin_amdgcn_readfirstlane(kr + t >= 10 ? kr + t - 10 : kr + t);
            GLS(ev[4 + sub], (const char*)xp + c * 128, lw, L_A + (t % 3) * A_SLOT + sub * 4096);
        } else {
            const int c = __builtin_amdgcn_readfirstlane(2 * kr + t >= 20 ? 2 * kr + t - 20 : 2 * kr + t);
            GLS(ev[sub], (const char*)wp + (size_t)c * (32 * NQKV * 2), lw, L_W + (t % NW) * W_SLOT + sub * 4096);
        }
        ring_issue<NA, P0 + 1, P1>(ev, xp, wp, kr, lw);
    }
}
template <int NA, int J>
__device__ __forceinline__ void ring_run(const unsigned (&ev)[9], const void* xp, const void* wp, int kr, unsigned lw) {
    if constexpr (J >= 0) ring_issue<NA, ring_cum(J - 1, NA), ring_cum(J, NA)>(ev, xp, wp, kr, lw);
    if constexpr (J < 16) {
        waitvm<ring_cum(J, NA) - ring_need(J, NA)>();
        BARRIER();
        ring_run<NA, J + 1>(ev, xp, wp, kr, lw);
    }
}

#define CONV_ASM \
    "s_mov_b32 m0, %[kvl]\n" \
    "v_mov_b32 v50, 1\n" \
    "global_load_lds_dwordx4 %[xo], %[wsp] offset:0 sc1\n" \
    "global_load_lds_dwordx4 %[xo], %[wsp] offset:1024 sc1\n" \
    "global_load_lds_dwordx4 %[xo], %[wsp] offset:2048 sc1\n" \
    "global_load_lds_dwordx4 %[xo], %[wsp] offset:3072 sc1\n" \
    "s_mov_b64 exec, 1\n" \
    "ds_add_u32 %[ctl], v50 offset:80\n" \
    "s_mov_b64 exec, -1\n" \
    "v_add_u32 v84, %[sx4], %[xp]\n" \
    "v_add_u32 v51, %[sh4], %[hp]\n" \
    "v_add_u32 v160, 0, %[xp]\n" \
    "v_add_u32 v161, 1152, %[xp]\n" \
    "v_add_u32 v85, 1152, v84\n" \
    "v_add_u32 v162, 2304, %[xp]\n" \
    "v_add_u32 v86, 2304, v84\n" \
    "v_add_u32 v163, 3456, %[xp]\n" \
    "v_add_u32 v87, 3456, v84\n" \
    "ds_read2_b32 v[88:89], v87 offset0:144 offset1:145\n" \
    "ds_read2_b32 v[90:91], v87 offset0:146 offset1:147\n" \
    "ds_read2_b32 v[92:93], v87 offset0:0 offset1:1\n" \
    "ds_read2_b32 v[94:95], v87 offset0:2 offset1:3\n" \
    "ds_read2_b32 v[96:97], v86 offset0:144 offset1:145\n" \
    "ds_read2_b32 v[98:99], v86 offset0:146 offset1:147\n" \
    "ds_read2_b32 v[100:101], v86 offset0:0 offset1:1\n" \
    "ds_read2_b32 v[102:103], v86 offset0:2 offset1:3\n" \
    "ds_read2_b32 v[104:105], v85 offset0:144 offset1:145\n" \
    "ds_read2_b32 v[106:107], v85 offset0:146 offset1:147\n" \
    "ds_read2_b32 v[108:109], v85 offset0:0 offset1:1\n" \
    "ds_read2_b32 v[110:111], v85 offset0:2 offset1:3\n" \
    "ds_read2_b32 v[112:113], v84 offset0:144 offset1:145\n" \
    "ds_read2_b32 v[114:115], v84 offset0:146 offset1:147\n" \
    "ds_read2_b32 v[116:117], v84 offset0:0 offset1:1\n" \
    "ds_read2_b32 v[118:119], v84 offset0:2 offset1:3\n" \
    "s_mov_b32 %[cnt], 0\n" \
    "Lsp_%=:\n" \
    "ds_read_b32 v50, %[ctl] offset:144\n" \
    "s_add_u32 %[cnt], %[cnt], 1\n" \
    "s_waitcnt lgkmcnt(0)\n" \
    "v_readfirstlane_b32 %[tmp], v50\n" \
    "s_cmp_ge_u32 %[cnt], %[cap]\n" \
    "s_cbranch_scc1 Lgo_%=\n" \
    "s_cmp_lt_u32 %[tmp], 4\n" \
    "s_cbranch_scc1 Lsp_%=\n" \
    "Lgo_%=:\n" \
    "ds_read_b128 v[148:151], v51 offset:3584\n" \
    "ds_read_b128 v[144:147], v51 offset:3072\n" \
    "ds_read_b128 v[140:143], v51 offset:2560\n" \
    "ds_read_b128 v[136:139], v51 offset:2048\n" \
    "ds_read_b128 v[132:135], v51 offset:1536\n" \
    "ds_read_b128 v[128:131], v51 offset:1024\n" \
    "ds_read_b128 v[124:127], v51 offset:512\n" \
    "ds_read_b128 v[120:123], v51 offset:0\n" \
    "s_waitcnt lgkmcnt(7)\n" \
    "v_mfma_f32_16x16x32_bf16 v[80:83], v[88:91], v[148:151], 0\n" \
    "ds_read_b128 v[148:151], v51 offset:3648\n" \
    "s_waitcnt lgkmcnt(7)\n" \
    "v_mfma_f32_16x16x32_bf16 v[76:79], v[88:91], v[144:147], 0\n" \
    "ds_read_b128 v[156:159], v51 offset:576\n" \
    "s_waitcnt lgkmcnt(7)\n" \
    "v_mfma_f32_16x16x32_bf16 v[72:75], v[88:91], v[140:143], 0\n" \
    "ds_read_b128 v[152:155], v51 offset:64\n" \
    "s_waitcnt lgkmcnt(7)\n" \
    "v_mfma_f32_16x16x32_bf16 v[68:71], v[88:91], v[136:139], 0\n" \
    "s_waitcnt lgkmcnt(6)\n" \
    "v_mfma_f32_16x16x32_bf16 v[64:67], v[88:91], v[132:135], 0\n" \
    "s_waitcnt lgkmcnt(5)\n" \
    "v_mfma_f32_16x16x32_bf16 v[60:63], v[88:91], v[128:131], 0\n" \
    "s_waitcnt lgkmcnt(4)\n" \
    "v_mfma_f32_16x16x32_bf16 v[56:59], v[88:91], v[124:127], 0\n" \
    "s_waitcnt lgkmcnt(3)\n" \
    "v_mfma_f32_16x16x32_bf16 v[52:55], v[88:91], v[120:123], 0\n" \
    "ds_read2_b32 v[88:89], v87 offset0:160 offset1:161\n" \
    "v_mfma_f32_16x16x32_bf16 v[80:83], v[92:95], v[144:147], v[80:83]\n" \
    "ds_read2_b32 v[90:91], v87 offset0:162 offset1:163\n" \
    "v_mfma_f32_16x16x32_bf16 v[76:79], v[92:95], v[140:143], v[76:79]\n" \
    "ds_read_b128 v[144:147], v51 offset:3136\n" \
    "v_mfma_f32_16x16x32_bf16 v[72:75], v[92:95], v[136:139], v[72:75]\n" \
    "v_mfma_f32_16x16x32_bf16 v[68:71], v[92:95], v[132:135], v[68:71]\n" \
    "v_mfma_f32_16x16x32_bf16 v[64:67], v[92:95], v[128:131], v[64:67]\n" \
    "v_mfma_f32_16x16x32_bf16 v[60:63], v[92:95], v[124:127], v[60:63]\n" \
    "v_mfma_f32_16x16x32_bf16 v[56:59], v[92:95], v[120:123], v[56:59]\n" \
    "ds_read2_b32 v[92:93], v87 offset0:16 offset1:17\n" \
    "v_mfma_f32_16x16x32_bf16 v[80:83], v[96:99], v[140:143], v[80:83]\n" \
    "ds_read_b128 v[140:143], v51 offset:2624\n" \
    "v_mfma_f32_16x16x32_bf16 v[76:79], v[96:99], v[136:139], v[76:79]\n" \
    "ds_read2_b32 v[94:95], v87 offset0:18 offset1:19\n" \
    "v_mfma_f32_16x16x32_bf16 v[72:75], v[96:99], v[132:135], v[72:75]\n" \
    "v_mfma_f32_16x16x32_bf16 v[68:71], v[96:99], v[128:131], v[68:71]\n" \
    "v_mfma_f32_16x16x32_bf16 v[64:67], v[96:99], v[124:127], v[64:67]\n" \
    "v_mfma_f32_16x16x32_bf16 v[60:63], v[96:99], v[120:123], v[60:63]\n" \
    "ds_read2_b32 v[96:97], v86 offset0:160 offset1:161\n" \
    "v_mfma_f32_16x16x32_bf16 v[80:83], v[100:103], v[136:139], v[80:83]\n" \
    "ds_read_b128 v[136:139], v51 offset:2112\n" \
    "v_mfma_f32_16x16x32_bf16 v[76:79], v[100:103], v[132:135], v[76:79]\n" \
    "ds_read2_b32 v[98:99], v86 offset0:162 offset1:163\n" \
    "v_mfma_f32_16x16x32_bf16 v[72:75], v[100:103], v[128:131], v[72:75]\n" \
    "v_mfma_f32_16x16x32_bf16 v[68:71], v[100:103], v[124:127], v[68:71]\n" \
    "v_mfma_f32_16x16x32_bf16 v[64:67], v[100:103], v[120:123], v[64:67]\n" \
    "ds_read2_b32 v[100:101], v86 offset0:16 offset1:17\n" \
    "v_mfma_f32_16x16x32_bf16 v[80:83], v[104:107], v[132:135], v[80:83]\n" \
    "ds_read_b128 v[132:135], v51 offset:1600\n" \
    "v_mfma_f32_16x16x32_bf16 v[76:79], v[104:107], v[128:131], v[76:79]\n" \
    "ds_read2_b32 v[102:103], v86 offset0:18 offset1:19\n" \
    "v_mfma_f32_16x16x32_bf16 v[72:75], v[104:107], v[124:127], v[72:75]\n" \
    "v_mfma_f32_16x16x32_bf16 v[68:71], v[104:107], v[120:123], v[68:71]\n" \
    "ds_read2_b32 v[104:105], v85 offset0:160 offset1:161\n" \
    "v_mfma_f32_16x16x32_bf16 v[80:83], v[108:111], v[128:131], v[80:83]\n" \
    "ds_read_b128 v[128:131], v51 offset:1088\n" \
    "v_mfma_f32_16x16x32_bf16 v[76:79], v[108:111], v[124:127], v[76:79]\n" \
    "ds_read2_b32 v[106:107], v85 offset0:162 offset1:163\n" \
    "v_mfma_f32_16x16x32_bf16 v[72:75], v[108:111], v[120:123], v[72:75]\n" \
    "ds_read2_b32 v[108:109], v85 offset0:16 offset1:17\n" \
    "v_mfma_f32_16x16x32_bf16 v[80:83], v[112:115], v[124:127], v[80:83]\n" \
    "ds_read2_b32 v[110:111], v85 offset0:18 offset1:19\n" \
    "v_mfma_f32_16x16x32_bf16 v[76:79], v[112:115], v[120:123], v[76:79]\n" \
    "ds_read2_b32 v[112:113], v84 offset0:160 offset1:161\n" \
    "v_mfma_f32_16x16x32_bf16 v[80:83], v[116:119], v[120:123], v[80:83]\n" \
    "ds_read2_b32 v[114:115], v84 offset0:162 offset1:163\n" \
    "s_waitcnt lgkmcnt(15)\n" \
    "v_mfma_f32_16x16x32_bf16 v[80:83], v[88:91], v[148:151], v[80:83]\n" \
    "ds_read2_b32 v[116:117], v84 offset0:16 offset1:17\n" \
    "v_mfma_f32_16x16x32_bf16 v[76:79], v[88:91], v[144:147], v[76:79]\n" \
    "ds_read2_b32 v[118:119], v84 offset0:18 offset1:19\n" \
    "s_waitcnt lgkmcnt(15)\n" \
    "v_mfma_f32_16x16x32_bf16 v[72:75], v[88:91], v[140:143], v[72:75]\n" \
    "s_waitcnt vmcnt(0)\n" \
    "ds_read_b128 v[148:151], %[hp] offset:3584\n" \
    "s_waitcnt lgkmcnt(14)\n" \
    "v_mfma_f32_16x16x32_bf16 v[68:71], v[88:91], v[136:139], v[68:71]\n" \
    "ds_read_b128 v[124:127], %[hp] offset:512\n" \
    "s_waitcnt lgkmcnt(12)\n" \
    "v_mfma_f32_16x16x32_bf16 v[64:67], v[88:91], v[132:135], v[64:67]\n" \
    "ds_read_b128 v[120:123], %[hp] offset:0\n" \
    "s_waitcnt lgkmcnt(10)\n" \
    "v_mfma_f32_16x16x32_bf16 v[60:63], v[88:91], v[128:131], v[60:63]\n" \
    "v_mfma_f32_16x16x32_bf16 v[56:59], v[88:91], v[156:159], v[56:59]\n" \
    "v_mfma_f32_16x16x32_bf16 v[52:55], v[88:91], v[152:155], v[52:55]\n" \
    "ds_read2_b32 v[88:89], v163 offset0:144 offset1:145\n" \
    "v_mfma_f32_16x16x32_bf16 v[80:83], v[92:95], v[144:147], v[80:83]\n" \
    "ds_read2_b32 v[90:91], v163 offset0:146 offset1:147\n" \
    "v_mfma_f32_16x16x32_bf16 v[76:79], v[92:95], v[140:143], v[76:79]\n" \
    "ds_read_b128 v[144:147], %[hp] offset:3072\n" \
    "v_mfma_f32_16x16x32_bf16 v[72:75], v[92:95], v[136:139], v[72:75]\n" \
    "v_mfma_f32_16x16x32_bf16 v[68:71], v[92:95], v[132:135], v[68:71]\n" \
    "ds_write_b128 %[scr], v[52:55] offset:0\n" \
    "v_mfma_f32_16x16x32_bf16 v[64:67], v[92:95], v[128:131], v[64:67]\n" \
    "ds_read_b64 v[86:87], %[kvr] offset:0\n" \
    "v_mfma_f32_16x16x32_bf16 v[60:63], v[92:95], v[156:159], v[60:63]\n" \
    "v_mfma_f32_16x16x32_bf16 v[56:59], v[92:95], v[152:155], v[56:59]\n" \
    "ds_read2_b32 v[92:93], v163 offset0:0 offset1:1\n" \
    "v_mfma_f32_16x16x32_bf16 v[80:83], v[96:99], v[140:143], v[80:83]\n" \
    "ds_read_b128 v[140:143], %[hp] offset:2560\n" \
    "v_mfma_f32_16x16x32_bf16 v[76:79], v[96:99], v[136:139], v[76:79]\n" \
    "ds_read2_b32 v[94:95], v163 offset0:2 offset1:3\n" \
    "v_mfma_f32_16x16x32_bf16 v[72:75], v[96:99], v[132:135], v[72:75]\n" \
    "v_mfma_f32_16x16x32_bf16 v[68:71], v[96:99], v[128:131], v[68:71]\n" \
    "ds_write_b128 %[scr], v[56:59] offset:1024\n" \
    "v_mfma_f32_16x16x32_bf16 v[64:67], v[96:99], v[156:159], v[64:67]\n" \
    "v_mfma_f32_16x16x32_bf16 v[60:63], v[96:99], v[152:155], v[60:63]\n" \
    "ds_read2_b32 v[96:97], v162 offset0:144 offset1:145\n" \
    "v_mfma_f32_16x16x32_bf16 v[80:83], v[100:103], v[136:139], v[80:83]\n" \
    "ds_read_b128 v[136:139], %[hp] offset:2048\n" \
    "v_mfma_f32_16x16x32_bf16 v[76:79], v[100:103], v[132:135], v[76:79]\n" \
    "ds_read2_b32 v[98:99], v162 offset0:146 offset1:147\n" \
    "v_mfma_f32_16x16x32_bf16 v[72:75], v[100:103], v[128:131], v[72:75]\n" \
    "v_mfma_f32_16x16x32_bf16 v[68:71], v[100:103], v[156:159], v[68:71]\n" \
    "s_waitcnt lgkmcnt(7)\n" \
    "v_lshlrev_b32 v84, 16, v86\n" \
    "v_mfma_f32_16x16x32_bf16 v[64:67], v[100:103], v[152:155], v[64:67]\n" \
    "ds_read2_b32 v[100:101], v162 offset0:0 offset1:1\n" \
    "v_mfma_f32_16x16x32_bf16 v[80:83], v[104:107], v[132:135], v[80:83]\n" \
    "ds_read_b128 v[132:135], %[hp] offset:1536\n" \
    "v_and_b32 v85, 0xffff0000, v86\n" \
    "v_mfma_f32_16x16x32_bf16 v[76:79], v[104:107], v[128:131], v[76:79]\n" \
    "ds_read2_b32 v[102:103], v162 offset0:2 offset1:3\n" \
    "v_mfma_f32_16x16x32_bf16 v[72:75], v[104:107], v[156:159], v[72:75]\n" \
    "v_lshlrev_b32 v86, 16, v87\n" \
    "v_mfma_f32_16x16x32_bf16 v[68:71], v[104:107], v[152:155], v[68:71]\n" \
    "ds_read2_b32 v[104:105], v161 offset0:144 offset1:145\n" \
    "v_mfma_f32_16x16x32_bf16 v[80:83], v[108:111], v[128:131], v[80:83]\n" \
    "ds_read_b128 v[128:131], %[hp] offset:1024\n" \
    "v_and_b32 v87, 0xffff0000, v87\n" \
    "v_mfma_f32_16x16x32_bf16 v[76:79], v[108:111], v[156:159], v[76:79]\n" \
    "ds_read2_b32 v[106:107], v161 offset0:146 offset1:147\n" \
    "v_mfma_f32_16x16x32_bf16 v[72:75], v[108:111], v[152:155], v[72:75]\n" \
    "ds_read2_b32 v[108:109], v161 offset0:0 offset1:1\n" \
    "v_mfma_f32_16x16x32_bf16 v[80:83], v[112:115], v[156:159], v[80:83]\n" \
    "ds_read2_b32 v[110:111], v161 offset0:2 offset1:3\n" \
    "v_mfma_f32_16x16x32_bf16 v[76:79], v[112:115], v[152:155], v[76:79]\n" \
    "ds_read2_b32 v[112:113], v160 offset0:144 offset1:145\n" \
    "v_mfma_f32_16x16x32_bf16 v[80:83], v[116:119], v[152:155], v[80:83]\n" \
    "ds_read2_b32 v[114:115], v160 offset0:146 offset1:147\n" \
    "v_mfma_f32_16x16x32_bf16 v[44:47], v[88:91], v[148:151], 0\n" \
    "ds_read2_b32 v[116:117], v160 offset0:0 offset1:1\n" \
    "v_mfma_f32_16x16x32_bf16 v[40:43], v[88:91], v[144:147], 0\n" \
    "ds_read2_b32 v[118:119], v160 offset0:2 offset1:3\n" \
    "s_waitcnt lgkmcnt(15)\n" \
    "v_mfma_f32_16x16x32_bf16 v[36:39], v[88:91], v[140:143], 0\n" \
    "ds_read_b128 v[148:151], %[hp] offset:3648\n" \
    "s_waitcnt lgkmcnt(14)\n" \
    "v_mfma_f32_16x16x32_bf16 v[32:35], v[88:91], v[136:139], 0\n" \
    "ds_read_b128 v[156:159], %[hp] offset:576\n" \
    "s_waitcnt lgkmcnt(12)\n" \
    "v_mfma_f32_16x16x32_bf16 v[28:31], v[88:91], v[132:135], 0\n" \
    "ds_read_b128 v[152:155], %[hp] offset:64\n" \
    "s_waitcnt lgkmcnt(10)\n" \
    "v_mfma_f32_16x16x32_bf16 v[24:27], v[88:91], v[128:131], 0\n" \
    "ds_write_b128 %[scr], v[60:63] offset:2048\n" \
    "v_mfma_f32_16x16x32_bf16 v[20:23], v[88:91], v[124:127], 0\n" \
    "v_mfma_f32_16x16x32_bf16 v[16:19], v[88:91], v[120:123], 0\n" \
    "ds_read2_b32 v[88:89], v163 offset0:160 offset1:161\n" \
    "v_mfma_f32_16x16x32_bf16 v[44:47], v[92:95], v[144:147], v[44:47]\n" \
    "ds_read2_b32 v[90:91], v163 offset0:162 offset1:163\n" \
    "v_mfma_f32_16x16x32_bf16 v[40:43], v[92:95], v[140:143], v[40:43]\n" \
    "ds_read_b128 v[144:147], %[hp] offset:3136\n" \
    "v_mfma_f32_16x16x32_bf16 v[36:39], v[92:95], v[136:139], v[36:39]\n" \
    "v_fmac_f32 v16, %[dd], v84\n" \
    "v_fmac_f32 v17, %[dd], v85\n" \
    "v_mfma_f32_16x16x32_bf16 v[32:35], v[92:95], v[132:135], v[32:35]\n" \
    "v_fmac_f32 v18, %[dd], v86\n" \
    "v_fmac_f32 v19, %[dd], v87\n" \
    "v_mfma_f32_16x16x32_bf16 v[28:31], v[92:95], v[128:131], v[28:31]\n" \
    "ds_read_b64 v[86:87], %[kvr] offset:512\n" \
    "v_mfma_f32_16x16x32_bf16 v[24:27], v[92:95], v[124:127], v[24:27]\n" \
    "ds_write_b128 %[scr], v[64:67] offset:3072\n" \
    "v_mfma_f32_16x16x32_bf16 v[20:23], v[92:95], v[120:123], v[20:23]\n" \
    "ds_read2_b32 v[92:93], v163 offset0:16 offset1:17\n" \
    "v_mfma_f32_16x16x32_bf16 v[44:47], v[96:99], v[140:143], v[44:47]\n" \
    "ds_read_b128 v[140:143], %[hp] offset:2624\n" \
    "v_mfma_f32_16x16x32_bf16 v[40:43], v[96:99], v[136:139], v[40:43]\n" \
    "ds_read2_b32 v[94:95], v163 offset0:18 offset1:19\n" \
    "v_mfma_f32_16x16x32_bf16 v[36:39], v[96:99], v[132:135], v[36:39]\n" \
    "ds_write_b128 %[scr], v[68:71] offset:4096\n" \
    "v_mfma_f32_16x16x32_bf16 v[32:35], v[96:99], v[128:131], v[32:35]\n" \
    "v_mfma_f32_16x16x32_bf16 v[28:31], v[96:99], v[124:127], v[28:31]\n" \
    "v_mfma_f32_16x16x32_bf16 v[24:27], v[96:99], v[120:123], v[24:27]\n" \
    "ds_read2_b32 v[96:97], v162 offset0:160 offset1:161\n" \
    "v_mfma_f32_16x16x32_bf16 v[44:47], v[100:103], v[136:139], v[44:47]\n" \
    "ds_read_b128 v[136:139], %[hp] offset:2112\n" \
    "v_mfma_f32_16x16x32_bf16 v[40:43], v[100:103], v[132:135], v[40:43]\n" \
    "ds_read2_b32 v[98:99], v162 offset0:162 offset1:163\n" \
    "v_mfma_f32_16x16x32_bf16 v[36:39], v[100:103], v[128:131], v[36:39]\n" \
    "ds_write_b128 %[scr], v[72:75] offset:5120\n" \
    "v_mfma_f32_16x16x32_bf16 v[32:35], v[100:103], v[124:127], v[32:35]\n" \
    "s_waitcnt lgkmcnt(9)\n" \
    "v_lshlrev_b32 v84, 16, v86\n" \
    "v_mfma_f32_16x16x32_bf16 v[28:31], v[100:103], v[120:123], v[28:31]\n" \
    "ds_read2_b32 v[100:101], v162 offset0:16 offset1:17\n" \
    "v_mfma_f32_16x16x32_bf16 v[44:47], v[104:107], v[132:135], v[44:47]\n" \
    "ds_read_b128 v[132:135], %[hp] offset:1600\n" \
    "v_and_b32 v85, 0xffff0000, v86\n" \
    "v_mfma_f32_16x16x32_bf16 v[40:43], v[104:107], v[128:131], v[40:43]\n" \
    "ds_read2_b32 v[102:103], v162 offset0:18 offset1:19\n" \
    "v_mfma_f32_16x16x32_bf16 v[36:39], v[104:107], v[124:127], v[36:39]\n" \
    "v_lshlrev_b32 v86, 16, v87\n" \
    "v_mfma_f32_16x16x32_bf16 v[32:35], v[104:107], v[120:123], v[32:35]\n" \
    "ds_read2_b32 v[104:105], v161 offset0:160 offset1:161\n" \
    "v_mfma_f32_16x16x32_bf16 v[44:47], v[108:111], v[128:131], v[44:47]\n" \
    "ds_read_b128 v[128:131], %[hp] offset:1088\n" \
    "v_and_b32 v87, 0xffff0000, v87\n" \
    "v_mfma_f32_16x16x32_bf16 v[40:43], v[108:111], v[124:127], v[40:43]\n" \
    "ds_read2_b32 v[106:107], v161 offset0:162 offset1:163\n" \
    "v_mfma_f32_16x16x32_bf16 v[36:39], v[108:111], v[120:123], v[36:39]\n" \
    "ds_read2_b32 v[108:109], v161 offset0:16 offset1:17\n" \
    "v_mfma_f32_16x16x32_bf16 v[44:47], v[112:115], v[124:127], v[44:47]\n" \
    "ds_read2_b32 v[110:111], v161 offset0:18 offset1:19\n" \
    "v_mfma_f32_16x16x32_bf16 v[40:43], v[112:115], v[120:123], v[40:43]\n" \
    "ds_read2_b32 v[112:113], v160 offset0:160 offset1:161\n" \
    "v_mfma_f32_16x16x32_bf16 v[44:47], v[116:119], v[120:123], v[44:47]\n" \
    "ds_read2_b32 v[114:115], v160 offset0:162 offset1:163\n" \
    "v_mfma_f32_16x16x32_bf16 v[44:47], v[88:91], v[148:151], v[44:47]\n" \
    "ds_read2_b32 v[116:117], v160 offset0:16 offset1:17\n" \
    "v_mfma_f32_16x16x32_bf16 v[40:43], v[88:91], v[144:147], v[40:43]\n" \
    "ds_read2_b32 v[118:119], v160 offset0:18 offset1:19\n" \
    "s_waitcnt lgkmcnt(15)\n" \
    "v_mfma_f32_16x16x32_bf16 v[36:39], v[88:91], v[140:143], v[36:39]\n" \
    "ds_read_b128 v[148:151], %[hp] offset:3712\n" \
    "s_waitcnt lgkmcnt(15)\n" \
    "v_mfma_f32_16x16x32_bf16 v[32:35], v[88:91], v[136:139], v[32:35]\n" \
    "ds_read_b128 v[124:127], %[hp] offset:640\n" \
    "s_waitcnt lgkmcnt(12)\n" \
    "v_mfma_f32_16x16x32_bf16 v[28:31], v[88:91], v[132:135], v[28:31]\n" \
    "ds_read_b128 v[120:123], %[hp] offset:128\n" \
    "s_waitcnt lgkmcnt(10)\n" \
    "v_mfma_f32_16x16x32_bf16 v[24:27], v[88:91], v[128:131], v[24:27]\n" \
    "ds_write_b128 %[scr], v[76:79] offset:6144\n" \
    "v_mfma_f32_16x16x32_bf16 v[20:23], v[88:91], v[156:159], v[20:23]\n" \
    "v_mfma_f32_16x16x32_bf16 v[16:19], v[88:91], v[152:155], v[16:19]\n" \
    "ds_read2_b32 v[88:89], v163 offset0:176 offset1:177\n" \
    "v_mfma_f32_16x16x32_bf16 v[44:47], v[92:95], v[144:147], v[44:47]\n" \
    "ds_read2_b32 v[90:91], v163 offset0:178 offset1:179\n" \
    "v_mfma_f32_16x16x32_bf16 v[40:43], v[92:95], v[140:143], v[40:43]\n" \
    "ds_read_b128 v[144:147], %[hp] offset:3200\n" \
    "v_fmac_f32 v20, %[dd], v84\n" \
    "v_fmac_f32 v21, %[dd], v85\n" \
    "v_mfma_f32_16x16x32_bf16 v[36:39], v[92:95], v[136:139], v[36:39]\n" \
    "v_fmac_f32 v22, %[dd], v86\n" \
    "v_fmac_f32 v23, %[dd], v87\n" \
    "v_mfma_f32_16x16x32_bf16 v[32:35], v[92:95], v[132:135], v[32:35]\n" \
    "ds_write_b128 %[scr], v[80:83] offset:7168\n" \
    "v_mov_b32 v50, 1\n" \
    "s_mov_b64 exec, 1\n" \
    "ds_add_u32 %[ctl], v50 offset:84\n" \
    "s_mov_b64 exec, -1\n" \
    "v_mfma_f32_16x16x32_bf16 v[28:31], v[92:95], v[128:131], v[28:31]\n" \
    "ds_read_b64 v[86:87], %[kvr] offset:1024\n" \
    "v_mfma_f32_16x16x32_bf16 v[24:27], v[92:95], v[156:159], v[24:27]\n" \
    "v_mfma_f32_16x16x32_bf16 v[20:23], v[92:95], v[152:155], v[20:23]\n" \
    "ds_read2_b32 v[92:93], v163 offset0:32 offset1:33\n" \
    "v_mfma_f32_16x16x32_bf16 v[44:47], v[96:99], v[140:143], v[44:47]\n" \
    "ds_read_b128 v[140:143], %[hp] offset:2688\n" \
    "v_mfma_f32_16x16x32_bf16 v[40:43], v[96:99], v[136:139], v[40:43]\n" \
    "ds_read2_b32 v[94:95], v163 offset0:34 offset1:35\n" \
    "v_mfma_f32_16x16x32_bf16 v[36:39], v[96:99], v[132:135], v[36:39]\n" \
    "v_mfma_f32_16x16x32_bf16 v[32:35], v[96:99], v[128:131], v[32:35]\n" \
    "v_mfma_f32_16x16x32_bf16 v[28:31], v[96:99], v[156:159], v[28:31]\n" \
    "v_mfma_f32_16x16x32_bf16 v[24:27], v[96:99], v[152:155], v[24:27]\n" \
    "ds_read2_b32 v[96:97], v162 offset0:176 offset1:177\n" \
    "v_mfma_f32_16x16x32_bf16 v[44:47], v[100:103], v[136:139], v[44:47]\n" \
    "ds_read_b128 v[136:139], %[hp] offset:2176\n" \
    "v_mfma_f32_16x16x32_bf16 v[40:43], v[100:103], v[132:135], v[40:43]\n" \
    "ds_read2_b32 v[98:99], v162 offset0:178 offset1:179\n" \
    "v_mfma_f32_16x16x32_bf16 v[36:39], v[100:103], v[128:131], v[36:39]\n" \
    "v_mfma_f32_16x16x32_bf16 v[32:35], v[100:103], v[156:159], v[32:35]\n" \
    "s_waitcnt lgkmcnt(6)\n" \
    "v_lshlrev_b32 v84, 16, v86\n" \
    "v_mfma_f32_16x16x32_bf16 v[28:31], v[100:103], v[152:155], v[28:31]\n" \
    "ds_read2_b32 v[100:101], v162 offset0:32 offset1:33\n" \
    "v_mfma_f32_16x16x32_bf16 v[44:47], v[104:107], v[132:135], v[44:47]\n" \
    "ds_read_b128 v[132:135], %[hp] offset:1664\n" \
    "v_and_b32 v85, 0xffff0000, v86\n" \
    "v_mfma_f32_16x16x32_bf16 v[40:43], v[104:107], v[128:131], v[40:43]\n" \
    "ds_read2_b32 v[102:103], v162 offset0:34 offset1:35\n" \
    "v_mfma_f32_16x16x32_bf16 v[36:39], v[104:107], v[156:159], v[36:39]\n" \
    "v_lshlrev_b32 v86, 16, v87\n" \
    "v_mfma_f32_16x16x32_bf16 v[32:35], v[104:107], v[152:155], v[32:35]\n" \
    "ds_read2_b32 v[104:105], v161 offset0:176 offset1:177\n" \
    "v_mfma_f32_16x16x32_bf16 v[44:47], v[108:111], v[128:131], v[44:47]\n" \
    "ds_read_b128 v[128:131], %[hp] offset:1152\n" \
    "v_and_b32 v87, 0xffff0000, v87\n" \
    "v_mfma_f32_16x16x32_bf16 v[40:43], v[108:111], v[156:159], v[40:43]\n" \
    "ds_read2_b32 v[106:107], v161 offset0:178 offset1:179\n" \
    "v_mfma_f32_16x16x32_bf16 v[36:39], v[108:111], v[152:155], v[36:39]\n" \
    "ds_read2_b32 v[108:109], v161 offset0:32 offset1:33\n" \
    "v_mfma_f32_16x16x32_bf16 v[44:47], v[112:115], v[156:159], v[44:47]\n" \
    "ds_read2_b32 v[110:111], v161 offset0:34 offset1:35\n" \
    "v_mfma_f32_16x16x32_bf16 v[40:43], v[112:115], v[152:155], v[40:43]\n" \
    "ds_read2_b32 v[112:113], v160 offset0:176 offset1:177\n" \
    "v_mfma_f32_16x16x32_bf16 v[44:47], v[116:119], v[152:155], v[44:47]\n" \
    "ds_read2_b32 v[114:115], v160 offset0:178 offset1:179\n" \
    "v_mfma_f32_16x16x32_bf16 v[44:47], v[88:91], v[148:151], v[44:47]\n" \
    "ds_read2_b32 v[116:117], v160 offset0:32 offset1:33\n" \
    "v_mfma_f32_16x16x32_bf16 v[40:43], v[88:91], v[144:147], v[40:43]\n" \
    "ds_read2_b32 v[118:119], v160 offset0:34 offset1:35\n" \
    "s_waitcnt lgkmcnt(15)\n" \
    "v_mfma_f32_16x16x32_bf16 v[36:39], v[88:91], v[140:143], v[36:39]\n" \
    "ds_read_b128 v[148:151], %[hp] offset:3776\n" \
    "s_waitcnt lgkmcnt(14)\n" \
    "v_mfma_f32_16x16x32_bf16 v[32:35], v[88:91], v[136:139], v[32:35]\n" \
    "ds_read_b128 v[156:159], %[hp] offset:704\n" \
    "s_waitcnt lgkmcnt(12)\n" \
    "v_mfma_f32_16x16x32_bf16 v[28:31], v[88:91], v[132:135], v[28:31]\n" \
    "ds_read_b128 v[152:155], %[hp] offset:192\n" \
    "s_waitcnt lgkmcnt(10)\n" \
    "v_mfma_f32_16x16x32_bf16 v[24:27], v[88:91], v[128:131], v[24:27]\n" \
    "v_mfma_f32_16x16x32_bf16 v[20:23], v[88:91], v[124:127], v[20:23]\n" \
    "v_mfma_f32_16x16x32_bf16 v[16:19], v[88:91], v[120:123], v[16:19]\n" \
    "ds_read2_b32 v[88:89], v163 offset0:192 offset1:193\n" \
    "v_mfma_f32_16x16x32_bf16 v[44:47], v[92:95], v[144:147], v[44:47]\n" \
    "ds_read2_b32 v[90:91], v163 offset0:194 offset1:195\n" \
    "v_fmac_f32 v24, %[dd], v84\n" \
    "v_fmac_f32 v25, %[dd], v85\n" \
    "v_mfma_f32_16x16x32_bf16 v[40:43], v[92:95], v[140:143], v[40:43]\n" \
    "ds_read_b128 v[144:147], %[hp] offset:3264\n" \
    "v_fmac_f32 v26, %[dd], v86\n" \
    "v_fmac_f32 v27, %[dd], v87\n" \
    "v_mfma_f32_16x16x32_bf16 v[36:39], v[92:95], v[136:139], v[36:39]\n" \
    "v_mfma_f32_16x16x32_bf16 v[32:35], v[92:95], v[132:135], v[32:35]\n" \
    "v_mfma_f32_16x16x32_bf16 v[28:31], v[92:95], v[128:131], v[28:31]\n" \
    "ds_read_b64 v[86:87], %[kvr] offset:1536\n" \
    "v_mfma_f32_16x16x32_bf16 v[24:27], v[92:95], v[124:127], v[24:27]\n" \
    "v_mfma_f32_16x16x32_bf16 v[20:23], v[92:95], v[120:123], v[20:23]\n" \
    "ds_read2_b32 v[92:93], v163 offset0:48 offset1:49\n" \
    "v_mfma_f32_16x16x32_bf16 v[44:47], v[96:99], v[140:143], v[44:47]\n" \
    "ds_read_b128 v[140:143], %[hp] offset:2752\n" \
    "v_mfma_f32_16x16x32_bf16 v[40:43], v[96:99], v[136:139], v[40:43]\n" \
    "ds_read2_b32 v[94:95], v163 offset0:50 offset1:51\n" \
    "v_mfma_f32_16x16x32_bf16 v[36:39], v[96:99], v[132:135], v[36:39]\n" \
    "v_mfma_f32_16x16x32_bf16 v[32:35], v[96:99], v[128:131], v[32:35]\n" \
    "v_mfma_f32_16x16x32_bf16 v[28:31], v[96:99], v[124:127], v[28:31]\n" \
    "v_mfma_f32_16x16x32_bf16 v[24:27], v[96:99], v[120:123], v[24:27]\n" \
    "ds_read2_b32 v[96:97], v162 offset0:192 offset1:193\n" \
    "v_mfma_f32_16x16x32_bf16 v[44:47], v[100:103], v[136:139], v[44:47]\n" \
    "ds_read_b128 v[136:139], %[hp] offset:2240\n" \
    "v_mfma_f32_16x16x32_bf16 v[40:43], v[100:103], v[132:135], v[40:43]\n" \
    "ds_read2_b32 v[98:99], v162 offset0:194 offset1:195\n" \
    "v_mfma_f32_16x16x32_bf16 v[36:39], v[100:103], v[128:131], v[36:39]\n" \
    "v_mfma_f32_16x16x32_bf16 v[32:35], v[100:103], v[124:127], v[32:35]\n" \
    "s_waitcnt lgkmcnt(6)\n" \
    "v_lshlrev_b32 v84, 16, v86\n" \
    "v_mfma_f32_16x16x32_bf16 v[28:31], v[100:103], v[120:123], v[28:31]\n" \
    "ds_read2_b32 v[100:101], v162 offset0:48 offset1:49\n" \
    "v_mfma_f32_16x16x32_bf16 v[44:47], v[104:107], v[132:135], v[44:47]\n" \
    "ds_read_b128 v[132:135], %[hp] offset:1728\n" \
    "v_and_b32 v85, 0xffff0000, v86\n" \
    "v_mfma_f32_16x16x32_bf16 v[40:43], v[104:107], v[128:131], v[40:43]\n" \
    "ds_read2_b32 v[102:103], v162 offset0:50 offset1:51\n" \
    "v_mfma_f32_16x16x32_bf16 v[36:39], v[104:107], v[124:127], v[36:39]\n" \
    "v_lshlrev_b32 v86, 16, v87\n" \
    "v_mfma_f32_16x16x32_bf16 v[32:35], v[104:107], v[120:123], v[32:35]\n" \
    "ds_read2_b32 v[104:105], v161 offset0:192 offset1:193\n" \
    "v_mfma_f32_16x16x32_bf16 v[44:47], v[108:111], v[128:131], v[44:47]\n" \
    "ds_read_b128 v[128:131], %[hp] offset:1216\n" \
    "v_and_b32 v87, 0xffff0000, v87\n" \
    "v_mfma_f32_16x16x32_bf16 v[40:43], v[108:111], v[124:127], v[40:43]\n" \
    "ds_read2_b32 v[106:107], v161 offset0:194 offset1:195\n" \
    "v_mfma_f32_16x16x32_bf16 v[36:39], v[108:111], v[120:123], v[36:39]\n" \
    "ds_read2_b32 v[108:109], v161 offset0:48 offset1:49\n" \
    "v_mfma_f32_16x16x32_bf16 v[44:47], v[112:115], v[124:127], v[44:47]\n" \
    "ds_read2_b32 v[110:111], v161 offset0:50 offset1:51\n" \
    "v_mfma_f32_16x16x32_bf16 v[40:43], v[112:115], v[120:123], v[40:43]\n" \
    "ds_read2_b32 v[112:113], v160 offset0:192 offset1:193\n" \
    "v_mfma_f32_16x16x32_bf16 v[44:47], v[116:119], v[120:123], v[44:47]\n" \
    "ds_read2_b32 v[114:115], v160 offset0:194 offset1:195\n" \
    "v_mfma_f32_16x16x32_bf16 v[44:47], v[88:91], v[148:151], v[44:47]\n" \
    "ds_read2_b32 v[116:117], v160 offset0:48 offset1:49\n" \
    "v_mfma_f32_16x16x32_bf16 v[40:43], v[88:91], v[144:147], v[40:43]\n" \
    "ds_read2_b32 v[118:119], v160 offset0:50 offset1:51\n" \
    "s_waitcnt lgkmcnt(15)\n" \
    "v_mfma_f32_16x16x32_bf16 v[36:39], v[88:91], v[140:143], v[36:39]\n" \
    "ds_read_b128 v[148:151], %[hp] offset:3840\n" \
    "s_waitcnt lgkmcnt(14)\n" \
    "v_mfma_f32_16x16x32_bf16 v[32:35], v[88:91], v[136:139], v[32:35]\n" \
    "ds_read_b128 v[124:127], %[hp] offset:768\n" \
    "s_waitcnt lgkmcnt(12)\n" \
    "v_mfma_f32_16x16x32_bf16 v[28:31], v[88:91], v[132:135], v[28:31]\n" \
    "ds_read_b128 v[120:123], %[hp] offset:256\n" \
    "s_waitcnt lgkmcnt(10)\n" \
    "v_mfma_f32_16x16x32_bf16 v[24:27], v[88:91], v[128:131], v[24:27]\n" \
    "v_mfma_f32_16x16x32_bf16 v[20:23], v[88:91], v[156:159], v[20:23]\n" \
    "v_mfma_f32_16x16x32_bf16 v[16:19], v[88:91], v[152:155], v[16:19]\n" \
    "ds_read2_b32 v[88:89], v163 offset0:216 offset1:217\n" \
    "v_fmac_f32 v28, %[dd], v84\n" \
    "v_fmac_f32 v29, %[dd], v85\n" \
    "v_mfma_f32_16x16x32_bf16 v[44:47], v[92:95], v[144:147], v[44:47]\n" \
    "ds_read2_b32 v[90:91], v163 offset0:218 offset1:219\n" \
    "v_fmac_f32 v30, %[dd], v86\n" \
    "v_fmac_f32 v31, %[dd], v87\n" \
    "v_mfma_f32_16x16x32_bf16 v[40:43], v[92:95], v[140:143], v[40:43]\n" \
    "ds_read_b128 v[144:147], %[hp] offset:3328\n" \
    "v_mfma_f32_16x16x32_bf16 v[36:39], v[92:95], v[136:139], v[36:39]\n" \
    "v_mfma_f32_16x16x32_bf16 v[32:35], v[92:95], v[132:135], v[32:35]\n" \
    "v_mfma_f32_16x16x32_bf16 v[28:31], v[92:95], v[128:131], v[28:31]\n" \
    "ds_read_b64 v[86:87], %[kvr] offset:2048\n" \
    "v_mfma_f32_16x16x32_bf16 v[24:27], v[92:95], v[156:159], v[24:27]\n" \
    "v_mfma_f32_16x16x32_bf16 v[20:23], v[92:95], v[152:155], v[20:23]\n" \
    "ds_read2_b32 v[92:93], v163 offset0:72 offset1:73\n" \
    "v_mfma_f32_16x16x32_bf16 v[44:47], v[96:99], v[140:143], v[44:47]\n" \
    "ds_read_b128 v[140:143], %[hp] offset:2816\n" \
    "v_mfma_f32_16x16x32_bf16 v[40:43], v[96:99], v[136:139], v[40:43]\n" \
    "ds_read2_b32 v[94:95], v163 offset0:74 offset1:75\n" \
    "v_mfma_f32_16x16x32_bf16 v[36:39], v[96:99], v[132:135], v[36:39]\n" \
    "v_mfma_f32_16x16x32_bf16 v[32:35], v[96:99], v[128:131], v[32:35]\n" \
    "v_mfma_f32_16x16x32_bf16 v[28:31], v[96:99], v[156:159], v[28:31]\n" \
    "v_mfma_f32_16x16x32_bf16 v[24:27], v[96:99], v[152:155], v[24:27]\n" \
    "ds_read2_b32 v[96:97], v162 offset0:216 offset1:217\n" \
    "v_mfma_f32_16x16x32_bf16 v[44:47], v[100:103], v[136:139], v[44:47]\n" \
    "ds_read_b128 v[136:139], %[hp] offset:2304\n" \
    "v_mfma_f32_16x16x32_bf16 v[40:43], v[100:103], v[132:135], v[40:43]\n" \
    "ds_read2_b32 v[98:99], v162 offset0:218 offset1:219\n" \
    "v_mfma_f32_16x16x32_bf16 v[36:39], v[100:103], v[128:131], v[36:39]\n" \
    "v_mfma_f32_16x16x32_bf16 v[32:35], v[100:103], v[156:159], v[32:35]\n" \
    "s_waitcnt lgkmcnt(6)\n" \
    "v_lshlrev_b32 v84, 16, v86\n" \
    "v_mfma_f32_16x16x32_bf16 v[28:31], v[100:103], v[152:155], v[28:31]\n" \
    "ds_read2_b32 v[100:101], v162 offset0:72 offset1:73\n" \
    "v_mfma_f32_16x16x32_bf16 v[44:47], v[104:107], v[132:135], v[44:47]\n" \
    "ds_read_b128 v[132:135], %[hp] offset:1792\n" \
    "v_and_b32 v85, 0xffff0000, v86\n" \
    "v_mfma_f32_16x16x32_bf16 v[40:43], v[104:107], v[128:131], v[40:43]\n" \
    "ds_read2_b32 v[102:103], v162 offset0:74 offset1:75\n" \
    "v_mfma_f32_16x16x32_bf16 v[36:39], v[104:107], v[156:159], v[36:39]\n" \
    "v_lshlrev_b32 v86, 16, v87\n" \
    "v_mfma_f32_16x16x32_bf16 v[32:35], v[104:107], v[152:155], v[32:35]\n" \
    "ds_read2_b32 v[104:105], v161 offset0:216 offset1:217\n" \
    "v_mfma_f32_16x16x32_bf16 v[44:47], v[108:111], v[128:131], v[44:47]\n" \
    "ds_read_b128 v[128:131], %[hp] offset:1280\n" \
    "v_and_b32 v87, 0xffff0000, v87\n" \
    "v_mfma_f32_16x16x32_bf16 v[40:43], v[108:111], v[156:159], v[40:43]\n" \
    "ds_read2_b32 v[106:107], v161 offset0:218 offset1:219\n" \
    "v_mfma_f32_16x16x32_bf16 v[36:39], v[108:111], v[152:155], v[36:39]\n" \
    "ds_read2_b32 v[108:109], v161 offset0:72 offset1:73\n" \
    "v_mfma_f32_16x16x32_bf16 v[44:47], v[112:115], v[156:159], v[44:47]\n" \
    "ds_read2_b32 v[110:111], v161 offset0:74 offset1:75\n" \
    "v_mfma_f32_16x16x32_bf16 v[40:43], v[112:115], v[152:155], v[40:43]\n" \
    "ds_read2_b32 v[112:113], v160 offset0:216 offset1:217\n" \
    "v_mfma_f32_16x16x32_bf16 v[44:47], v[116:119], v[152:155], v[44:47]\n" \
    "ds_read2_b32 v[114:115], v160 offset0:218 offset1:219\n" \
    "v_add_u32 v160, 288, v160\n" \
    "v_add_u32 v161, 288, v161\n" \
    "v_add_u32 v162, 288, v162\n" \
    "v_add_u32 v163, 288, v163\n" \
    "v_mfma_f32_16x16x32_bf16 v[44:47], v[88:91], v[148:151], v[44:47]\n" \
    "ds_read2_b32 v[116:117], v160 offset0:0 offset1:1\n" \
    "v_mfma_f32_16x16x32_bf16 v[40:43], v[88:91], v[144:147], v[40:43]\n" \
    "ds_read2_b32 v[118:119], v160 offset0:2 offset1:3\n" \
    "s_waitcnt lgkmcnt(15)\n" \
    "v_mfma_f32_16x16x32_bf16 v[36:39], v[88:91], v[140:143], v[36:39]\n" \
    "ds_read_b128 v[148:151], %[hp] offset:3904\n" \
    "s_waitcnt lgkmcnt(14)\n" \
    "v_mfma_f32_16x16x32_bf16 v[32:35], v[88:91], v[136:139], v[32:35]\n" \
    "ds_read_b128 v[156:159], %[hp] offset:832\n" \
    "s_waitcnt lgkmcnt(12)\n" \
    "v_mfma_f32_16x16x32_bf16 v[28:31], v[88:91], v[132:135], v[28:31]\n" \
    "ds_read_b128 v[152:155], %[hp] offset:320\n" \
    "s_waitcnt lgkmcnt(10)\n" \
    "v_mfma_f32_16x16x32_bf16 v[24:27], v[88:91], v[128:131], v[24:27]\n" \
    "v_mfma_f32_16x16x32_bf16 v[20:23], v[88:91], v[124:127], v[20:23]\n" \
    "v_fmac_f32 v32, %[dd], v84\n" \
    "v_fmac_f32 v33, %[dd], v85\n" \
    "v_mfma_f32_16x16x32_bf16 v[16:19], v[88:91], v[120:123], v[16:19]\n" \
    "ds_read2_b32 v[88:89], v163 offset0:160 offset1:161\n" \
    "v_fmac_f32 v34, %[dd], v86\n" \
    "v_fmac_f32 v35, %[dd], v87\n" \
    "v_mfma_f32_16x16x32_bf16 v[44:47], v[92:95], v[144:147], v[44:47]\n" \
    "ds_read2_b32 v[90:91], v163 offset0:162 offset1:163\n" \
    "v_mfma_f32_16x16x32_bf16 v[40:43], v[92:95], v[140:143], v[40:43]\n" \
    "ds_read_b128 v[144:147], %[hp] offset:3392\n" \
    "v_mfma_f32_16x16x32_bf16 v[36:39], v[92:95], v[136:139], v[36:39]\n" \
    "v_mfma_f32_16x16x32_bf16 v[32:35], v[92:95], v[132:135], v[32:35]\n" \
    "v_mfma_f32_16x16x32_bf16 v[28:31], v[92:95], v[128:131], v[28:31]\n" \
    "ds_read_b64 v[86:87], %[kvr] offset:2560\n" \
    "v_mfma_f32_16x16x32_bf16 v[24:27], v[92:95], v[124:127], v[24:27]\n" \
    "v_mfma_f32_16x16x32_bf16 v[20:23], v[92:95], v[120:123], v[20:23]\n" \
    "ds_read2_b32 v[92:93], v163 offset0:16 offset1:17\n" \
    "v_mfma_f32_16x16x32_bf16 v[44:47], v[96:99], v[140:143], v[44:47]\n" \
    "ds_read_b128 v[140:143], %[hp] offset:2880\n" \
    "v_mfma_f32_16x16x32_bf16 v[40:43], v[96:99], v[136:139], v[40:43]\n" \
    "ds_read2_b32 v[94:95], v163 offset0:18 offset1:19\n" \
    "v_mfma_f32_16x16x32_bf16 v[36:39], v[96:99], v[132:135], v[36:39]\n" \
    "v_mfma_f32_16x16x32_bf16 v[32:35], v[96:99], v[128:131], v[32:35]\n" \
    "v_mfma_f32_16x16x32_bf16 v[28:31], v[96:99], v[124:127], v[28:31]\n" \
    "v_mfma_f32_16x16x32_bf16 v[24:27], v[96:99], v[120:123], v[24:27]\n" \
    "ds_read2_b32 v[96:97], v162 offset0:160 offset1:161\n" \
    "v_mfma_f32_16x16x32_bf16 v[44:47], v[100:103], v[136:139], v[44:47]\n" \
    "ds_read_b128 v[136:139], %[hp] offset:2368\n" \
    "v_mfma_f32_16x16x32_bf16 v[40:43], v[100:103], v[132:135], v[40:43]\n" \
    "ds_read2_b32 v[98:99], v162 offset0:162 offset1:163\n" \
    "v_mfma_f32_16x16x32_bf16 v[36:39], v[100:103], v[128:131], v[36:39]\n" \
    "v_mfma_f32_16x16x32_bf16 v[32:35], v[100:103], v[124:127], v[32:35]\n" \
    "s_waitcnt lgkmcnt(6)\n" \
    "v_lshlrev_b32 v84, 16, v86\n" \
    "v_mfma_f32_16x16x32_bf16 v[28:31], v[100:103], v[120:123], v[28:31]\n" \
    "ds_read2_b32 v[100:101], v162 offset0:16 offset1:17\n" \
    "v_mfma_f32_16x16x32_bf16 v[44:47], v[104:107], v[132:135], v[44:47]\n" \
    "ds_read_b128 v[132:135], %[hp] offset:1856\n" \
    "v_and_b32 v85, 0xffff0000, v86\n" \
    "v_mfma_f32_16x16x32_bf16 v[40:43], v[104:107], v[128:131], v[40:43]\n" \
    "ds_read2_b32 v[102:103], v162 offset0:18 offset1:19\n" \
    "v_mfma_f32_16x16x32_bf16 v[36:39], v[104:107], v[124:127], v[36:39]\n" \
    "v_lshlrev_b32 v86, 16, v87\n" \
    "v_mfma_f32_16x16x32_bf16 v[32:35], v[104:107], v[120:123], v[32:35]\n" \
    "ds_read2_b32 v[104:105], v161 offset0:160 offset1:161\n" \
    "v_mfma_f32_16x16x32_bf16 v[44:47], v[108:111], v[128:131], v[44:47]\n" \
    "ds_read_b128 v[128:131], %[hp] offset:1344\n" \
    "v_and_b32 v87, 0xffff0000, v87\n" \
    "v_mfma_f32_16x16x32_bf16 v[40:43], v[108:111], v[124:127], v[40:43]\n" \
    "ds_read2_b32 v[106:107], v161 offset0:162 offset1:163\n" \
    "v_mfma_f32_16x16x32_bf16 v[36:39], v[108:111], v[120:123], v[36:39]\n" \
    "ds_read2_b32 v[108:109], v161 offset0:16 offset1:17\n" \
    "v_mfma_f32_16x16x32_bf16 v[44:47], v[112:115], v[124:127], v[44:47]\n" \
    "ds_read2_b32 v[110:111], v161 offset0:18 offset1:19\n" \
    "v_mfma_f32_16x16x32_bf16 v[40:43], v[112:115], v[120:123], v[40:43]\n" \
    "ds_read2_b32 v[112:113], v160 offset0:160 offset1:161\n" \
    "v_mfma_f32_16x16x32_bf16 v[44:47], v[116:119], v[120:123], v[44:47]\n" \
    "ds_read2_b32 v[114:115], v160 offset0:162 offset1:163\n" \
    "v_mfma_f32_16x16x32_bf16 v[44:47], v[88:91], v[148:151], v[44:47]\n" \
    "ds_read2_b32 v[116:117], v160 offset0:16 offset1:17\n" \
    "v_mfma_f32_16x16x32_bf16 v[40:43], v[88:91], v[144:147], v[40:43]\n" \
    "ds_read2_b32 v[118:119], v160 offset0:18 offset1:19\n" \
    "s_waitcnt lgkmcnt(15)\n" \
    "v_mfma_f32_16x16x32_bf16 v[36:39], v[88:91], v[140:143], v[36:39]\n" \
    "ds_read_b128 v[148:151], %[hp] offset:3968\n" \
    "s_waitcnt lgkmcnt(14)\n" \
    "v_mfma_f32_16x16x32_bf16 v[32:35], v[88:91], v[136:139], v[32:35]\n" \
    "ds_read_b128 v[124:127], %[hp] offset:896\n" \
    "s_waitcnt lgkmcnt(12)\n" \
    "v_mfma_f32_16x16x32_bf16 v[28:31], v[88:91], v[132:135], v[28:31]\n" \
    "ds_read_b128 v[120:123], %[hp] offset:384\n" \
    "s_waitcnt lgkmcnt(10)\n" \
    "v_mfma_f32_16x16x32_bf16 v[24:27], v[88:91], v[128:131], v[24:27]\n" \
    "v_fmac_f32 v36, %[dd], v84\n" \
    "v_fmac_f32 v37, %[dd], v85\n" \
    "v_mfma_f32_16x16x32_bf16 v[20:23], v[88:91], v[156:159], v[20:23]\n" \
    "v_fmac_f32 v38, %[dd], v86\n" \
    "v_fmac_f32 v39, %[dd], v87\n" \
    "v_mfma_f32_16x16x32_bf16 v[16:19], v[88:91], v[152:155], v[16:19]\n" \
    "ds_read2_b32 v[88:89], v163 offset0:176 offset1:177\n" \
    "v_mfma_f32_16x16x32_bf16 v[44:47], v[92:95], v[144:147], v[44:47]\n" \
    "ds_read2_b32 v[90:91], v163 offset0:178 offset1:179\n" \
    "v_mfma_f32_16x16x32_bf16 v[40:43], v[92:95], v[140:143], v[40:43]\n" \
    "ds_read_b128 v[144:147], %[hp] offset:3456\n" \
    "v_mfma_f32_16x16x32_bf16 v[36:39], v[92:95], v[136:139], v[36:39]\n" \
    "v_mfma_f32_16x16x32_bf16 v[32:35], v[92:95], v[132:135], v[32:35]\n" \
    "v_mfma_f32_16x16x32_bf16 v[28:31], v[92:95], v[128:131], v[28:31]\n" \
    "ds_read_b64 v[86:87], %[kvr] offset:3072\n" \
    "v_mfma_f32_16x16x32_bf16 v[24:27], v[92:95], v[156:159], v[24:27]\n" \
    "v_mfma_f32_16x16x32_bf16 v[20:23], v[92:95], v[152:155], v[20:23]\n" \
    "ds_read2_b32 v[92:93], v163 offset0:32 offset1:33\n" \
    "v_mfma_f32_16x16x32_bf16 v[44:47], v[96:99], v[140:143], v[44:47]\n" \
    "ds_read_b128 v[140:143], %[hp] offset:2944\n" \
    "v_mfma_f32_16x16x32_bf16 v[40:43], v[96:99], v[136:139], v[40:43]\n" \
    "ds_read2_b32 v[94:95], v163 offset0:34 offset1:35\n" \
    "v_mfma_f32_16x16x32_bf16 v[36:39], v[96:99], v[132:135], v[36:39]\n" \
    "v_mfma_f32_16x16x32_bf16 v[32:35], v[96:99], v[128:131], v[32:35]\n" \
    "v_mfma_f32_16x16x32_bf16 v[28:31], v[96:99], v[156:159], v[28:31]\n" \
    "v_mfma_f32_16x16x32_bf16 v[24:27], v[96:99], v[152:155], v[24:27]\n" \
    "ds_read2_b32 v[96:97], v162 offset0:176 offset1:177\n" \
    "v_mfma_f32_16x16x32_bf16 v[44:47], v[100:103], v[136:139], v[44:47]\n" \
    "ds_read_b128 v[136:139], %[hp] offset:2432\n" \
    "v_mfma_f32_16x16x32_bf16 v[40:43], v[100:103], v[132:135], v[40:43]\n" \
    "ds_read2_b32 v[98:99], v162 offset0:178 offset1:179\n" \
    "v_mfma_f32_16x16x32_bf16 v[36:39], v[100:103], v[128:131], v[36:39]\n" \
    "v_mfma_f32_16x16x32_bf16 v[32:35], v[100:103], v[156:159], v[32:35]\n" \
    "s_waitcnt lgkmcnt(6)\n" \
    "v_lshlrev_b32 v84, 16, v86\n" \
    "v_mfma_f32_16x16x32_bf16 v[28:31], v[100:103], v[152:155], v[28:31]\n" \
    "ds_read2_b32 v[100:101], v162 offset0:32 offset1:33\n" \
    "v_mfma_f32_16x16x32_bf16 v[44:47], v[104:107], v[132:135], v[44:47]\n" \
    "ds_read_b128 v[132:135], %[hp] offset:1920\n" \
    "v_and_b32 v85, 0xffff0000, v86\n" \
    "v_mfma_f32_16x16x32_bf16 v[40:43], v[104:107], v[128:131], v[40:43]\n" \
    "ds_read2_b32 v[102:103], v162 offset0:34 offset1:35\n" \
    "v_mfma_f32_16x16x32_bf16 v[36:39], v[104:107], v[156:159], v[36:39]\n" \
    "v_lshlrev_b32 v86, 16, v87\n" \
    "v_mfma_f32_16x16x32_bf16 v[32:35], v[104:107], v[152:155], v[32:35]\n" \
    "ds_read2_b32 v[104:105], v161 offset0:176 offset1:177\n" \
    "v_mfma_f32_16x16x32_bf16 v[44:47], v[108:111], v[128:131], v[44:47]\n" \
    "ds_read_b128 v[128:131], %[hp] offset:1408\n" \
    "v_and_b32 v87, 0xffff0000, v87\n" \
    "v_mfma_f32_16x16x32_bf16 v[40:43], v[108:111], v[156:159], v[40:43]\n" \
    "ds_read2_b32 v[106:107], v161 offset0:178 offset1:179\n" \
    "v_mfma_f32_16x16x32_bf16 v[36:39], v[108:111], v[152:155], v[36:39]\n" \
    "ds_read2_b32 v[108:109], v161 offset0:32 offset1:33\n" \
    "v_mfma_f32_16x16x32_bf16 v[44:47], v[112:115], v[156:159], v[44:47]\n" \
    "ds_read2_b32 v[110:111], v161 offset0:34 offset1:35\n" \
    "v_mfma_f32_16x16x32_bf16 v[40:43], v[112:115], v[152:155], v[40:43]\n" \
    "ds_read2_b32 v[112:113], v160 offset0:176 offset1:177\n" \
    "v_mfma_f32_16x16x32_bf16 v[44:47], v[116:119], v[152:155], v[44:47]\n" \
    "ds_read2_b32 v[114:115], v160 offset0:178 offset1:179\n" \
    "v_mfma_f32_16x16x32_bf16 v[44:47], v[88:91], v[148:151], v[44:47]\n" \
    "ds_read2_b32 v[116:117], v160 offset0:32 offset1:33\n" \
    "v_mfma_f32_16x16x32_bf16 v[40:43], v[88:91], v[144:147], v[40:43]\n" \
    "ds_read2_b32 v[118:119], v160 offset0:34 offset1:35\n" \
    "s_waitcnt lgkmcnt(15)\n" \
    "v_mfma_f32_16x16x32_bf16 v[36:39], v[88:91], v[140:143], v[36:39]\n" \
    "ds_read_b128 v[152:155], %[hp] offset:448\n" \
    "s_waitcnt lgkmcnt(14)\n" \
    "v_mfma_f32_16x16x32_bf16 v[32:35], v[88:91], v[136:139], v[32:35]\n" \
    "ds_read_b128 v[156:159], %[hp] offset:960\n" \
    "s_waitcnt lgkmcnt(12)\n" \
    "v_mfma_f32_16x16x32_bf16 v[28:31], v[88:91], v[132:135], v[28:31]\n" \
    "ds_read_b128 v[148:151], %[hp] offset:4032\n" \
    "v_fmac_f32 v40, %[dd], v84\n" \
    "v_fmac_f32 v41, %[dd], v85\n" \
    "s_waitcnt lgkmcnt(10)\n" \
    "v_mfma_f32_16x16x32_bf16 v[24:27], v[88:91], v[128:131], v[24:27]\n" \
    "v_fmac_f32 v42, %[dd], v86\n" \
    "v_fmac_f32 v43, %[dd], v87\n" \
    "v_mfma_f32_16x16x32_bf16 v[20:23], v[88:91], v[124:127], v[20:23]\n" \
    "v_mfma_f32_16x16x32_bf16 v[16:19], v[88:91], v[120:123], v[16:19]\n" \
    "ds_read2_b32 v[88:89], v163 offset0:192 offset1:193\n" \
    "v_mfma_f32_16x16x32_bf16 v[44:47], v[92:95], v[144:147], v[44:47]\n" \
    "ds_read2_b32 v[90:91], v163 offset0:194 offset1:195\n" \
    "v_mfma_f32_16x16x32_bf16 v[40:43], v[92:95], v[140:143], v[40:43]\n" \
    "ds_read_b128 v[144:147], %[hp] offset:3520\n" \
    "v_mfma_f32_16x16x32_bf16 v[36:39], v[92:95], v[136:139], v[36:39]\n" \
    "v_mfma_f32_16x16x32_bf16 v[32:35], v[92:95], v[132:135], v[32:35]\n" \
    "v_mfma_f32_16x16x32_bf16 v[28:31], v[92:95], v[128:131], v[28:31]\n" \
    "ds_read_b64 v[86:87], %[kvr] offset:3584\n" \
    "v_mfma_f32_16x16x32_bf16 v[24:27], v[92:95], v[124:127], v[24:27]\n" \
    "v_mfma_f32_16x16x32_bf16 v[20:23], v[92:95], v[120:123], v[20:23]\n" \
    "ds_read2_b32 v[92:93], v163 offset0:48 offset1:49\n" \
    "v_mfma_f32_16x16x32_bf16 v[44:47], v[96:99], v[140:143], v[44:47]\n" \
    "ds_read2_b32 v[94:95], v163 offset0:50 offset1:51\n" \
    "v_mfma_f32_16x16x32_bf16 v[40:43], v[96:99], v[136:139], v[40:43]\n" \
    "ds_read_b128 v[140:143], %[hp] offset:3008\n" \
    "v_mfma_f32_16x16x32_bf16 v[36:39], v[96:99], v[132:135], v[36:39]\n" \
    "v_mfma_f32_16x16x32_bf16 v[32:35], v[96:99], v[128:131], v[32:35]\n" \
    "v_mfma_f32_16x16x32_bf16 v[28:31], v[96:99], v[124:127], v[28:31]\n" \
    "v_mfma_f32_16x16x32_bf16 v[24:27], v[96:99], v[120:123], v[24:27]\n" \
    "ds_read2_b32 v[96:97], v162 offset0:192 offset1:193\n" \
    "v_mfma_f32_16x16x32_bf16 v[44:47], v[100:103], v[136:139], v[44:47]\n" \
    "ds_read2_b32 v[98:99], v162 offset0:194 offset1:195\n" \
    "v_mfma_f32_16x16x32_bf16 v[40:43], v[100:103], v[132:135], v[40:43]\n" \
    "ds_read_b128 v[136:139], %[hp] offset:2496\n" \
    "v_mfma_f32_16x16x32_bf16 v[36:39], v[100:103], v[128:131], v[36:39]\n" \
    "v_mfma_f32_16x16x32_bf16 v[32:35], v[100:103], v[124:127], v[32:35]\n" \
    "s_waitcnt lgkmcnt(6)\n" \
    "v_lshlrev_b32 v84, 16, v86\n" \
    "v_mfma_f32_16x16x32_bf16 v[28:31], v[100:103], v[120:123], v[28:31]\n" \
    "ds_read2_b32 v[100:101], v162 offset0:48 offset1:49\n" \
    "v_mfma_f32_16x16x32_bf16 v[44:47], v[104:107], v[132:135], v[44:47]\n" \
    "ds_read_b128 v[132:135], %[hp] offset:1984\n" \
    "v_and_b32 v85, 0xffff0000, v86\n" \
    "v_mfma_f32_16x16x32_bf16 v[40:43], v[104:107], v[128:131], v[40:43]\n" \
    "ds_read2_b32 v[102:103], v162 offset0:50 offset1:51\n" \
    "v_mfma_f32_16x16x32_bf16 v[36:39], v[104:107], v[124:127], v[36:39]\n" \
    "v_lshlrev_b32 v86, 16, v87\n" \
    "v_mfma_f32_16x16x32_bf16 v[32:35], v[104:107], v[120:123], v[32:35]\n" \
    "ds_read2_b32 v[104:105], v161 offset0:192 offset1:193\n" \
    "v_mfma_f32_16x16x32_bf16 v[44:47], v[108:111], v[128:131], v[44:47]\n" \
    "ds_read_b128 v[128:131], %[hp] offset:1472\n" \
    "v_and_b32 v87, 0xffff0000, v87\n" \
    "v_mfma_f32_16x16x32_bf16 v[40:43], v[108:111], v[124:127], v[40:43]\n" \
    "ds_read2_b32 v[106:107], v161 offset0:194 offset1:195\n" \
    "v_mfma_f32_16x16x32_bf16 v[36:39], v[108:111], v[120:123], v[36:39]\n" \
    "ds_read2_b32 v[108:109], v161 offset0:48 offset1:49\n" \
    "v_mfma_f32_16x16x32_bf16 v[44:47], v[112:115], v[124:127], v[44:47]\n" \
    "ds_read2_b32 v[110:111], v161 offset0:50 offset1:51\n" \
    "v_mfma_f32_16x16x32_bf16 v[40:43], v[112:115], v[120:123], v[40:43]\n" \
    "ds_read2_b32 v[112:113], v160 offset0:192 offset1:193\n" \
    "v_mfma_f32_16x16x32_bf16 v[44:47], v[116:119], v[120:123], v[44:47]\n" \
    "ds_read2_b32 v[114:115], v160 offset0:194 offset1:195\n" \
    "v_mfma_f32_16x16x32_bf16 v[16:19], v[88:91], v[152:155], v[16:19]\n" \
    "ds_read2_b32 v[116:117], v160 offset0:48 offset1:49\n" \
    "v_mfma_f32_16x16x32_bf16 v[20:23], v[88:91], v[156:159], v[20:23]\n" \
    "ds_read2_b32 v[118:119], v160 offset0:50 offset1:51\n" \
    "s_waitcnt lgkmcnt(15)\n" \
    "v_mfma_f32_16x16x32_bf16 v[20:23], v[92:95], v[152:155], v[20:23]\n" \
    "ds_read_b32 v50, %[ctl] offset:52\n" \
    "s_waitcnt lgkmcnt(8)\n" \
    "v_mfma_f32_16x16x32_bf16 v[24:27], v[88:91], v[128:131], v[24:27]\n" \
    "v_fmac_f32 v44, %[dd], v84\n" \
    "v_fmac_f32 v45, %[dd], v85\n" \
    "v_mfma_f32_16x16x32_bf16 v[24:27], v[92:95], v[156:159], v[24:27]\n" \
    "v_fmac_f32 v46, %[dd], v86\n" \
    "v_fmac_f32 v47, %[dd], v87\n" \
    "v_mfma_f32_16x16x32_bf16 v[24:27], v[96:99], v[152:155], v[24:27]\n" \
    "v_mfma_f32_16x16x32_bf16 v[28:31], v[88:91], v[132:135], v[28:31]\n" \
    "v_cvt_pk_bf16_f32 v52, v16, v17\n" \
    "v_cvt_pk_bf16_f32 v54, v20, v21\n" \
    "v_mfma_f32_16x16x32_bf16 v[28:31], v[92:95], v[128:131], v[28:31]\n" \
    "v_cvt_pk_bf16_f32 v53, v18, v19\n" \
    "v_cvt_pk_bf16_f32 v55, v22, v23\n" \
    "v_mfma_f32_16x16x32_bf16 v[28:31], v[96:99], v[156:159], v[28:31]\n" \
    "v_min_u32 v52, -2, v52\n" \
    "v_min_u32 v54, -2, v54\n" \
    "v_mfma_f32_16x16x32_bf16 v[28:31], v[100:103], v[152:155], v[28:31]\n" \
    "v_min_u32 v53, -2, v53\n" \
    "v_min_u32 v55, -2, v55\n" \
    "v_mfma_f32_16x16x32_bf16 v[32:35], v[88:91], v[136:139], v[32:35]\n" \
    "v_permlane16_swap_b32 v52, v54\n" \
    "v_add_u32 v68, 0, %[yo]\n" \
    "v_mfma_f32_16x16x32_bf16 v[32:35], v[92:95], v[132:135], v[32:35]\n" \
    "v_permlane16_swap_b32 v53, v55\n" \
    "v_mfma_f32_16x16x32_bf16 v[32:35], v[96:99], v[128:131], v[32:35]\n" \
    "s_waitcnt lgkmcnt(0)\n" \
    "v_readfirstlane_b32 %[tmp], v50\n" \
    "s_mov_b32 %[cnt], 0\n" \
    "s_cmp_lg_u32 %[tmp], 0\n" \
    "s_cbranch_scc1 Lok_%=\n" \
    "Ls2_%=:\n" \
    "ds_read_b32 v50, %[ctl] offset:52\n" \
    "s_add_u32 %[cnt], %[cnt], 1\n" \
    "s_waitcnt lgkmcnt(0)\n" \
    "v_readfirstlane_b32 %[tmp], v50\n" \
    "s_cmp_ge_u32 %[cnt], %[cap]\n" \
    "s_cbranch_scc1 Lok_%=\n" \
    "s_cmp_eq_u32 %[tmp], 0\n" \
    "s_cbranch_scc1 Ls2_%=\n" \
    "Lok_%=:\n" \
    "global_store_dwordx4 v68, v[52:55], %[wsp] sc1\n" \
    "v_mfma_f32_16x16x32_bf16 v[32:35], v[100:103], v[156:159], v[32:35]\n" \
    "v_cvt_pk_bf16_f32 v56, v24, v25\n" \
    "v_cvt_pk_bf16_f32 v58, v28, v29\n" \
    "v_mfma_f32_16x16x32_bf16 v[32:35], v[104:107], v[152:155], v[32:35]\n" \
    "v_cvt_pk_bf16_f32 v57, v26, v27\n" \
    "v_cvt_pk_bf16_f32 v59, v30, v31\n" \
    "v_mfma_f32_16x16x32_bf16 v[36:39], v[88:91], v[140:143], v[36:39]\n" \
    "v_min_u32 v56, -2, v56\n" \
    "v_min_u32 v58, -2, v58\n" \
    "v_mfma_f32_16x16x32_bf16 v[36:39], v[92:95], v[136:139], v[36:39]\n" \
    "v_min_u32 v57, -2, v57\n" \
    "v_min_u32 v59, -2, v59\n" \
    "v_mfma_f32_16x16x32_bf16 v[36:39], v[96:99], v[132:135], v[36:39]\n" \
    "v_permlane16_swap_b32 v56, v58\n" \
    "v_add_u32 v69, 655360, %[yo]\n" \
    "v_mfma_f32_16x16x32_bf16 v[36:39], v[100:103], v[128:131], v[36:39]\n" \
    "v_permlane16_swap_b32 v57, v59\n" \
    "v_mfma_f32_16x16x32_bf16 v[36:39], v[104:107], v[156:159], v[36:39]\n" \
    "global_store_dwordx4 v69, v[56:59], %[wsp] sc1\n" \
    "v_mfma_f32_16x16x32_bf16 v[36:39], v[108:111], v[152:155], v[36:39]\n" \
    "v_mfma_f32_16x16x32_bf16 v[40:43], v[88:91], v[144:147], v[40:43]\n" \
    "v_mfma_f32_16x16x32_bf16 v[40:43], v[92:95], v[140:143], v[40:43]\n" \
    "v_mfma_f32_16x16x32_bf16 v[40:43], v[96:99], v[136:139], v[40:43]\n" \
    "v_mfma_f32_16x16x32_bf16 v[40:43], v[100:103], v[132:135], v[40:43]\n" \
    "v_cvt_pk_bf16_f32 v60, v32, v33\n" \
    "v_cvt_pk_bf16_f32 v62, v36, v37\n" \
    "v_mfma_f32_16x16x32_bf16 v[40:43], v[104:107], v[128:131], v[40:43]\n" \
    "v_cvt_pk_bf16_f32 v61, v34, v35\n" \
    "v_cvt_pk_bf16_f32 v63, v38, v39\n" \
    "v_mfma_f32_16x16x32_bf16 v[40:43], v[108:111], v[156:159], v[40:43]\n" \
    "v_min_u32 v60, -2, v60\n" \
    "v_min_u32 v62, -2, v62\n" \
    "v_mfma_f32_16x16x32_bf16 v[40:43], v[112:115], v[152:155], v[40:43]\n" \
    "v_min_u32 v61, -2, v61\n" \
    "v_min_u32 v63, -2, v63\n" \
    "v_mfma_f32_16x16x32_bf16 v[44:47], v[88:91], v[148:151], v[44:47]\n" \
    "v_permlane16_swap_b32 v60, v62\n" \
    "v_add_u32 v70, 1310720, %[yo]\n" \
    "v_mfma_f32_16x16x32_bf16 v[44:47], v[92:95], v[144:147], v[44:47]\n" \
    "v_permlane16_swap_b32 v61, v63\n" \
    "v_mfma_f32_16x16x32_bf16 v[44:47], v[96:99], v[140:143], v[44:47]\n" \
    "global_store_dwordx4 v70, v[60:63], %[wsp] sc1\n" \
    "v_mfma_f32_16x16x32_bf16 v[44:47], v[100:103], v[136:139], v[44:47]\n" \
    "v_mfma_f32_16x16x32_bf16 v[44:47], v[104:107], v[132:135], v[44:47]\n" \
    "v_mfma_f32_16x16x32_bf16 v[44:47], v[108:111], v[128:131], v[44:47]\n" \
    "v_mfma_f32_16x16x32_bf16 v[44:47], v[112:115], v[156:159], v[44:47]\n" \
    "v_mfma_f32_16x16x32_bf16 v[44:47], v[116:119], v[152:155], v[44:47]\n" \
    "s_nop 7\n" \
    "v_cvt_pk_bf16_f32 v64, v40, v41\n" \
    "v_cvt_pk_bf16_f32 v66, v44, v45\n" \
    "v_cvt_pk_bf16_f32 v65, v42, v43\n" \
    "v_cvt_pk_bf16_f32 v67, v46, v47\n" \
    "v_min_u32 v64, -2, v64\n" \
    "v_min_u32 v66, -2, v66\n" \
    "v_min_u32 v65, -2, v65\n" \
    "v_min_u32 v67, -2, v67\n" \
    "v_permlane16_swap_b32 v64, v66\n" \
    "v_add_u32 v71, 1966080, %[yo]\n" \
    "v_permlane16_swap_b32 v65, v67\n" \
    "global_store_dwordx4 v71, v[64:67], %[wsp] sc1\n" \
    "s_nop 1\n"
#define CONV_CLOB "v16", "v17", "v18", "v19", "v20", "v21", "v22", "v23", "v24", "v25", "v26", "v27", "v28", "v29", "v30", "v31", "v32", "v33", "v34", "v35", "v36", "v37", "v38", "v39", "v40", "v41", "v42", "v43", "v44", "v45", "v46", "v47", "v50", "v51", "v52", "v53", "v54", "v55", "v56", "v57", "v58", "v59", "v60", "v61", "v62", "v63", "v64", "v65", "v66", "v67", "v68", "v69", "v70", "v71", "v72", "v73", "v74", "v75", "v76", "v77", "v78", "v79", "v80", "v81", "v82", "v83", "v84", "v85", "v86", "v87", "v88", "v89", "v90", "v91", "v92", "v93", "v94", "v95", "v96", "v97", "v98", "v99", "v100", "v101", "v102", "v103", "v104", "v105", "v106", "v107", "v108", "v109", "v110", "v111", "v112", "v113", "v114", "v115", "v116", "v117", "v118", "v119", "v120", "v121", "v122", "v123", "v124", "v125", "v126", "v127", "v128", "v129", "v130", "v131", "v132", "v133", "v134", "v135", "v136", "v137", "v138", "v139", "v140", "v141", "v142", "v143", "v144", "v145", "v146", "v147", "v148", "v149", "v150", "v151", "v152", "v153", "v154", "v155", "v156", "v157", "v158", "v159", "v160", "v161", "v162", "v163", "scc", "memory"

static_assert(C_HR - L_CTRL == 144 && C_RED - L_CTRL == 84 && C_XD - L_CTRL == 80 && C_SENT - L_CTRL == 52, "offsets in the conv stream");
typedef __attribute__((ext_vector_type(16))) unsigned u32x16;
typedef __attribute__((address_space(3))) const unsigned lds_cu32;
typedef __attribute__((address_space(3))) u32x2 lds_u32x2;
typedef __attribute__((address_space(3))) u32x4 lds_u32x4;
typedef __attribute__((address_space(3))) f32x4 lds_f32x4;
#define GPTR(T, lo, hi) ((T*)(__attribute__((address_space(1))) T*)(((u64)(hi) << 32) | (u64)(lo)))
#if defined(__HIP_DEVICE_COMPILE__)
#define NOPK __attribute__((target("no-packed-fp32-ops")))
#else
#define NOPK
#endif
__global__ NOPK __launch_bounds__(768, 3) void fused_kernel(const u16* x_, const u16* wqkv_, const u16* kin_, const u16* vin_, const u16* shift_k_,
                                                       const u16* ssm_k_, const u16* ssm_D_, const u16* wo_,
                                                       const u16* gw_, char* ws_, u16* out_) {
    u32x16 ka0;
    u32x4 ka1;
    u32x2 ka2;
    asm volatile("s_load_dwordx16 %0, %3, 0\n\ts_load_dwordx4 %1, %3, 64\n\ts_load_dwordx2 %2, %3, 80"
                 : "=&s"(ka0), "=&s"(ka1), "=&s"(ka2) : "s"(__builtin_amdgcn_kernarg_segment_ptr()));
#define x GPTR(const u16, ka0[0], ka0[1])
#define wqkv GPTR(const u16, ka0[2], ka0[3])
#define kin GPTR(const u16, ka0[4], ka0[5])
#define vin GPTR(const u16, ka0[6], ka0[7])
#define shift_k GPTR(const u16, ka0[8], ka0[9])
#define ssm_k GPTR(const u16, ka0[10], ka0[11])
#define ssm_D GPTR(const u16, ka0[12], ka0[13])
#define wo GPTR(const u16, ka0[14], ka0[15])
#define gw GPTR(const u16, ka1[0], ka1[1])
#define ws GPTR(char, ka1[2], ka1[3])
#define out GPTR(u16, ka2[0], ka2[1])
#define wsr __builtin_amdgcn_make_buffer_rsrc(ws, 0, 0x4000000, 0x00020000)
    __shared__ __attribute__((aligned(16))) char smem[LDS_TOTAL];
    const int bid = __builtin_amdgcn_workgroup_id_x();
    const int tid = __builtin_amdgcn_workitem_id_x();
    const int lane = tid & 63;
    const int wave = __builtin_amdgcn_readfirstlane(tid >> 6);
    const int xcd = bid & 7, r5 = bid >> 3, r = r5 & 15;
    const bool isG = r5 >= 16;
    const int hg = (xcd & 1) * 4 + (r & 3);
    const int rb2 = (xcd >> 1) * 4 + (r >> 2);
    const int cq = (xcd & 1) * 2 + (r & 1), rbv = (xcd >> 1) * 8 + (r >> 1);
    const int vid = (cq ^ CQ_X) * 32 + rbv;
    const int sid = rbv * 4 + cq;
    const int rbo = xcd * 4 + (r >> 3) * 2 + ((r5 >> 4) ^ GH ^ 1), oc = r5 & 7;
    const int w = wave & 3;
    const int g = lane >> 4, n16 = lane & 15, q4 = n16 >> 2, p4 = lane & 3;
    const unsigned lb = (unsigned)(size_t)(lds_char*)smem;
    u64 tag;
    {
        u64 id = llvm_dispatch_id();
        u64 dp = (u64)(size_t)__builtin_amdgcn_dispatch_ptr();
        tag = ((u64)((unsigned)id ^ 0x5bd1e995u) << 32) | (u64)((unsigned)(dp >> 6) ^ (unsigned)(id >> 32) ^ 0x9e3779b9u);
    }
    const unsigned tlo = (unsigned)tag, thi = (unsigned)(tag >> 32);
    u64 code0;
    asm volatile("s_getpc_b64 %0" : "=s"(code0));
    code0 &= ~255ull;
    if (wave == 0) {
        const unsigned cl = lane * 32 + r5;
        if (cl < CODE_LINES) GLT(cl * 128, code0);
    }
    const u32x4 ones = {0xffffffffu, 0xffffffffu, 0xffffffffu, 0xffffffffu};

#define WO_OFF(c) ({ const int rho_ = (c) / 10, ch_ = (c) - rho_ * 10; (unsigned)((rho_ ^ ((rho_ & 8) >> 1)) * (D_DIM * 2) + oc * 160 + ch_ * 16); })
    const int kr = KROT;
#define T_C(i) __builtin_amdgcn_readfirstlane((kr + (i)) >= 10 ? kr + (i) - 10 : kr + (i))
#define T_WV() ({ const int j_ = min(n16, 14); (unsigned)((r * 4 + g) * (NQKV * 2) + j_ * 128 + ((j_ >= 5) + (j_ >= 10) + (xcd & 1)) * 640); })
#define T_XV() ((unsigned)(((xcd >> 1) * 512 + r * 32 + (lane & 31)) * (D_DIM * 2)))
#define T_W(wv, i) GLT(wv, (const char*)wqkv + (size_t)T_C(i) * (64 * NQKV * 2))
#define T_X(xv, i) { const int c0_ = T_C(i), c1_ = T_C((i) + 1); GLT((xv) + (lane < 32 ? c0_ : c1_) * 128, x); }
#define T_PAIR(i) { T_W(wv, i); T_X(xv_, i); T_W(wv, (i) + 1); }

    unsigned e0;
    if (isG) {
        const unsigned t0_ = T_WV() + (kr + (w >> 1)) * (64 * NQKV * 2);
        const unsigned t1_ = ((xcd >> 1) * 512 + (w == 1 ? r * 32 + (lane & 31) : r - ((xcd >> 1) ? 16 : 0))) * (D_DIM * 2) + (w == 1 ? kr + (lane >> 5) : min(lane, 9)) * 128;
        e0 = (w & 1) ? t1_ : t0_;
    } else {
        const unsigned c = (wave >= 5 ? wave - 5 : wave) * 64 + lane, row = (c * 3277) >> 16;
        e0 = rbv * (64 * D_DIM * 2) + cq * 320 + row * (D_DIM * 2 - 320) + c * 16;
    }
    asm volatile("" : "+v"(e0));
    asm volatile("s_waitcnt lgkmcnt(0)" : "+s"(ka0), "+s"(ka1), "+s"(ka2) :: "memory");
    if (isG) {
        if ((wave >> 2) == 1) GLT(e0, (w & 1) ? x : wqkv);
        BARRIER();
    }
    u32x2 skw;
    if (!isG && wave < 10) {
        const int tens = wave >= 5, i5 = wave - 5 * tens;
        const unsigned lw = lb + (tens ? V_VST : V_KST) + i5 * 1024;
        const char* kb = (const char*)(tens ? vin : kin);
        __builtin_amdgcn_s_sleep(V_DELAY);
        GLS(e0, kb, lw, 0);
        GLS(e0, kb + 16 * D_DIM * 2, lw, 5152);
        GLS(e0, kb + 32 * D_DIM * 2, lw, 2 * 5152);
        GLS(e0, kb + 48 * D_DIM * 2, lw, 3 * 5152);
        asm volatile("global_load_dwordx2 %0, %1, %2" : "=v"(skw) : "v"((cq * 160 + wave * 16 + n16) * 8), "s"(shift_k) : "memory");
        if (wave == 0) { if (lane < 60) GLS(e0, kb - (rbv ? 3 * D_DIM * 2 : 0), lb, V_KST - 32 - 960); }
    }
    if (wave == 11) {
        if (lane < 16) *(u32x4*)(smem + L_CTRL + lane * 16) = (u32x4){0u, 0u, 0u, 0u};
        asm volatile("s_waitcnt lgkmcnt(0)" ::: "memory");
    }
    STAMP(1);
#define Y_OFF (WS_Y + rbo * (D_DIM * 128) + oc * 10240 + w * 1024)
#define Y_FIRST() u32x4 yv[3]; float g1; { yv[0] = __builtin_amdgcn_raw_buffer_load_b128(wsr, lane * 16, Y_OFF, 16); yv[1] = __builtin_amdgcn_raw_buffer_load_b128(wsr, lane * 16, Y_OFF + 4096, 16); \
        yv[2] = __builtin_amdgcn_raw_buffer_load_b128(wsr, lane * 16, Y_OFF + (w < 2 ? 8192 : 0), 16); \
        g1 = __uint_as_float(__builtin_amdgcn_raw_buffer_load_b32(wsr, (lane & 15) * 4, WS_G + (rbo * 64 + w * 16) * 4, 16)); }
#define RELANE() const int lane_ = opaque(lane); const int g = lane_ >> 4, n16 = lane_ & 15, q4 = n16 >> 2, p4 = lane_ & 3; \
        const unsigned qa = lb + (isG ? G_Q : V_Q) + w * 2560 + n16 * 160 + 8 * g
#define SENT4(v) __any(max(max((v)[0], (v)[1]), max((v)[2], (v)[3])) == 0xffffffffu)
#define Q_READ(base) f32x4 gq[5]; { const int l_ = opaque(lane); _Pragma("unroll") for (int ct = 0; ct < 5; ++ct) { \
            const u32x2 q2 = *(const lds_u32x2*)(size_t)(lb + (base) + w * 2560 + (l_ & 15) * 160 + 8 * (l_ >> 4) + ct * 32); \
            gq[ct] = (f32x4){lo_f(q2[0]), hi_f(q2[0]), lo_f(q2[1]), hi_f(q2[1])}; } \
        asm volatile("" : "+v"(gq[0]), "+v"(gq[1]), "+v"(gq[2]), "+v"(gq[3]), "+v"(gq[4]) :: "memory"); }
#define P_STAGE() { \
        RELANE(); \
        const unsigned l_y = lb + (isG ? G_Y : V_Y); \
        const unsigned yw = l_y + (w * 8 + (lane >> 3)) * Y_PITCH + (lane & 7) * 16; \
        const unsigned yr = l_y + (4 * g + q4) * Y_PITCH + w * 32 + p4 * 8; \
_Pragma("nounroll") \
        for (int it = 0; it < SPIN_CAP; ++it) { \
            const bool b0 = SENT4(yv[0]), b1 = SENT4(yv[1]), b2 = SENT4(yv[2]); \
            if (!(b0 | b1 | b2)) break; \
            if (it || Y_SL0) __builtin_amdgcn_s_sleep(Y_SLEEP); \
            asm volatile("" ::: "memory"); \
            if (b0) yv[0] = __builtin_amdgcn_raw_buffer_load_b128(wsr, lane * 16, Y_OFF, 16); \
            if (b1) yv[1] = __builtin_amdgcn_raw_buffer_load_b128(wsr, lane * 16, Y_OFF + 4096, 16); \
            if (b2) yv[2] = __builtin_amdgcn_raw_buffer_load_b128(wsr, lane * 16, Y_OFF + (w < 2 ? 8192 : 0), 16); \
        } \
        STAMP(8); \
        *(lds_u32x4*)(size_t)yw = yv[0]; \
        *(lds_u32x4*)(size_t)(yw + 32 * Y_PITCH) = yv[1]; \
        if (w < 2) *(lds_u32x4*)(size_t)(yw + 64 * Y_PITCH) = yv[2]; \
        lds_add(lb + C_Y, lane); \
_Pragma("unroll") \
        for (int ct = 0; ct < 5; ++ct) gq[ct] *= g1; \
        asm volatile("" : "+v"(gq[0]), "+v"(gq[1]), "+v"(gq[2]), "+v"(gq[3]), "+v"(gq[4])); \
        spin_ge(lb + C_Y, 4); \
        u32x2 y2[5]; \
_Pragma("unroll") \
        for (int ct = 0; ct < 5; ++ct) y2[ct] = __builtin_bit_cast(u32x2, TRRD((lds_char*)(size_t)(yr + ct * 16 * Y_PITCH))); \
_Pragma("unroll") \
        for (int ct = 0; ct < 5; ++ct) \
            *(lds_u32x2*)(size_t)(qa + ct * 32) = (u32x2){pack2(gq[ct][0] * lo_f(y2[ct][0]), gq[ct][1] * hi_f(y2[ct][0])), pack2(gq[ct][2] * lo_f(y2[ct][1]), gq[ct][3] * hi_f(y2[ct][1]))}; \
        lds_add(lb + C_XCH + w * 4, lane); \
        STAMP(9); \
        FLUSH(); \
        return; }

    if (__builtin_expect(isG, 1)) {
      if (__builtin_expect(wave >= 4, 0)) {
        const int half = (wave >> 2) - 1;
        if (half) __builtin_amdgcn_s_setprio(C1_PRIO); else __builtin_amdgcn_s_setprio(C0_PRIO);
        const int rb = rb2 * 2 + half;
        f32x4 acc[4][5];
        unsigned aofs[2];
#pragma unroll
        for (int sub = 0; sub < 2; ++sub) aofs[sub] = lb + L_A + half * 8192 + n16 * 128 + (((sub * 4 + g) ^ ((n16 >> 1) & 7)) * 16);
        unsigned wofs[4];
#pragma unroll
        for (int i = 0; i < 4; ++i) {
            const int part = i < 3 ? i : (p4 < 3 ? p4 : 2);
            const int Q = i < 3 ? 5 * w + (w & 1) + p4 : 5 * w + ((w & 1) ? 0 : 4);
            const int c2 = (Q >> 1) - 2 * (hg & 1);
            const int cc = ((unsigned)c2 < 8u ? part * 8 + c2 : 24 + part * 2 + (c2 & 1)) ^ (2 * ((g & 1) * 4 + q4));
            wofs[i] = lb + L_W + (8 * g + q4) * W_PITCH + cc * 16 + (Q & 1) * 8;
        }
        bf16x8 af[5], bA[4], bB[4];
        lds_char* wp[2][4];
        lds_char* ap[2];
#pragma unroll
        for (int i = 0; i < 4; ++i) { wp[0][i] = (lds_char*)(size_t)opaque(wofs[i]); wp[1][i] = (lds_char*)(size_t)opaque(wofs[i] + 3 * W_SLOT); }
        ap[0] = (lds_char*)(size_t)opaque(aofs[0]);
        ap[1] = (lds_char*)(size_t)opaque(aofs[1]);
#define WSL(J, i) (wp[((J) % NW) / 3][i] + (((J) % NW) % 3) * W_SLOT)
#define MFMA16A(c, a, b) asm volatile("v_mfma_f32_16x16x32_bf16 %0, %1, %2, %0" : "+v"(c) : "v"(a), "v"(b))
#define MFMA16Z(c, a, b) asm volatile("v_mfma_f32_16x16x32_bf16 %0, %1, %2, 0" : "=&v"(c) : "v"(a), "v"(b))
#define MFMA16(c, a, b) do { if (first_) MFMA16Z(c, a, b); else MFMA16A(c, a, b); } while (0)
#define BORD(rt) (((rt) + 1) & 3)
#define STEP(J, bc, bn, R0) { \
            const bool first_ = (J) == 0; \
            _Pragma("unroll") for (int rt = R0; rt < 5; ++rt) { \
                s16x4 b0_, b1_; \
                if (rt > 0) MFMA16(acc[0][rt], bc[0], af[rt]); SB(); \
                if ((J) < 19 && rt < 4 + R0) { b0_ = TRRD(WSL((J) + 1, BORD(rt - R0))); SB(); } \
                MFMA16(acc[1][rt], bc[1], af[rt]); SB(); \
                if ((J) < 19 && rt < 4 + R0) { b1_ = TRRD(WSL((J) + 1, BORD(rt - R0)) + 4 * W_PITCH); bn[BORD(rt - R0)] = cat8(b0_, b1_); SB(); } \
                MFMA16(acc[2][rt], af[rt], bc[2]); SB(); \
                MFMA16(acc[3][rt], bc[3], af[rt]); SB(); \
                if ((J) < 19) { af[rt] = *(lds_cbf16x8*)(ap[((J) + 1) & 1] + ((((J) + 1) >> 1) % 3) * A_SLOT + rt * 2048); SB(); } \
            } \
            if ((J) >= 16 && (J) < 19) lds_st32(lb + C_FREE + (wave - 4) * 4, (J));        \
            if ((J) <= JL) BARRIER(); \
            SB(); }
#define GEMM(R0) { \
        _Pragma("unroll") for (int i = 0; i < 4; ++i) bA[i] = cat8(TRRD(wp[0][i]), TRRD(wp[0][i] + 4 * W_PITCH)); \
        _Pragma("unroll") for (int rt = R0; rt < 5; ++rt) af[rt] = *(lds_cbf16x8*)(ap[0] + rt * 2048); \
        SB(); \
        GT(0) STEP(0, bA, bB, R0) GT(1) STEP(1, bB, bA, R0) STAMP(5); GT(2) STEP(2, bA, bB, R0) GT(3) STEP(3, bB, bA, R0) GT(4) STEP(4, bA, bB, R0) GT(5) STEP(5, bB, bA, R0) \
        STAMP(6); GT(6) STEP(6, bA, bB, R0) GT(7) STEP(7, bB, bA, R0) GT(8) STEP(8, bA, bB, R0) GT(9) STEP(9, bB, bA, R0) STAMP(14); GT(10) STEP(10, bA, bB, R0) GT(11) STEP(11, bB, bA, R0) \
        GT(12) STEP(12, bA, bB, R0) GT(13) STEP(13, bB, bA, R0) GT(14) STEP(14, bA, bB, R0) GT(15) STEP(15, bB, bA, R0) GT(16) STEP(16, bA, bB, R0) GT(17) STEP(17, bB, bA, R0) \
        TS(0); GT(18) STEP(18, bA, bB, R0) GT(19) STEP(19, bB, bA, R0) GT(20) }
#define GT(j)
        const int qi = n16, par = lane & 1;
        f32x4 cm0, cm1;
        u32x4 psel, vxa;
#pragma unroll
        for (int e = 0; e < 4; ++e) {
            const bool own = 4 * g + e <= qi, pm = (e & 1) == par;
            cm0[e] = pm && !own ? 0.f : -3.0e38f;
            cm1[e] = pm && own ? 0.f : -3.0e38f;
            const bool ow = 4 * g + par + 2 * (e & 1) <= qi;
            const unsigned by = (e & 1) ? 0x0302u : 0x0100u;
            psel[e] = (ow == (e >= 2)) ? (par ? (by << 16) | 0x0c0cu : 0x0c0c0000u | by) : 0x0c0c0c0cu;
            vxa[e] = p4 == e ? lb + G_VX + w * 1152 + (half * 4 + e) * 128 + (4 * g + q4) * 8 : lb + L_CTRL;
        }
        unsigned scw = g == 2 ? lb + G_VX + w * 1152 + half * 512 + qi * 8 : lb + G_VXD;
        const int cc = (hg * 80 + w * 20 + (w & 1) * 4) / 8 + (g >> 1), colx = hg * 80 + w * 20 + ((w & 1) ? 0 : 16);
        unsigned oo = WS_O + ((rb * 4 + (g & 1)) * 20 + (cc >> 2)) * 1024 + ((cc & 3) * 16 + qi) * 16;
        unsigned ox = (g * 20 + (colx >> 5)) * 1024 + (((colx >> 3) & 3) * 16 + qi) * 16 + (colx & 4) * 2;
        asm volatile("" : "+v"(cm0), "+v"(cm1), "+v"(psel), "+v"(vxa), "+v"(scw), "+v"(oo), "+v"(ox));
        BARRIER();
        STAMP(7);
        CSTAMP(0);
        if (half == 0) GEMM(0) else GEMM(1)
        TS(1);
        asm volatile("s_nop 7\n\ts_nop 7" ::: "memory");
        STAMP(2);
        CSTAMP(1);

        __builtin_amdgcn_s_setprio(T_PRIO);
        typedef __attribute__((address_space(3))) volatile unsigned lds_vu32;
        const unsigned sent0 = *(lds_vu32*)(size_t)(lb + C_SENT);
        const unsigned hx0 = half ? *(lds_vu32*)(size_t)(lb + C_HX + w * 4) : 1u;
#define HINT() { if (wave == 11 - 4 * GH) { if (lane == 0) __hip_atomic_store((u64*)(ws + WS_HF + (rb2 * 8 + hg) * 128), tag, __ATOMIC_RELAXED, __HIP_MEMORY_SCOPE_AGENT); } }
        if (HINT_AT < 0) HINT();
        u32x4 kop[5], qop[5];
        u32x16 vv;
        const unsigned hx = lb + G_HX + w * 1152;
#pragma unroll
        for (int rt = 4; rt >= 0; --rt) {
            if (rt == 0 && half) break;
            const unsigned d0 = pack2(acc[3][rt][0], acc[3][rt][1]), d1 = pack2(acc[3][rt][2], acc[3][rt][3]);
            asm volatile("ds_write2_b32 %0, %1, %2 offset0:%3 offset1:%4" ::"v"(scw), "v"(d0), "v"(d1), "n"(rt * 32), "n"(rt * 32 + 1) : "memory");
            const auto sw2 = __builtin_amdgcn_permlane16_swap(d0, d1, false, false);
            kop[rt][0] = pack2(acc[1][rt][0], acc[1][rt][1]);
            kop[rt][1] = pack2(acc[1][rt][2], acc[1][rt][3]);
            kop[rt][2] = sw2[1];
            kop[rt][3] = 0u;
            vv[2 * rt] = pack2(acc[2][rt][0], acc[2][rt][1]);
            vv[2 * rt + 1] = pack2(acc[2][rt][2], acc[2][rt][3]);
            if (rt == 4 && half == 0) {
                asm volatile("ds_write2_b64 %0, %1, %2 offset1:1" ::"v"(hx + lane * 16), "v"((u32x2){kop[4][0], kop[4][1]}), "v"((u32x2){vv[8], vv[9]}) : "memory");
                asm volatile("ds_write_b32 %0, %1" ::"v"(g < 2 ? hx + 1024 + lane * 4 : lb + L_TOUCH), "v"(kop[4][2]) : "memory");
                lds_st32(lb + C_HX + w * 4, 1);
            }
            if (rt > 0) {
                qop[rt][0] = pack2(acc[0][rt][0], acc[0][rt][1]);
                qop[rt][1] = pack2(acc[0][rt][2], acc[0][rt][3]);
                qop[rt][2] = g < 2 ? sw2[0] : 0u;
                qop[rt][3] = 0u;
            }
        }
        TS(2);
        if (half == 1) {
            if (!__builtin_amdgcn_readfirstlane(hx0)) spin_ge(lb + C_HX + w * 4, 1);
            const u32x4 a = lds_v128(hx + lane * 16);
            const unsigned b = lds_v32(hx + 1024 + (lane & 31) * 4);
            kop[0] = (u32x4){a[0], a[1], b, 0u};
            vv[0] = a[2];
            vv[1] = a[3];
        }
        bf16x8 vx[5];
#pragma unroll
        for (int rt = 4; rt >= 1; --rt) vx[rt] = cat8(TRRD((lds_char*)(size_t)vxa[rt - 1]), TRRD((lds_char*)(size_t)(vxa[rt - 1] + 128)));
        TS(3);
        STAMP(3);
        if (HINT_AT == 0) HINT();
        const float scale = 0.22360679774997896f * 1.4426950408889634f;
        const f32x4 z4 = {0.f, 0.f, 0.f, 0.f};
        float l0[5], l1[5], mx[5];
#pragma unroll
        for (int rt = 4; rt >= 1; --rt) {
            const f32x4 s0 = __builtin_amdgcn_mfma_f32_16x16x32_bf16(__builtin_bit_cast(bf16x8, kop[rt - 1]), __builtin_bit_cast(bf16x8, qop[rt]), cm0, 0, 0, 0);
            const f32x4 s1 = __builtin_amdgcn_mfma_f32_16x16x32_bf16(__builtin_bit_cast(bf16x8, kop[rt]), __builtin_bit_cast(bf16x8, qop[rt]), cm1, 0, 0, 0);
            if (rt == 1 && rb == 0) {
                l0[rt] = fmax_raw(s1[0], s1[1]);
                l1[rt] = fmax_raw(s1[2], s1[3]);
            } else {
                l0[rt] = fmax_raw(fmax3_raw(s0[0], s0[1], s1[0]), s1[1]);
                l1[rt] = fmax_raw(fmax3_raw(s0[2], s0[3], s1[2]), s1[3]);
            }
            mx[rt] = fmax_raw(l0[rt], l1[rt]);
        }
#define FU(v) __float_as_uint(v)
#define UF(v) __uint_as_float(v)
#define RED4(dst, src, OP) { \
            const auto ra_ = __builtin_amdgcn_permlane16_swap(FU(src[1]), FU(src[2]), false, false); \
            const auto rb_ = __builtin_amdgcn_permlane16_swap(FU(src[3]), FU(src[4]), false, false); \
            const auto rc_ = __builtin_amdgcn_permlane32_swap(FU(OP(UF(ra_[0]), UF(ra_[1]))), FU(OP(UF(rb_[0]), UF(rb_[1]))), false, false); \
            dst = OP(UF(rc_[0]), UF(rc_[1])); }
#define BC4(dst, src) { \
            const auto ra_ = __builtin_amdgcn_permlane32_swap(FU(src), FU(src), false, false); \
            const auto rb_ = __builtin_amdgcn_permlane16_swap(ra_[0], ra_[0], false, false); \
            const auto rc_ = __builtin_amdgcn_permlane16_swap(ra_[1], ra_[1], false, false); \
            dst[1] = UF(rb_[0]); dst[2] = UF(rb_[1]); dst[3] = UF(rc_[0]); dst[4] = UF(rc_[1]); }
#define FADD(a, b) ((a) + (b))
        SB(); TS(4); SB();
        if (HINT_AT == 1) HINT();
        float red;
        RED4(red, mx, fmax_raw);
        red *= scale;
        BC4(mx, red);
        f32x2 pp[5];
        float sm[5];
#pragma unroll
        for (int rt = 4; rt >= 1; --rt) {
            pp[rt] = (f32x2){__builtin_amdgcn_exp2f(__builtin_fmaf(l0[rt], scale, -mx[rt])), __builtin_amdgcn_exp2f(__builtin_fmaf(l1[rt], scale, -mx[rt]))};
            sm[rt] = pp[rt][0] + pp[rt][1];
        }
        RED4(red, sm, FADD);
        red = __builtin_amdgcn_rcpf(red);
        BC4(sm, red);
        SB(); TS(5); SB();
        if (HINT_AT == 2) HINT();
        f32x4 o1[5], o2 = z4;
#pragma unroll
        for (int rt = 4; rt >= 1; --rt) {
            const unsigned pk = pack2(pp[rt][0] * sm[rt], pp[rt][1] * sm[rt]);
            const u32x4 pop = {__builtin_amdgcn_perm(pk, pk, psel[0]), __builtin_amdgcn_perm(pk, pk, psel[1]), __builtin_amdgcn_perm(pk, pk, psel[2]), __builtin_amdgcn_perm(pk, pk, psel[3])};
            const u32x4 vop = {vv[2 * rt - 2], vv[2 * rt - 1], vv[2 * rt], vv[2 * rt + 1]};
            o1[rt] = __builtin_amdgcn_mfma_f32_16x16x32_bf16(__builtin_bit_cast(bf16x8, vop), __builtin_bit_cast(bf16x8, pop), z4, 0, 0, 0);
            o2 = __builtin_amdgcn_mfma_f32_16x16x32_bf16(vx[rt], __builtin_bit_cast(bf16x8, pop), o2, 0, 0, 0);
        }
        SB(); TS(6); SB();
        if (!__builtin_amdgcn_readfirstlane(sent0)) spin_ge(lb + C_SENT, 1);
#pragma unroll
        for (int rt = 3; rt >= 1; rt -= 2) {
            const auto r0 = __builtin_amdgcn_permlane16_swap(min(pack2(o1[rt][0], o1[rt][1]), 0xfffffffeu), min(pack2(o1[rt + 1][0], o1[rt + 1][1]), 0xfffffffeu), false, false);
            const auto r1 = __builtin_amdgcn_permlane16_swap(min(pack2(o1[rt][2], o1[rt][3]), 0xfffffffeu), min(pack2(o1[rt + 1][2], o1[rt + 1][3]), 0xfffffffeu), false, false);
            __builtin_amdgcn_raw_buffer_store_b128((u32x4){r0[0], r1[0], r0[1], r1[1]}, wsr, oo + (rt - 1) * 20480, 0, 16);
        }
        __builtin_amdgcn_raw_buffer_store_b64((u32x2){min(pack2(o2[0], o2[1]), 0xfffffffeu), min(pack2(o2[2], o2[3]), 0xfffffffeu)}, wsr, ox, WS_O + rb * 4 * 20480, 16);
        TS(7);
        TSFLUSH();
        if (HINT_AT == 3) HINT();
        if (half) {
            if (GY_DELAY > 0) { lds_add(lb + C_OST, lane); __builtin_amdgcn_s_sleep(GY_DELAY); }
            Y_FIRST();
            if (GY_DELAY == 0) lds_add(lb + C_OST, lane);
            STAMP(4);
            spin_ge(lb + C_WO, 4);
            spin_all(lb + C_FREE + (lane & 7) * 4, 18);
            Q_READ(G_Q);
            P_STAGE();
        }
        lds_add(lb + C_OST, lane);
        STAMP(4);
      } else {
        __builtin_amdgcn_s_setprio(L_PRIO);
        unsigned ev[9];
        {
            const int hi = lane >> 5, pos = lane & 31;
#pragma unroll
            for (int v = 0; v < 2; ++v) {
                const int row = 2 * w + hi + 8 * v;
                int L = pos ^ (2 * (v * 4 + (row & 3)));
                L = L > 29 ? L - 2 : L;
                const int part = L < 24 ? L >> 3 : (L - 24) >> 1;
                const int chunk = L < 24 ? (L & 7) + 2 * (hg & 1) : (L & 1) + ((hg & 1) ? 0 : 8);
                ev[v] = row * (NQKV * 2) + chunk * 16 + part * 1280 + hg * 160;
                ev[v + 2] = ev[v] + 16 * NQKV * 2;
            }
            const int ch = (lane & 7) ^ ((4 * (w & 1) + g) & 7);
#pragma unroll
            for (int j = 0; j < 5; ++j) {
                int t = rb2 * 128 - 16 + 8 * w + (lane >> 3) + 32 * j;
                t = t < 0 ? 0 : t;
                t = t > S_LEN - 1 ? S_LEN - 1 : t;
                ev[4 + j] = t * (D_DIM * 2) + ch * 16;
            }
        }
        if (w < 2) ring_issue<5, 0, ring_cum(-1, 5)>(ev, x, wqkv, kr, lb + w * 1024); else ring_issue<4, 0, ring_cum(-1, 4)>(ev, x, wqkv, kr, lb + w * 1024);
        SB();
        STAMP(5);
        unsigned wf[2][3], qf[3];
#pragma unroll
        for (int v = 0; v < 2; ++v)
#pragma unroll
            for (int j = 0; j < 3; ++j) wf[v][j] = opaque(WO_OFF((((w + 2 * v) & 3) + 4 * j) * 64 + lane));
#pragma unroll
        for (int j = 0; j < 3; ++j) { const int c = min(j * 64 + lane, 159); const int row = c / 10, ch = c - row * 10; qf[j] = opaque(row * (D_DIM * 2) + ch * 16); }
        const unsigned sfo = opaque(WS_SF + (lane < 8 ? rb2 * 8 + lane : rbo * 4 + p4) * 128);
        const unsigned fra = opaque(lb + C_FREE + (lane & 7) * 4);
        SB();
        if (w < 2) ring_run<5, -1>(ev, x, wqkv, kr, lb + w * 1024); else ring_run<4, -1>(ev, x, wqkv, kr, lb + w * 1024);
        STAMP(2);
#define WO_PART(chunk, pos, v) { const char* wb_ = (const char*)wo + (size_t)(chunk) * (64 * D_DIM * 2); const unsigned sl_ = lb + ((w + 2 * (v)) & 3) * 1024; \
            GLS(wf[v][0], wb_, sl_, (pos) * 10240); GLS(wf[v][1], wb_, sl_, (pos) * 10240 + 4096); if (((w + 2 * (v)) & 3) < 2) GLS(wf[v][2], wb_, sl_, (pos) * 10240 + 8192); }
        WO_PART(0, 9, 0);
        WO_PART(5, 2, 1);
        if (JL == 16) WAIT_VM(5); else WAIT_VM(9);
        BARRIER();
        WO_PART(1, 10, 0);
        WO_PART(2, 11, 1);
        if (JL == 17) { WAIT_VM(10); BARRIER(); }
        __builtin_amdgcn_s_setprio(WO_PRIO);
        u32x2 fv;
        if (w == 3) asm volatile("global_load_dwordx2 %0, %1, %2 sc1" : "=v"(fv) : "v"(sfo), "s"(ws) : "memory");
        spin_all(fra, 16);
        WO_PART(6, 3, 0);
        WO_PART(3, 12, 1);
        if (w == 3) {
            asm volatile("s_waitcnt vmcnt(5)" : "+v"(fv) :: "memory");
            if (__all(fv[0] == tlo && fv[1] == thi)) lds_st32(lb + C_SENT, 1);
        }
        WO_PART(7, 4, 0);
        WO_PART(4, 13, 1);
        spin_all(fra, 17);
        WO_PART(8, 5, 0);
        WO_PART(9, 6, 1);
#pragma unroll
        for (int j = 0; j < 3; ++j)
            if (j < 2 || lane < 32) GLS(qf[j], (const char*)x + (size_t)(rbo * 64 + w * 16) * (D_DIM * 2) + oc * 160, lb + w * 2560, G_Q + j * 1024);
        STAMP(3);
        if (w == 3 && !__all(fv[0] == tlo && fv[1] == thi)) {
            poll_flags(wsr, sfo, tlo, thi);
            lds_st32(lb + C_SENT, 1);
        }
        WAIT_VM(0);
        lds_add(lb + C_WO, lane);
        STAMP(4);
      }
    } else {
        __builtin_amdgcn_s_setprio(3);
        const int c5 = wave < 4 ? wave : 4;
        const int d = vid * 5 + c5;
        unsigned Dw = *(const __attribute__((address_space(4))) unsigned*)(size_t)(ssm_D + (d & ~1));
        unsigned hp0 = *(const __attribute__((address_space(4))) unsigned*)(size_t)(ssm_k + (vid * 5 + 4) * S_LEN + w * 512 - 2);
        BARRIER();
        if (wave <= 4) hp0 = 0;
        asm volatile("" : "+s"(Dw), "+s"(hp0));
#define SF_DONE() do { WAIT_VM(0); if (lds_add_rtn(lb + C_SFC, lane) == 5) { \
            if (lane == 0) __hip_atomic_store((u64*)(ws + WS_SF + sid * 128), tag, __ATOMIC_RELAXED, __HIP_MEMORY_SCOPE_AGENT); lds_add(lb + C_SFC, lane); } } while (0)
#define WO_CHUNK(chunk, lds) { const char* wb_ = (const char*)wo + (size_t)(chunk) * (64 * D_DIM * 2); \
            _Pragma("unroll") for (int p = 0; p < 10; ++p) GLS(wof[p], wb_, lds, p * 1024); }
#define KV_DONE() do { if (lds_add_rtn(lb + C_KVD, lane) == 9) { \
            if (lane == 0) __hip_atomic_store((u64*)(ws + WS_XF + (cq * 32 + rbv) * 128), tag, __ATOMIC_RELAXED, __HIP_MEMORY_SCOPE_AGENT); lds_add(lb + C_KVD, lane); } } while (0)
        if (wave < 10) {
            const int m0 = (g & 1) * 2 + (g >> 1);
            const unsigned kp = opaque(lb + V_KST + (8 * m0 + q4) * 320 + (m0 >> 1) * 32 + wave * 32 + p4 * 8);
            const unsigned km = opaque(kp - 1280 - ((m0 & 1) ? 0 : 32));
            const unsigned sw_ = opaque(lb + V_STG + wave * 2048 + n16 * 128 + ((m0 + n16) & 7) * 16);
            const unsigned sr_ = opaque(lb + V_STG + wave * 2048 + (lane >> 3) * 128 + ((lane + (lane >> 3)) & 7) * 16);
            const unsigned e1 = opaque(WS_KV + (cq * 160 + wave * 16 + (lane >> 3)) * KV_PITCH + rbv * 128 + (lane & 7) * 16), e2 = opaque(e1 + 8 * KV_PITCH);
            asm volatile("s_waitcnt vmcnt(0)" : "+v"(skw) :: "memory");
            lds_add(lb + C_KV, lane);
            const f32x2 s0 = {lo_f(skw[0]), lo_f(skw[0])}, s1 = {hi_f(skw[0]), hi_f(skw[0])}, s2 = {lo_f(skw[1]), lo_f(skw[1])}, s3 = {hi_f(skw[1]), hi_f(skw[1])};
            spin_ge(lb + C_KV, 10);
            STAMP(2);
            u32x2 kq[2][3], vq[2][2];
#pragma unroll
            for (int i = 0; i < 2; ++i) {
                kq[i][0] = __builtin_bit_cast(u32x2, TRRD((lds_char*)(size_t)(km + i * 10304)));
#pragma unroll
                for (int e = 0; e < 2; ++e) kq[i][e + 1] = __builtin_bit_cast(u32x2, TRRD((lds_char*)(size_t)(kp + i * 10304 + e * 1280)));
#pragma unroll
                for (int e = 0; e < 2; ++e) vq[i][e] = __builtin_bit_cast(u32x2, TRRD((lds_char*)(size_t)(kp + i * 10304 + (V_VST - V_KST) + e * 1280)));
            }
            if (rbv == 0 && m0 == 0) kq[0][0] = (u32x2){0u, 0u};
            f32x2 kf[12], res[8];
#pragma unroll
            for (int e = 1; e < 12; ++e) {
                const unsigned u0 = kq[0][e >> 2][(e >> 1) & 1], u1 = kq[1][e >> 2][(e >> 1) & 1];
                kf[e] = (e & 1) ? (f32x2){hi_f(u0), hi_f(u1)} : (f32x2){lo_f(u0), lo_f(u1)};
            }
#pragma unroll
            for (int e = 0; e < 8; ++e) {
                const unsigned u0 = vq[0][e >> 2][(e >> 1) & 1], u1 = vq[1][e >> 2][(e >> 1) & 1];
                f32x2 a = kf[e + 1] * s3;
                a = kf[e + 2] * s2 + a;
                a = kf[e + 3] * s1 + a;
                a = kf[e + 4] * s0 + a;
                res[e] = a * ((e & 1) ? (f32x2){hi_f(u0), hi_f(u1)} : (f32x2){lo_f(u0), lo_f(u1)});
            }
#pragma unroll
            for (int i = 0; i < 2; ++i) {
                u32x4 pk;
#pragma unroll
                for (int e = 0; e < 4; ++e) pk[e] = pack2(res[2 * e][i], res[2 * e + 1][i]);
                *(lds_vu32x4*)(size_t)(sw_ ^ (i * 64)) = pk;
            }
            const u32x4 pk0 = lds_v128(sr_), pk1 = lds_v128(sr_ + 1024);
            __builtin_amdgcn_raw_buffer_store_b128(pk0, wsr, e1, 0, 16);
            __builtin_amdgcn_raw_buffer_store_b128(pk1, wsr, e2, 0, 16);
            lds_add(lb + C_KVST, lane);
            STAMP(3);
        }
        if (wave >= 8) {
            const unsigned wv = T_WV(), xv_ = T_XV();
            if (wave == 10) {
                T_PAIR(2);
                T_PAIR(4);
                if (WO_MALL && lane < 50) GLT((unsigned)((vid * 50 + lane) * 128), wo);
            } else if (wave == 11) {
                if (lane < 56) {
#pragma unroll
                    for (int c5 = 0; c5 < 5; ++c5)
                        *(lds_u32x4*)(size_t)(lb + (lane < 16 ? V_KV(c5) + lane * 16 : V_F + c5 * XC_BYTES + 15 * XC_ROW + 256 + (lane < 36 ? XC_COPY + (lane - 16) * 16 : XC_B + 4 + (lane - 36) * 16))) = (u32x4){0u, 0u, 0u, 0u};
                }
                const unsigned x4o = opaque(WS_KV + d * KV_PITCH + lane * 16);
                spin_ge(lb + C_KVD, 11);
                if (POLL_DELAY > 0) __builtin_amdgcn_s_sleep(POLL_DELAY);
                const unsigned xfo = WS_XF + ((cq ^ CQ_X) * 32 + (lane & 31)) * 128;
                STAMP(3);
                const u32x2 fv = __builtin_amdgcn_raw_buffer_load_b64(wsr, xfo, 0, 16);
                const bool ok0_ = __all(fv[0] == tlo && fv[1] == thi);
                STAMP(4);
                if (!ok0_) {
                    lds_st32(lb + C_GO, 1);
#pragma nounroll
                    for (int i = 0; i < SPIN_CAP; ++i) {
                        asm volatile("" ::: "memory");
                        const u32x2 v = __builtin_amdgcn_raw_buffer_load_b64(wsr, xfo, 0, 16);
                        if (__all(v[0] == tlo && v[1] == thi)) break;
                        __builtin_amdgcn_s_sleep(POLL_SLEEP);
                    }
                }
                asm volatile("s_mov_b32 m0, %2\n\ts_nop 0\n\tglobal_load_lds_dwordx4 %0, %1 sc1\n\tglobal_load_lds_dwordx4 %0, %1 offset:1024 sc1\n\t"
                             "global_load_lds_dwordx4 %0, %1 offset:2048 sc1\n\tglobal_load_lds_dwordx4 %0, %1 offset:3072 sc1" ::"v"(x4o), "s"(ws), "s"(lb + V_KV4 + 256) : "memory");
                lds_add(lb + C_KVR, lane);
                STAMP(2);
                WAIT_VM(0);
                lds_st32(lb + C_HR, 4);
                STAMP(12);
                spin_ge(lb + C_XD, 4);
                lds_st32(lb + C_GO, 1);
            } else {
                WAIT_VM(0);
                KV_DONE();
                STAMP(11);
                spin_ge(lb + SENT_W, SENT_N);
#pragma unroll
                for (int i = 0; i < 20; ++i) {
                    if (i >= SENT_PACE) waitvm<SENT_PACE - 1>();
                    __builtin_amdgcn_raw_buffer_store_b128(ones, wsr, (wave == 8 ? WS_O : WS_Y) + sid * 20480 + lane * 16 + i * 1024, 0, 16);
                }
                STAMP(4);
                SF_DONE();
                STAMP(12);
            }
            unsigned wof[10];
#pragma unroll
            for (int p = 0; p < 10; ++p) wof[p] = opaque(WO_OFF(p * 64 + lane));
            if (wave == 10) {
                spin_ge(lb + GO_W, GO_N);
                T_PAIR(6);
                if (T4_EARLY) WAIT_VM(1); else spin_ge(lb + C_XD, 4);
                T_PAIR(8);
                {
                    u64 pc = (u64)(size_t)code0;
                    const unsigned cl = lane * 16 + r;
                    if (cl < CODE_LINES) GLT(cl * 128, pc);
                }
                spin_ge(lb + C_XD, 4);
#pragma unroll
                for (int i = 0; i < 7; ++i) {
                    waitvm<WOT_PACE>();
                    if (i < 6 || lane < 16) GLT((unsigned)(lane * 128), (const char*)wo + (r * 400 + i * 64) * 128);
                }
                WAIT_VM(0);
                lds_st32(lb + C_WT, 1);
                STAMP(4);
            }
            {
                unsigned qf[3];
#pragma unroll
                for (int j = 0; j < 3; ++j) { const int c = min(j * 64 + lane, 159); const int row = c / 10, ch = c - row * 10; qf[j] = opaque(row * (D_DIM * 2) + ch * 16); }
                spin_mid(lb + C_XD, 4);
#pragma unroll
                for (int j = 0; j < 3; ++j)
                    if (j < 2 || lane < 32) GLS(qf[j], (const char*)x + (size_t)(rbo * 64 + w * 16) * (D_DIM * 2) + oc * 160, lb + w * 2560, V_QS + j * 1024);
                WAIT_VM(0);
            }
            Q_READ(V_QS);
            lds_add(lb + C_QR, lane);
            spin_mid(lb + C_WT, 1);
            STAMP(5);
            WO_CHUNK(2 + w, lb + V_WO + (2 + w) * 10240);
            if (wave == 11) {
                spin_mid(lb + C_SFC, 7);
                poll_flags(wsr, WS_SF + (lane < 32 ? lane * 4 + (cq ^ CQ_X) : rbo * 4 + p4) * 128, tlo, thi);
                lds_st32(lb + C_SENT, 1);
                STAMP(10);
            } else if (w < 2) {
                spin_mid(lb + (w ? C_QR : C_RED2), 4);
                WO_CHUNK(w, lb + V_WO + w * 10240);
            }
            WAIT_VM(0);
            lds_add(lb + C_WO, lane);
            STAMP(6);
            spin_mid(lb + C_CONVN, 8);
            if (VY_DELAY > 0) { lds_add(lb + C_OST, lane); __builtin_amdgcn_s_sleep(VY_DELAY); }
            Y_FIRST();
            if (VY_DELAY == 0) lds_add(lb + C_OST, lane);
            STAMP(7);
            P_STAGE();
        } else {
            const unsigned xb = lb + V_F + c5 * XC_BYTES;
            const unsigned hb = lb + V_KV(c5) + 256;
            const float Dd = (d & 1) ? hi_f(Dw) : lo_f(Dw);
            u32x4 hv[4];
            const unsigned l16 = lane * 16;
            {
                const char* hsrc = (const char*)ssm_k + (size_t)d * (S_LEN * 2) + (wave < 4 ? 0 : w * 1024);
                spin_ge(lb + C_KVST, 10);
#define H_LD(i) asm volatile("global_load_dwordx4 %0, %1, %2 offset:%3" : "=v"(hv[i]) : "v"(l16), "s"(hsrc), "n"((i) * 1024) : "memory")
                H_LD(0);
                if (wave < 4) { H_LD(1); H_LD(2); H_LD(3); WAIT_VM(4); } else WAIT_VM(1);
                KV_DONE();
                STAMP(4);
            }
            {
                const unsigned da = xb + (3 - g) * XC_ROW + (15 - n16) * 16 + (wave < 4 ? 0 : -w * (4 * XC_ROW));
                u32x4 ra[4], rb[4];
                asm volatile("s_waitcnt vmcnt(0)" : "+v"(hv[0]), "+v"(hv[1]), "+v"(hv[2]), "+v"(hv[3]) :: "memory");
#define H_PREV(i) __builtin_amdgcn_update_dpp((i) == 0 ? hp0 : __builtin_amdgcn_readlane(hv[(i) == 0 ? 0 : (i) - 1][3], 63), hv[i][3], 0x138, 0xf, 0xf, false)
#define BFI16(lo, hi) (((lo) & 0xffffu) | ((hi) & 0xffff0000u))
#define H_PREP(i) { ra[i] = (u32x4){__builtin_amdgcn_alignbit(hv[i][3], hv[i][3], 16), __builtin_amdgcn_alignbit(hv[i][2], hv[i][2], 16), __builtin_amdgcn_alignbit(hv[i][1], hv[i][1], 16), __builtin_amdgcn_alignbit(hv[i][0], hv[i][0], 16)}; \
                    rb[i] = (u32x4){BFI16(hv[i][3], hv[i][2]), BFI16(hv[i][2], hv[i][1]), BFI16(hv[i][1], hv[i][0]), BFI16(hv[i][0], H_PREV(i))}; \
                    *(lds_u32x4*)(size_t)(da + (3 - (i)) * (4 * XC_ROW) + XC_COPY) = ra[i]; *(lds_u32x4*)(size_t)(da + (3 - (i)) * (4 * XC_ROW) + XC_B + 4) = rb[i]; }
#define H_DUP(i) { *(lds_u32x4*)(size_t)(da + (3 - (i)) * (4 * XC_ROW) + XC_COPY - 32) = ra[i]; *(lds_u32x4*)(size_t)(da + (3 - (i)) * (4 * XC_ROW) + XC_B + 4 - 32) = rb[i]; }
                H_PREP(0)
                if (wave < 4) {
                    H_PREP(1) H_PREP(2) H_PREP(3)
                    if (n16 == 15) { H_DUP(0) H_DUP(1) H_DUP(2) if (g < 3) H_DUP(3) }
                } else {
                    if (n16 == 15 && (w < 3 || g < 3)) H_DUP(0)
                }
            }
            STAMP(9);
            const int j0 = wave >= 4 ? 2 * w : 0;
            const unsigned kp = hb + 2 * (256 * j0 + 128 * (g >> 1) + 8 * n16 + 4 * (g & 1));
            const int ts = 256 * (j0 + (g & 1)) + 128 * (g >> 1) + 8 * n16;
            const unsigned yo = WS_Y + d * 128 + (ts >> 6) * (D_DIM * 128) + (ts & 63) * 2;
            if (wave < 4) {
                const int b1 = n16 >> 3, phi = n16 & 7;
                const unsigned xp = xb + ((phi & 1) ? XC_COPY - 4 : XC_B) + (1 - b1) * XC_ROW + 2 * (8 - 2 * (phi >> 1) + 8 * g);
                const unsigned hp = hb + 2 * (8 * (n16 + g) - 120);
                const int sx4 = (4 - w) * XC_BYTES + (w >> 1) * XC_ROW + (w & 1) * 128, sh4 = V_KV4 - w * KV_BYTES + w * 128;
                unsigned c0_, c1_;
                __builtin_amdgcn_s_setprio(CONV_PRIO);
                spin_ge(lb + C_KVR, 5);
                STAMP(6);
                CSTAMP(0);
                TS(11);
                asm volatile(CONV_ASM
                             : [cnt] "=&s"(c0_), [tmp] "=&s"(c1_)
                             : [xp] "v"(xp), [hp] "v"(hp), [sx4] "s"(sx4), [sh4] "s"(sh4), [kvl] "s"(hb), [kvr] "v"(kp), [scr] "v"(lb + V_SCR + w * 8192 + l16),
                               [ctl] "v"(lb + L_CTRL), [dd] "s"(Dd), [cap] "s"(SPIN_CAP), [xo] "v"(WS_KV + d * KV_PITCH + l16), [wsp] "s"(ws), [yo] "v"(yo)
                             : CONV_CLOB);
                __builtin_amdgcn_s_setprio(3);
                CSTAMP(1);
            } else {
                lds_add(lb + C_KVR, lane);
                {
                    const int grow = (xcd >> 1) * 512 + r * 32 + (xcd & 1) * 16 + w * 4 + g;
                    const unsigned go = opaque(grow * (D_DIM * 2) + n16 * 16);
                    if (GATE_LATE) spin_mid(lb + C_XD, 4);
                    u32x4 gx[5], gv[5];
#pragma unroll
                    for (int i = 0; i < 5; ++i) {
                        gx[i] = *(const u32x4*)((const char*)x + go + i * 256);
                        gv[i] = *(const u32x4*)((const char*)gw + n16 * 16 + i * 256);
                    }
                    float sa[4] = {0.f, 0.f, 0.f, 0.f};
#pragma unroll
                    for (int i = 0; i < 5; ++i)
#pragma unroll
                        for (int e = 0; e < 4; ++e) sa[e] = __builtin_fmaf(hi_f(gx[i][e]), hi_f(gv[i][e]), __builtin_fmaf(lo_f(gx[i][e]), lo_f(gv[i][e]), sa[e]));
                    float sacc = (sa[0] + sa[1]) + (sa[2] + sa[3]);
                    sacc += __builtin_amdgcn_update_dpp(0.f, sacc, 0x128, 0xf, 0xf, true);
                    sacc += __builtin_amdgcn_update_dpp(0.f, sacc, 0x124, 0xf, 0xf, true);
                    sacc += __builtin_amdgcn_update_dpp(0.f, sacc, 0x122, 0xf, 0xf, true);
                    sacc += __builtin_amdgcn_update_dpp(0.f, sacc, 0x121, 0xf, 0xf, true);
                    const float gres = rbf(__builtin_amdgcn_rcpf(1.f + __expf(-rbf(sacc))));
                    if (n16 == 0) __builtin_amdgcn_raw_buffer_store_b32(__float_as_uint(gres), wsr, WS_G + grow * 4, 0, 16);
                    SF_DONE();
                    STAMP(6);
                }
                unsigned wof[10];
#pragma unroll
                for (int p = 0; p < 10; ++p) wof[p] = opaque(WO_OFF(p * 64 + lane));
                spin_mid(lb + C_RED, 4);
                STAMP(5);
                f32x4 acc[2];
#pragma unroll
                for (int e = 0; e < 2; ++e) {
                    const u32x2 kk = *(const lds_u32x2*)(size_t)(kp + e * 512);
                    acc[e] = (f32x4){lo_f(kk[0]) * Dd, hi_f(kk[0]) * Dd, lo_f(kk[1]) * Dd, hi_f(kk[1]) * Dd};
#pragma unroll
                    for (int o = 0; o < 4; ++o) acc[e] += *(const lds_f32x4*)(size_t)(lb + V_SCR + o * 8192 + (2 * w + e) * 1024 + l16);
                }
                lds_add(lb + C_RED2, lane);
                const auto r0 = __builtin_amdgcn_permlane16_swap(min(pack2(acc[0][0], acc[0][1]), 0xfffffffeu), min(pack2(acc[1][0], acc[1][1]), 0xfffffffeu), false, false);
                const auto r1 = __builtin_amdgcn_permlane16_swap(min(pack2(acc[0][2], acc[0][3]), 0xfffffffeu), min(pack2(acc[1][2], acc[1][3]), 0xfffffffeu), false, false);
                STAMP(7);
                spin_ge(lb + C_RED2, 4);
                spin_mid(lb + C_WT, 1);
                WO_CHUNK(6 + w, lb + V_WO + (6 + w) * 10240);
                WAIT_VM(0);
                lds_add(lb + C_WO, lane);
                spin_mid(lb + C_SENT, 1);
                __builtin_amdgcn_raw_buffer_store_b128((u32x4){r0[0], r1[0], r0[1], r1[1]}, wsr, yo, 0, 16);
            }
            lds_add(lb + C_CONVN, lane);
            STAMP(12);
        }
    }

    const int t0 = rbo * 64;
    __builtin_amdgcn_s_setprio(T_PRIO);
    {
        RELANE();
        const int kh = wave < 4;
        const int ofo = WS_O + ((rbo * 4 + w) * 20 + kh * 10) * 1024 + lane * 16;
        const int hsw = (g & 1) * 4;
        const unsigned lofs = lb + (8 * g + hsw + q4) * 160 + p4 * 8;
        lds_char* pc = (lds_char*)(size_t)(lofs + (isG ? (kh ? 2 : 9) * 10240 : V_WO + kh * 51200));
        lds_char* pd = pc + (4 - 2 * hsw) * 160;
#define WO_RD1(dst, ks, ct) dst = cat8(TRRD(pc + (ks) * 5120 + (ct) * 32), TRRD(pd + (ks) * 5120 + (ct) * 32))
        bf16x8 wB[NBUF][5];
        spin_mid(lb + C_WO, isG ? 4 : 8);
#pragma unroll
        for (int ks = 0; ks < NBUF; ++ks)
#pragma unroll
            for (int ct = 0; ct < 5; ++ct) WO_RD1(wB[ks][ct], ks, ct);
        spin_ge(lb + C_SENT, 1);
        if (isG) {
            spin_ge(lb + C_OST, G_OST);
            if (O_DELAY > 0) __builtin_amdgcn_s_sleep(O_DELAY);
        } else {
            if (wave == 4) {
                if (W_LATE) spin_ge(lb + C_OST, 4);
#pragma nounroll
                for (int it = 0; it < SPIN_CAP; ++it) {
                    asm volatile("" ::: "memory");
                    const int pr = lane & 7;
                    if (HINT_AT < 9) {
                        const u32x2 v = __builtin_amdgcn_raw_buffer_load_b64(wsr, WS_HF + ((rbo >> 1) * 8 + pr) * 128, 0, 16);
                        if (__all(v[0] == tlo && v[1] == thi)) break;
                    } else {
                        const unsigned v = __builtin_amdgcn_raw_buffer_load_b32(wsr, WS_O + ((rbo * 4 + WATCH_RT) * 20 + ((pr * 80) >> 5)) * 1024 + (((pr * 10) & 3) * 16) * 16, 0, 16);
                        if (!__any(v == 0xffffffffu)) break;
                    }
                    __builtin_amdgcn_s_sleep(W_SLEEP);
                }
                if (V_ODELAY > 0) __builtin_amdgcn_s_sleep(V_ODELAY);
                lds_add(lb + C_OST, lane);
            }
            spin_ge(lb + C_OST, 5);
        }
        __builtin_amdgcn_s_setprio(PASS_PRIO);
        STAMP(8);
        u32x4 of[10];
#pragma unroll
        for (int ks = 0; ks < O_AHEAD; ++ks) of[ks] = __builtin_amdgcn_raw_buffer_load_b128(wsr, ofo + ks * 1024, 0, 16);
        SB();
        STAMP(9);
        f32x4 acc[5];
        float g0;
        const f32x4 z4 = {0.f, 0.f, 0.f, 0.f};
#define WO_PASS(first) { SB(); \
            _Pragma("unroll") for (int ks = 0; ks < 10; ++ks) \
                _Pragma("unroll") for (int ct = 0; ct < 5; ++ct) { \
                    acc[ct] = __builtin_amdgcn_mfma_f32_16x16x32_bf16(wB[ks % NBUF][ct], __builtin_bit_cast(bf16x8, of[ks]), ks ? acc[ct] : z4, 0, 0, 0); SB(); \
                    if (first && ct == 0 && ks + O_AHEAD < 10) { of[ks + O_AHEAD] = __builtin_amdgcn_raw_buffer_load_b128(wsr, ofo + (ks + O_AHEAD) * 1024, 0, 16); SB(); } \
                    if (first && ct == 0 && ks + O_AHEAD == 10) { g0 = __uint_as_float(__builtin_amdgcn_raw_buffer_load_b32(wsr, WS_G + (t0 + w * 16 + n16) * 4, 0, 16)); SB(); } \
                    if (ks + NBUF < 10) { WO_RD1(wB[ks % NBUF][ct], ks + NBUF, ct); SB(); } \
                } }
        WO_PASS(true);
        g0 = 1.f - g0;
        STAMP(10);
        if (__any(acc[0][0] != acc[0][0])) {
#pragma unroll
            for (int ks = 0; ks < NBUF; ++ks)
#pragma unroll
                for (int ct = 0; ct < 5; ++ct) WO_RD1(wB[ks][ct], ks, ct);
            SB();
#pragma nounroll
            for (int it = 0; it < SPIN_CAP; ++it) {
                bool any = false;
#pragma unroll
                for (int ks = 0; ks < 10; ++ks)
                    if (SENT4(of[ks])) {
                        asm volatile("global_load_dwordx4 %0, %1, %2 sc1" : "+v"(of[ks]) : "v"(ofo), "s"(ws + ks * 1024) : "memory");
                        any = true;
                    }
                if (!any) break;
                asm volatile("s_waitcnt vmcnt(0)" : "+v"(of[0]), "+v"(of[1]), "+v"(of[2]), "+v"(of[3]), "+v"(of[4]), "+v"(of[5]), "+v"(of[6]), "+v"(of[7]), "+v"(of[8]), "+v"(of[9]) :: "memory");
            }
            WO_PASS(false);
        }
        STAMP(11);
        const unsigned xa = lb + (isG ? G_XCH : V_XCH) + w * 5120 + lane * 16;
        if (kh) {
#pragma unroll
            for (int ct = 0; ct < 5; ++ct) *(lds_f32x4*)(size_t)(xa + ct * 1024) = acc[ct];
            lds_add(lb + C_XCH + w * 4, lane);
        } else {
            spin_ge(lb + C_XCH + w * 4, 2);
            STAMP(13);
            unsigned pk[5][2];
#pragma unroll
            for (int ct = 0; ct < 5; ++ct) {
                const u32x2 p2 = *(const lds_u32x2*)(size_t)(qa + ct * 32);
                const f32x4 fin = (f32x4){lo_f(p2[0]), hi_f(p2[0]), lo_f(p2[1]), hi_f(p2[1])} + g0 * (acc[ct] + *(const lds_f32x4*)(size_t)(xa + ct * 1024));
                pk[ct][0] = pack2(fin[0], fin[1]);
                pk[ct][1] = pack2(fin[2], fin[3]);
            }
            u16* ob = out + (size_t)(t0 + w * 16 + n16) * D_DIM + oc * 80;
#pragma unroll
            for (int p = 0; p < 2; ++p) {
                const auto r0 = __builtin_amdgcn_permlane16_swap(pk[2 * p][0], pk[2 * p + 1][0], false, false);
                const auto r1 = __builtin_amdgcn_permlane16_swap(pk[2 * p][1], pk[2 * p + 1][1], false, false);
                *(u32x4*)(ob + (2 * p + (g & 1)) * 16 + (g >> 1) * 8) = (u32x4){r0[0], r1[0], r0[1], r1[1]};
            }
            *(u32x2*)(ob + 64 + 4 * g) = (u32x2){pk[4][0], pk[4][1]};
        }
        FLUSH();
    }
}

#undef x
#undef kin
#undef vin
#undef shift_k
#undef ssm_k
#undef ssm_D
#undef wqkv
#undef wo
#undef gw
#undef ws
#undef out
#undef wsr
extern "C" void kernel_launch(void* const* d_in, const int* in_sizes, int n_in, void* d_out, int out_size,
                              void* d_ws, size_t ws_size, hipStream_t stream) {
    const u16* q = (const u16*)d_in[0];
    const u16* k = (const u16*)d_in[1];
    const u16* v = (const u16*)d_in[2];
    const u16* shift_k = (const u16*)d_in[3];
    const u16* ssm_k = (const u16*)d_in[4];
    const u16* ssm_D = (const u16*)d_in[5];
    const u16* wqkv = (const u16*)d_in[7];
    const u16* wo = (const u16*)d_in[8];
    const u16* gw = (const u16*)d_in[9];
    fused_kernel<<<256, 768, 0, stream>>>(q, wqkv, k, v, shift_k, ssm_k, ssm_D, wo, gw, (char*)d_ws, (u16*)d_out);
}
```

```cpp
#define CODE_LINES 371
#define V_DELAY 6
#define MAP1 1
#define HINT_AT -1
#define VY_DELAY 10
#define GY_DELAY 4
#include <hip/hip_runtime.h>
#include <hip/hip_bf16.h>
#include <stdint.h>

#define S_LEN 2048
#define D_DIM 640
#define NQKV 1920

typedef unsigned short u16;
typedef unsigned long long u64;
typedef __attribute__((ext_vector_type(8))) short bf16x8;
typedef __attribute__((ext_vector_type(4))) short s16x4;
typedef __attribute__((ext_vector_type(4))) float f32x4;
typedef __attribute__((ext_vector_type(2))) float f32x2;
typedef __attribute__((ext_vector_type(4))) unsigned u32x4;
typedef __attribute__((ext_vector_type(2))) unsigned u32x2;
typedef __attribute__((ext_vector_type(2))) __bf16 bf16x2_t;
typedef __attribute__((address_space(3))) s16x4 lds_s16x4;
typedef __attribute__((address_space(3))) const s16x4 lds_cs16x4;
typedef __attribute__((address_space(3))) const bf16x8 lds_cbf16x8;
typedef __attribute__((address_space(3))) char lds_char;

#define __float_as_uint(f) __builtin_bit_cast(unsigned, (float)(f))
#define __uint_as_float(u) __builtin_bit_cast(float, (unsigned)(u))
#define __any(c) (__builtin_amdgcn_ballot_w64(c) != 0)
#define __all(c) (__builtin_amdgcn_ballot_w64(!(c)) == 0)
extern "C" __device__ unsigned long long llvm_dispatch_id() __asm("llvm.amdgcn.dispatch.id");

__device__ __forceinline__ float bf2f(u16 x) { return __uint_as_float(((unsigned)x) << 16); }
__device__ __forceinline__ u16 f2bf(float f) {
    __hip_bfloat16 b = __float2bfloat16(f);
    return *reinterpret_cast<u16*>(&b);
}
__device__ __forceinline__ float rbf(float f) { return bf2f(f2bf(f)); }
__device__ __forceinline__ float lo_f(unsigned u) { return __uint_as_float(u << 16); }
__device__ __forceinline__ float hi_f(unsigned u) { return __uint_as_float(u & 0xffff0000u); }
__device__ __forceinline__ unsigned pack2(float a, float b) {
    return __builtin_bit_cast(unsigned, __builtin_convertvector((f32x2){a, b}, bf16x2_t));
}
__device__ __forceinline__ float dot2(unsigned a, unsigned b, float c) {
    return __builtin_amdgcn_fdot2_f32_bf16(__builtin_bit_cast(bf16x2_t, a), __builtin_bit_cast(bf16x2_t, b), c, false);
}

#define SB() __builtin_amdgcn_sched_barrier(0)
#define WAIT_VM(n) asm volatile("s_waitcnt vmcnt(" #n ")" ::: "memory")
#define RAW_BARRIER() do { asm volatile("s_waitcnt lgkmcnt(0)" ::: "memory"); __builtin_amdgcn_s_barrier(); } while (0)

#define KV_PITCH 4224
#define WS_KV 4096
#define WS_O (WS_KV + D_DIM * KV_PITCH)
#define WS_Y (WS_O + D_DIM * S_LEN * 2)
#define WS_G (WS_Y + D_DIM * S_LEN * 2)
#define WS_XF (WS_G + S_LEN * 4)
#define WS_SF (WS_XF + 128 * 128)
#define WS_HF (WS_SF + 128 * 128)
#define WS_HN (WS_HF + 128 * 128)
#define WS_HX (WS_HN + 128 * 128)
#define WS_END (WS_HX + 128 * 5120)
#define WS_DBG (WS_END + 4096)

#define L_A 0
#define A_SLOT 18432
#define L_W 55296
#define W_SLOT 16384
#define W_PITCH 512
#define NW 6
#define G_XCH 69632
#define G_Y 0
#define G_WO1 18432
#define G_WO0 (G_XCH + 20480)
#define G_Q (G_WO0 + 51200)
#define G_VX 153600
#define G_HX 158208
#define G_VXD (G_HX + 4608)
#define KV_BYTES 4352
#define V_F (4 * KV_BYTES)
#define XC_ROW 288
#define XC_B 12
#define XC_COPY 4944
#define XC_BYTES (XC_COPY + 17 * XC_ROW)
#define V_KV4 (V_F + 5 * XC_BYTES)
#define V_KV(c) ((c) < 4 ? (c) * KV_BYTES : V_KV4)
#define V_WO (V_KV4 + KV_BYTES - 10240)
#define V_KST (V_WO + 10240 + 1312)
#define V_VST (V_KST + 20608)
#define V_STG 113536
#define V_QS (V_WO + 10240)
#define V_SCR 130560
#define V_XCH 0
#define V_Y 40960
#define Y_PITCH 160
#define L_TOUCH 163328
#define L_CTRL 163584
#define LDS_TOTAL 163840
static_assert(V_VST + 20608 <= V_STG && V_STG % 128 == 0 && V_STG + 20480 <= L_TOUCH && V_WO >= V_F + 4 * XC_BYTES && V_WO + 10 * 10240 <= L_TOUCH &&
              V_WO + 6 * 10240 <= V_SCR && V_SCR + 32768 <= L_TOUCH && V_Y + 80 * Y_PITCH <= V_WO && V_WO % 16 == 0, "lds");
static_assert(G_Q + 10240 <= G_VX && G_WO1 + 51200 <= G_XCH && L_W + NW * W_SLOT <= G_VX && G_VX + 4608 <= G_HX && G_VXD + 520 <= L_CTRL && G_Y + 80 * Y_PITCH <= A_SLOT && G_Q + 20480 <= G_VXD, "lds");
#define C_FREE (L_CTRL + 16)
#define C_OST (L_CTRL + 48)
#define C_SENT (L_CTRL + 52)
#define C_WO (L_CTRL + 56)
#define C_KV (L_CTRL + 60)
#define C_KVST (L_CTRL + 64)
#define C_CONVN (L_CTRL + 68)
#define C_Y (L_CTRL + 72)
#define C_KVR (L_CTRL + 76)
#define C_XD (L_CTRL + 80)
#define C_RED (L_CTRL + 84)
#define C_OWN (L_CTRL + 88)
#define C_QR (L_CTRL + 92)
#define C_TY (L_CTRL + 96)
#define C_TR (L_CTRL + 112)
#define C_OS1 (L_CTRL + 104)
#define C_OSC (L_CTRL + 108)
#define C_SFC (L_CTRL + 136)
#define C_GO (L_CTRL + 140)
#define C_HR (L_CTRL + 144)
#define C_RED2 (L_CTRL + 148)
#define C_KVD (L_CTRL + 152)
#define C_WT (L_CTRL + 156)
#define C_HX (L_CTRL + 160)
#define C_XCH (L_CTRL + 176)
#define C_FULL (L_CTRL + 208)
#define C_HS (L_CTRL + 100)
#define C_LK (L_CTRL + 52)
#define C_TM (L_CTRL + 192)

#define KNOB(n, v)
#define SPIN_CAP 4000000
#define GH 0
#define WO_PRIO 0
#define T_PRIO 3
#define C0_PRIO 2
#define C1_PRIO 1
#define L_PRIO 3
#define O_SLEEP 4
#define O_DELAY 0
#define POLL_SLEEP 2
#define Y_SLEEP 8
#define W_SLEEP 4
#define X_SLEEP 2
#define X_DELAY 0
#define CONV_PRIO 2
#define NBUF 4
#define WATCH_RT 2
#define JL 17
#define RING_E0 70
#define RING_F 105
#define WOT_SPLIT 0
#define GO_W C_KVD
#define GO_N 11
#define SENT_PACE 10
#define GATE_LATE 0
#define WOT_PACE 7
#define SENT_W C_XD
#define SENT_N 4
#define O_AHEAD 4
#define PASS_PRIO 1
#define G_OST 8
#define V_ODELAY 0
#define W_LATE 1
#define Y_SL0 0
#define SF_DELAY 8
#define WO_MALL 1
#define T4_EARLY 0
#define POLL_DELAY 16
#define T4_W C_KV
#define T4_N 10
#define SENT_DELAY 0
#define CF_W C_RED2
#define CF_N 4
#define CF_DELAY 0
#define NB3 6
#define GW0_DELAY 4
#define GW1_DELAY 4
#define VW0_DELAY 0
#define VW1_DELAY 6
#define ROT_OC 3
#define AUX_PRIO 1
#define RING_T 5
#define H_EARLY 0
#define T8_AT 1
#define RING_J 7
#define RING_F2 105
#define LATE_W C_RED
#define LATE_N 4
#define WOT_W C_XD
#define WOT_N 4
#define GATE_W C_XD
#define GATE_N 4
#define WO2_W C_XD
#define WO2_N 4
#define V_BOTH true
#define NRES 4
#define OS_EARLY 1
#define OS_DELAY 0
#define WO2_FIRST 0
#define HINT_W 3
#define D0 5000
#define D1 5700
#define DY 7400
#define CF_SLEEP 6
#define V_MORE 2
#define G_MORE 1
#define SELF_AT 0
#define LOOK_AT 2
#define VL_DELAY 0
#define ENT_PRIO 1
#define G_GATE0 8
#define G_GATE1 8
#define YEARLY 1
#define Y_SOON 10
#define T_JOIN 0
#define X_NOOS 0
#define X_NOYS 0
#define X_NOG 0
#define X_TALL 0
#define L_FIRST 1
#define X_SHIFT 0
#define D_AHEAD 9000
#define HALO_EARLY 1
#define OUT_P 0
#define OUT_POL ""
#define G_LEAD 0
#define XF_AUX 16
#define CQ_X 2
#define HYB 0
#define RETOUCH 1
#define HX_AT 2
#define HX_SLEEP 2
#define NHRETRY()
#define HL_AT 3
#define HS_N 4
#define HS_POS 1
#define FB_T 30
#define KROT ((0x27057250u >> (xcd * 4)) & 15)

#define STAMP(id)
#define FLUSH()
#define CSTAMP(i)

#define FLUSH_LO()
#define TS(i)
#define TSFLUSH()
__device__ __forceinline__ unsigned lds_ld32(unsigned a) {
    unsigned v;
    asm volatile("ds_read_b32 %0, %1\n\ts_waitcnt lgkmcnt(0)" : "=v"(v) : "v"(a) : "memory");
    return v;
}
__device__ __forceinline__ unsigned lds_min4(unsigned a) {
    u32x4 v;
    asm volatile("ds_read_b128 %0, %1\n\ts_waitcnt lgkmcnt(0)" : "=v"(v) : "v"(a) : "memory");
    return min(min(v[0], v[1]), min(v[2], v[3]));
}
__device__ __forceinline__ unsigned lds_min8(unsigned a) {
    u32x4 v, u;
    asm volatile("ds_read_b128 %0, %2\n\tds_read_b128 %1, %2 offset:16\n\ts_waitcnt lgkmcnt(0)" : "=&v"(v), "=&v"(u) : "v"(a) : "memory");
    return min(min(min(v[0], v[1]), min(v[2], v[3])), min(min(u[0], u[1]), min(u[2], u[3])));
}
__device__ __forceinline__ void lds_st32(unsigned a, unsigned v) {
    asm volatile("ds_write_b32 %0, %1" ::"v"(a), "v"(v) : "memory");
}
__device__ __forceinline__ unsigned lds_add_rtn(unsigned a, int lane) {
    unsigned old = 0;
    if (lane == 0) {
        unsigned one = 1;
        asm volatile("ds_add_rtn_u32 %0, %1, %2\n\ts_waitcnt lgkmcnt(0)" : "=v"(old) : "v"(a), "v"(one) : "memory");
    }
    return __builtin_amdgcn_readfirstlane(old);
}
__device__ __forceinline__ void lds_add(unsigned a, int lane, unsigned n = 1) {
    if (lane == 0) asm volatile("ds_add_u32 %0, %1" ::"v"(a), "v"(n) : "memory");
}
__device__ __forceinline__ void spin_min4(unsigned a, unsigned target) {
#pragma nounroll
    for (int i = 0; i < SPIN_CAP; ++i) {
        if (__builtin_amdgcn_readfirstlane(lds_min4(a)) >= target) break;
        __builtin_amdgcn_s_sleep(1);
    }
}
__device__ __forceinline__ void spin_min8(unsigned a, unsigned target) {
#pragma nounroll
    for (int i = 0; i < SPIN_CAP; ++i) {
        if (__builtin_amdgcn_readfirstlane(lds_min8(a)) >= target) break;
        __builtin_amdgcn_s_sleep(1);
    }
}
__device__ __forceinline__ void spin_ge(unsigned a, unsigned target) {
#pragma nounroll
    for (int i = 0; i < SPIN_CAP; ++i) {
        if (__builtin_amdgcn_readfirstlane(lds_ld32(a)) >= target) break;
        __builtin_amdgcn_s_sleep(1);
    }
}
typedef __attribute__((address_space(3))) volatile u32x4 lds_vu32x4;
typedef __attribute__((address_space(3))) volatile unsigned lds_vu32_t;
__device__ __forceinline__ u32x4 lds_v128(unsigned a) { return *(lds_vu32x4*)(size_t)a; }
__device__ __forceinline__ unsigned lds_v32(unsigned a) { return *(lds_vu32_t*)(size_t)a; }
__device__ __forceinline__ float fmax_raw(float a, float b) { return __builtin_elementwise_maximum(a, b); }
__device__ __forceinline__ float fmax3_raw(float a, float b, float c) { return __builtin_elementwise_maximum(__builtin_elementwise_maximum(a, b), c); }
__device__ __forceinline__ unsigned opaque(unsigned v) { asm volatile("" : "+v"(v)); return v; }
__device__ __forceinline__ bf16x8 cat8(s16x4 a, s16x4 b) { return __builtin_shufflevector(a, b, 0, 1, 2, 3, 4, 5, 6, 7); }

__device__ __forceinline__ void poll_flags(__amdgpu_buffer_rsrc_t rs, unsigned off, unsigned tlo, unsigned thi) {
#pragma nounroll
    for (int i = 0; i < SPIN_CAP; ++i) {
        asm volatile("" ::: "memory");
        const u32x2 v = __builtin_amdgcn_raw_buffer_load_b64(rs, off, 0, 16);
        if (__all(v[0] == tlo && v[1] == thi)) break;
        __builtin_amdgcn_s_sleep(POLL_SLEEP);
    }
    __builtin_amdgcn_fence(__ATOMIC_ACQUIRE, "wavefront");
}

__device__ __forceinline__ void spin_slow(unsigned a, unsigned target) {
#pragma nounroll
    for (int i = 0; i < SPIN_CAP; ++i) {
        if (__builtin_amdgcn_readfirstlane(lds_ld32(a)) >= target) break;
        __builtin_amdgcn_s_sleep(6);
    }
}
__device__ __forceinline__ void spin_mid(unsigned a, unsigned target) {
#pragma nounroll
    for (int i = 0; i < SPIN_CAP; ++i) {
        if (__builtin_amdgcn_readfirstlane(lds_ld32(a)) >= target) break;
        __builtin_amdgcn_s_sleep(3);
    }
}
__device__ __forceinline__ void spin_all(unsigned a, unsigned target) {
#pragma nounroll
    for (int i = 0; i < SPIN_CAP; ++i) {
        if (__all(lds_ld32(a) >= target)) break;
        __builtin_amdgcn_s_sleep(1);
    }
}

template <int N> __device__ __forceinline__ void waitvm() { asm volatile("s_waitcnt vmcnt(%0)" ::"n"(N) : "memory"); }
#define BARRIER() asm volatile("s_barrier" ::: "memory")
#define GLS(voff, sbase, lw, ldsoff) asm volatile("s_add_u32 m0, %2, %3\n\ts_nop 0\n\tglobal_load_lds_dwordx4 %0, %1" ::"v"(voff), "s"(sbase), "s"(lw), "n"(ldsoff) : "memory", "scc")
#define GLT(voff, sbase) asm volatile("s_mov_b32 m0, %2\n\ts_nop 0\n\tglobal_load_lds_dword %0, %1" ::"v"(voff), "s"(sbase), "s"(lb + L_TOUCH) : "memory")
#define TRRD(p) __builtin_amdgcn_ds_read_tr16_b64_v4i16((lds_s16x4*)(p))

constexpr int ring_total(int NA) { return 10 * (8 + NA); }
constexpr int ring_need(int J, int NA) { return J < 0 ? NA + 8 : (J & 1) ? (NA + 8) * ((J + 3) / 2) : (NA + 8) * ((J + 2) / 2) + 4 + NA; }
constexpr int ring_isA(int p, int NA) { return p < NA ? 1 : p < NA + 8 ? 0 : (p % (8 + NA) >= 4 && p % (8 + NA) < 4 + NA); }
constexpr int ring_tile(int p, int NA) { return p < NA ? 0 : p < NA + 8 ? (p - NA) / 4 : ring_isA(p, NA) ? p / (8 + NA) : 2 * (p / (8 + NA)) + (p % (8 + NA) >= 4); }
constexpr int ring_sub(int p, int NA) { return p < NA ? p : p < NA + 8 ? (p - NA) % 4 : p % (8 + NA) < 4 ? p % (8 + NA) : p % (8 + NA) < 4 + NA ? p % (8 + NA) - 4 : p % (8 + NA) - 4 - NA; }
constexpr int ring_cum(int J, int NA) {
    if (J > JL || J >= 16) return ring_total(NA);
    int c = NA + 8 + (8 + NA) * (2 * RING_E0 + RING_F * (J + 1) + (J > RING_J ? (RING_F2 - RING_F) * (J - RING_J) : 0)) / 200;
    if (c > ring_total(NA)) c = ring_total(NA);
    if (c < ring_need(J, NA)) c = ring_need(J, NA);
    while (ring_isA(c - 1, NA) ? 2 * (ring_tile(c - 1, NA) - 3) + 1 > J - 1 : ring_tile(c - 1, NA) - NW > J - 1) --c;
    return c;
}
static_assert(JL >= 12 && JL <= 17, "behind the last barrier (the compute waves have read the operands of step JL + 1) every slot is free");
template <int NA, int P0, int P1>
__device__ __forceinline__ void ring_issue(const unsigned (&ev)[9], const void* xp, const void* wp, int kr, unsigned lw) {
    if constexpr (P0 < P1) {
        constexpr int t = ring_tile(P0, NA), sub = ring_sub(P0, NA);
        if constexpr (ring_isA(P0, NA)) {
            const int c = __builtin_amdgcn_readfirstlane(kr + t >= 10 ? kr + t - 10 : kr + t);
            GLS(ev[4 + sub], (const char*)xp + c * 128, lw, L_A + 2048 + (t % 3) * A_SLOT + sub * 4096);
        } else {
            const int c = __builtin_amdgcn_readfirstlane(2 * kr + t >= 20 ? 2 * kr + t - 20 : 2 * kr + t);
            GLS(ev[sub], (const char*)wp + (size_t)c * (32 * NQKV * 2), lw, L_W + (t % NW) * W_SLOT + sub * 4096);
        }
        ring_issue<NA, P0 + 1, P1>(ev, xp, wp, kr, lw);
    }
}
template <int NA, int J>
__device__ __forceinline__ void ring_run17(const unsigned (&ev)[9], const void* xp, const void* wp, int kr, unsigned lw, unsigned& c0, unsigned& r0) {
    if constexpr (J >= 0 || !L_FIRST) ring_issue<NA, (J < 0 ? 0 : ring_cum(J - 1, NA)), ring_cum(J, NA)>(ev, xp, wp, kr, lw);
    if constexpr (J < 16) {
        waitvm<ring_cum(J, NA) - ring_need(J, NA)>();
        BARRIER();
        if constexpr (J == 9) { c0 = (unsigned)__builtin_amdgcn_s_memtime(); r0 = (unsigned)__builtin_amdgcn_s_memrealtime(); }
        ring_run17<NA, J + 1>(ev, xp, wp, kr, lw, c0, r0);
    }
}
constexpr int ring_cum2(int J, int NA) {
    int c = ring_cum(JL, NA) + (J - JL - 1) * RING_T;
    if (c < ring_need(J, NA)) c = ring_need(J, NA);
    if (J == 17 || c > ring_total(NA)) c = ring_total(NA);
    return c;
}
template <int NA, int J>
__device__ __forceinline__ void ring_run(const unsigned (&ev)[9], const void* xp, const void* wp, int kr, unsigned lw, unsigned full, int lane) {
    if constexpr (J <= JL) {
        ring_issue<NA, (J < 0 ? 0 : ring_cum(J - 1, NA)), ring_cum(J, NA)>(ev, xp, wp, kr, lw);
        waitvm<ring_cum(J, NA) - ring_need(J, NA)>();
        BARRIER();
        ring_run<NA, J + 1>(ev, xp, wp, kr, lw, full, lane);
    } else if constexpr (J <= 17) {
        ring_issue<NA, (J == JL + 1 ? ring_cum(JL, NA) : ring_cum2(J - 1, NA)), ring_cum2(J, NA)>(ev, xp, wp, kr, lw);
        waitvm<ring_cum2(J, NA) - ring_need(J, NA)>();
        lds_add(full + (J - 13) * 4, lane);
        ring_run<NA, J + 1>(ev, xp, wp, kr, lw, full, lane);
    }
}

#define CONV_ASM \
    "s_mov_b32 m0, %[kvl]\n" \
    "v_mov_b32 v50, 1\n" \
    "global_load_lds_dwordx4 %[xo], %[wsp] offset:0 sc1\n" \
    "global_load_lds_dwordx4 %[xo], %[wsp] offset:1024 sc1\n" \
    "global_load_lds_dwordx4 %[xo], %[wsp] offset:2048 sc1\n" \
    "global_load_lds_dwordx4 %[xo], %[wsp] offset:3072 sc1\n" \
    "s_mov_b64 exec, 1\n" \
    "ds_add_u32 %[ctl], v50 offset:80\n" \
    "s_mov_b64 exec, -1\n" \
    "v_add_u32 v84, %[sx4], %[xp]\n" \
    "v_add_u32 v51, %[sh4], %[hp]\n" \
    "v_add_u32 v160, 0, %[xp]\n" \
    "v_add_u32 v161, 1152, %[xp]\n" \
    "v_add_u32 v85, 1152, v84\n" \
    "v_add_u32 v162, 2304, %[xp]\n" \
    "v_add_u32 v86, 2304, v84\n" \
    "v_add_u32 v163, 3456, %[xp]\n" \
    "v_add_u32 v87, 3456, v84\n" \
    "ds_read2_b32 v[88:89], v87 offset0:144 offset1:145\n" \
    "ds_read2_b32 v[90:91], v87 offset0:146 offset1:147\n" \
    "ds_read2_b32 v[92:93], v87 offset0:0 offset1:1\n" \
    "ds_read2_b32 v[94:95], v87 offset0:2 offset1:3\n" \
    "ds_read2_b32 v[96:97], v86 offset0:144 offset1:145\n" \
    "ds_read2_b32 v[98:99], v86 offset0:146 offset1:147\n" \
    "ds_read2_b32 v[100:101], v86 offset0:0 offset1:1\n" \
    "ds_read2_b32 v[102:103], v86 offset0:2 offset1:3\n" \
    "ds_read2_b32 v[104:105], v85 offset0:144 offset1:145\n" \
    "ds_read2_b32 v[106:107], v85 offset0:146 offset1:147\n" \
    "ds_read2_b32 v[108:109], v85 offset0:0 offset1:1\n" \
    "ds_read2_b32 v[110:111], v85 offset0:2 offset1:3\n" \
    "ds_read2_b32 v[112:113], v84 offset0:144 offset1:145\n" \
    "ds_read2_b32 v[114:115], v84 offset0:146 offset1:147\n" \
    "ds_read2_b32 v[116:117], v84 offset0:0 offset1:1\n" \
    "ds_read2_b32 v[118:119], v84 offset0:2 offset1:3\n" \
    "s_mov_b32 %[cnt], 0\n" \
    "Lsp_%=:\n" \
    "ds_read_b32 v50, %[ctl] offset:144\n" \
    "s_add_u32 %[cnt], %[cnt], 1\n" \
    "s_waitcnt lgkmcnt(0)\n" \
    "v_readfirstlane_b32 %[tmp], v50\n" \
    "s_cmp_ge_u32 %[cnt], %[cap]\n" \
    "s_cbranch_scc1 Lgo_%=\n" \
    "s_cmp_lt_u32 %[tmp], 4\n" \
    "s_cbranch_scc1 Lsp_%=\n" \
    "Lgo_%=:\n" \
    "ds_read_b128 v[148:151], v51 offset:3584\n" \
    "ds_read_b128 v[144:147], v51 offset:3072\n" \
    "ds_read_b128 v[140:143], v51 offset:2560\n" \
    "ds_read_b128 v[136:139], v51 offset:2048\n" \
    "ds_read_b128 v[132:135], v51 offset:1536\n" \
    "ds_read_b128 v[128:131], v51 offset:1024\n" \
    "ds_read_b128 v[124:127], v51 offset:512\n" \
    "ds_read_b128 v[120:123], v51 offset:0\n" \
    "s_waitcnt lgkmcnt(7)\n" \
    "v_mfma_f32_16x16x32_bf16 v[80:83], v[88:91], v[148:151], 0\n" \
    "ds_read_b128 v[148:151], v51 offset:3648\n" \
    "s_waitcnt lgkmcnt(7)\n" \
    "v_mfma_f32_16x16x32_bf16 v[76:79], v[88:91], v[144:147], 0\n" \
    "ds_read_b128 v[156:159], v51 offset:576\n" \
    "s_waitcnt lgkmcnt(7)\n" \
    "v_mfma_f32_16x16x32_bf16 v[72:75], v[88:91], v[140:143], 0\n" \
    "ds_read_b128 v[152:155], v51 offset:64\n" \
    "s_waitcnt lgkmcnt(7)\n" \
    "v_mfma_f32_16x16x32_bf16 v[68:71], v[88:91], v[136:139], 0\n" \
    "s_waitcnt lgkmcnt(6)\n" \
    "v_mfma_f32_16x16x32_bf16 v[64:67], v[88:91], v[132:135], 0\n" \
    "s_waitcnt lgkmcnt(5)\n" \
    "v_mfma_f32_16x16x32_bf16 v[60:63], v[88:91], v[128:131], 0\n" \
    "s_waitcnt lgkmcnt(4)\n" \
    "v_mfma_f32_16x16x32_bf16 v[56:59], v[88:91], v[124:127], 0\n" \
    "s_waitcnt lgkmcnt(3)\n" \
    "v_mfma_f32_16x16x32_bf16 v[52:55], v[88:91], v[120:123], 0\n" \
    "ds_read2_b32 v[88:89], v87 offset0:160 offset1:161\n" \
    "v_mfma_f32_16x16x32_bf16 v[80:83], v[92:95], v[144:147], v[80:83]\n" \
    "ds_read2_b32 v[90:91], v87 offset0:162 offset1:163\n" \
    "v_mfma_f32_16x16x32_bf16 v[76:79], v[92:95], v[140:143], v[76:79]\n" \
    "ds_read_b128 v[144:147], v51 offset:3136\n" \
    "v_mfma_f32_16x16x32_bf16 v[72:75], v[92:95], v[136:139], v[72:75]\n" \
    "v_mfma_f32_16x16x32_bf16 v[68:71], v[92:95], v[132:135], v[68:71]\n" \
    "v_mfma_f32_16x16x32_bf16 v[64:67], v[92:95], v[128:131], v[64:67]\n" \
    "v_mfma_f32_16x16x32_bf16 v[60:63], v[92:95], v[124:127], v[60:63]\n" \
    "v_mfma_f32_16x16x32_bf16 v[56:59], v[92:95], v[120:123], v[56:59]\n" \
    "ds_read2_b32 v[92:93], v87 offset0:16 offset1:17\n" \
    "v_mfma_f32_16x16x32_bf16 v[80:83], v[96:99], v[140:143], v[80:83]\n" \
    "ds_read_b128 v[140:143], v51 offset:2624\n" \
    "v_mfma_f32_16x16x32_bf16 v[76:79], v[96:99], v[136:139], v[76:79]\n" \
    "ds_read2_b32 v[94:95], v87 offset0:18 offset1:19\n" \
    "v_mfma_f32_16x16x32_bf16 v[72:75], v[96:99], v[132:135], v[72:75]\n" \
    "v_mfma_f32_16x16x32_bf16 v[68:71], v[96:99], v[128:131], v[68:71]\n" \
    "v_mfma_f32_16x16x32_bf16 v[64:67], v[96:99], v[124:127], v[64:67]\n" \
    "v_mfma_f32_16x16x32_bf16 v[60:63], v[96:99], v[120:123], v[60:63]\n" \
    "ds_read2_b32 v[96:97], v86 offset0:160 offset1:161\n" \
    "v_mfma_f32_16x16x32_bf16 v[80:83], v[100:103], v[136:139], v[80:83]\n" \
    "ds_read_b128 v[136:139], v51 offset:2112\n" \
    "v_mfma_f32_16x16x32_bf16 v[76:79], v[100:103], v[132:135], v[76:79]\n" \
    "ds_read2_b32 v[98:99], v86 offset0:162 offset1:163\n" \
    "v_mfma_f32_16x16x32_bf16 v[72:75], v[100:103], v[128:131], v[72:75]\n" \
    "v_mfma_f32_16x16x32_bf16 v[68:71], v[100:103], v[124:127], v[68:71]\n" \
    "v_mfma_f32_16x16x32_bf16 v[64:67], v[100:103], v[120:123], v[64:67]\n" \
    "ds_read2_b32 v[100:101], v86 offset0:16 offset1:17\n" \
    "v_mfma_f32_16x16x32_bf16 v[80:83], v[104:107], v[132:135], v[80:83]\n" \
    "ds_read_b128 v[132:135], v51 offset:1600\n" \
    "v_mfma_f32_16x16x32_bf16 v[76:79], v[104:107], v[128:131], v[76:79]\n" \
    "ds_read2_b32 v[102:103], v86 offset0:18 offset1:19\n" \
    "v_mfma_f32_16x16x32_bf16 v[72:75], v[104:107], v[124:127], v[72:75]\n" \
    "v_mfma_f32_16x16x32_bf16 v[68:71], v[104:107], v[120:123], v[68:71]\n" \
    "ds_read2_b32 v[104:105], v85 offset0:160 offset1:161\n" \
    "v_mfma_f32_16x16x32_bf16 v[80:83], v[108:111], v[128:131], v[80:83]\n" \
    "ds_read_b128 v[128:131], v51 offset:1088\n" \
    "v_mfma_f32_16x16x32_bf16 v[76:79], v[108:111], v[124:127], v[76:79]\n" \
    "ds_read2_b32 v[106:107], v85 offset0:162 offset1:163\n" \
    "v_mfma_f32_16x16x32_bf16 v[72:75], v[108:111], v[120:123], v[72:75]\n" \
    "ds_read2_b32 v[108:109], v85 offset0:16 offset1:17\n" \
    "v_mfma_f32_16x16x32_bf16 v[80:83], v[112:115], v[124:127], v[80:83]\n" \
    "ds_read2_b32 v[110:111], v85 offset0:18 offset1:19\n" \
    "v_mfma_f32_16x16x32_bf16 v[76:79], v[112:115], v[120:123], v[76:79]\n" \
    "ds_read2_b32 v[112:113], v84 offset0:160 offset1:161\n" \
    "v_mfma_f32_16x16x32_bf16 v[80:83], v[116:119], v[120:123], v[80:83]\n" \
    "ds_read2_b32 v[114:115], v84 offset0:162 offset1:163\n" \
    "s_waitcnt lgkmcnt(15)\n" \
    "v_mfma_f32_16x16x32_bf16 v[80:83], v[88:91], v[148:151], v[80:83]\n" \
    "ds_read2_b32 v[116:117], v84 offset0:16 offset1:17\n" \
    "v_mfma_f32_16x16x32_bf16 v[76:79], v[88:91], v[144:147], v[76:79]\n" \
    "ds_read2_b32 v[118:119], v84 offset0:18 offset1:19\n" \
    "s_waitcnt lgkmcnt(15)\n" \
    "v_mfma_f32_16x16x32_bf16 v[72:75], v[88:91], v[140:143], v[72:75]\n" \
    "s_waitcnt vmcnt(0)\n" \
    "ds_read_b128 v[148:151], %[hp] offset:3584\n" \
    "s_waitcnt lgkmcnt(14)\n" \
    "v_mfma_f32_16x16x32_bf16 v[68:71], v[88:91], v[136:139], v[68:71]\n" \
    "ds_read_b128 v[124:127], %[hp] offset:512\n" \
    "s_waitcnt lgkmcnt(12)\n" \
    "v_mfma_f32_16x16x32_bf16 v[64:67], v[88:91], v[132:135], v[64:67]\n" \
    "ds_read_b128 v[120:123], %[hp] offset:0\n" \
    "s_waitcnt lgkmcnt(10)\n" \
    "v_mfma_f32_16x16x32_bf16 v[60:63], v[88:91], v[128:131], v[60:63]\n" \
    "v_mfma_f32_16x16x32_bf16 v[56:59], v[88:91], v[156:159], v[56:59]\n" \
    "v_mfma_f32_16x16x32_bf16 v[52:55], v[88:91], v[152:155], v[52:55]\n" \
    "ds_read2_b32 v[88:89], v163 offset0:144 offset1:145\n" \
    "v_mfma_f32_16x16x32_bf16 v[80:83], v[92:95], v[144:147], v[80:83]\n" \
    "ds_read2_b32 v[90:91], v163 offset0:146 offset1:147\n" \
    "v_mfma_f32_16x16x32_bf16 v[76:79], v[92:95], v[140:143], v[76:79]\n" \
    "ds_read_b128 v[144:147], %[hp] offset:3072\n" \
    "v_mfma_f32_16x16x32_bf16 v[72:75], v[92:95], v[136:139], v[72:75]\n" \
    "v_mfma_f32_16x16x32_bf16 v[68:71], v[92:95], v[132:135], v[68:71]\n" \
    "ds_write_b128 %[scr], v[52:55] offset:0\n" \
    "v_mfma_f32_16x16x32_bf16 v[64:67], v[92:95], v[128:131], v[64:67]\n" \
    "ds_read_b64 v[86:87], %[kvr] offset:0\n" \
    "v_mfma_f32_16x16x32_bf16 v[60:63], v[92:95], v[156:159], v[60:63]\n" \
    "v_mfma_f32_16x16x32_bf16 v[56:59], v[92:95], v[152:155], v[56:59]\n" \
    "ds_read2_b32 v[92:93], v163 offset0:0 offset1:1\n" \
    "v_mfma_f32_16x16x32_bf16 v[80:83], v[96:99], v[140:143], v[80:83]\n" \
    "ds_read_b128 v[140:143], %[hp] offset:2560\n" \
    "v_mfma_f32_16x16x32_bf16 v[76:79], v[96:99], v[136:139], v[76:79]\n" \
    "ds_read2_b32 v[94:95], v163 offset0:2 offset1:3\n" \
    "v_mfma_f32_16x16x32_bf16 v[72:75], v[96:99], v[132:135], v[72:75]\n" \
    "v_mfma_f32_16x16x32_bf16 v[68:71], v[96:99], v[128:131], v[68:71]\n" \
    "ds_write_b128 %[scr], v[56:59] offset:1024\n" \
    "v_mfma_f32_16x16x32_bf16 v[64:67], v[96:99], v[156:159], v[64:67]\n" \
    "v_mfma_f32_16x16x32_bf16 v[60:63], v[96:99], v[152:155], v[60:63]\n" \
    "ds_read2_b32 v[96:97], v162 offset0:144 offset1:145\n" \
    "v_mfma_f32_16x16x32_bf16 v[80:83], v[100:103], v[136:139], v[80:83]\n" \
    "ds_read_b128 v[136:139], %[hp] offset:2048\n" \
    "v_mfma_f32_16x16x32_bf16 v[76:79], v[100:103], v[132:135], v[76:79]\n" \
    "ds_read2_b32 v[98:99], v162 offset0:146 offset1:147\n" \
    "v_mfma_f32_16x16x32_bf16 v[72:75], v[100:103], v[128:131], v[72:75]\n" \
    "v_mfma_f32_16x16x32_bf16 v[68:71], v[100:103], v[156:159], v[68:71]\n" \
    "s_waitcnt lgkmcnt(7)\n" \
    "v_lshlrev_b32 v84, 16, v86\n" \
    "v_mfma_f32_16x16x32_bf16 v[64:67], v[100:103], v[152:155], v[64:67]\n" \
    "ds_read2_b32 v[100:101], v162 offset0:0 offset1:1\n" \
    "v_mfma_f32_16x16x32_bf16 v[80:83], v[104:107], v[132:135], v[80:83]\n" \
    "ds_read_b128 v[132:135], %[hp] offset:1536\n" \
    "v_and_b32 v85, 0xffff0000, v86\n" \
    "v_mfma_f32_16x16x32_bf16 v[76:79], v[104:107], v[128:131], v[76:79]\n" \
    "ds_read2_b32 v[102:103], v162 offset0:2 offset1:3\n" \
    "v_mfma_f32_16x16x32_bf16 v[72:75], v[104:107], v[156:159], v[72:75]\n" \
    "v_lshlrev_b32 v86, 16, v87\n" \
    "v_mfma_f32_16x16x32_bf16 v[68:71], v[104:107], v[152:155], v[68:71]\n" \
    "ds_read2_b32 v[104:105], v161 offset0:144 offset1:145\n" \
    "v_mfma_f32_16x16x32_bf16 v[80:83], v[108:111], v[128:131], v[80:83]\n" \
    "ds_read_b128 v[128:131], %[hp] offset:1024\n" \
    "v_and_b32 v87, 0xffff0000, v87\n" \
    "v_mfma_f32_16x16x32_bf16 v[76:79], v[108:111], v[156:159], v[76:79]\n" \
    "ds_read2_b32 v[106:107], v161 offset0:146 offset1:147\n" \
    "v_mfma_f32_16x16x32_bf16 v[72:75], v[108:111], v[152:155], v[72:75]\n" \
    "ds_read2_b32 v[108:109], v161 offset0:0 offset1:1\n" \
    "v_mfma_f32_16x16x32_bf16 v[80:83], v[112:115], v[156:159], v[80:83]\n" \
    "ds_read2_b32 v[110:111], v161 offset0:2 offset1:3\n" \
    "v_mfma_f32_16x16x32_bf16 v[76:79], v[112:115], v[152:155], v[76:79]\n" \
    "ds_read2_b32 v[112:113], v160 offset0:144 offset1:145\n" \
    "v_mfma_f32_16x16x32_bf16 v[80:83], v[116:119], v[152:155], v[80:83]\n" \
    "ds_read2_b32 v[114:115], v160 offset0:146 offset1:147\n" \
    "v_mfma_f32_16x16x32_bf16 v[44:47], v[88:91], v[148:151], 0\n" \
    "ds_read2_b32 v[116:117], v160 offset0:0 offset1:1\n" \
    "v_mfma_f32_16x16x32_bf16 v[40:43], v[88:91], v[144:147], 0\n" \
    "ds_read2_b32 v[118:119], v160 offset0:2 offset1:3\n" \
    "s_waitcnt lgkmcnt(15)\n" \
    "v_mfma_f32_16x16x32_bf16 v[36:39], v[88:91], v[140:143], 0\n" \
    "ds_read_b128 v[148:151], %[hp] offset:3648\n" \
    "s_waitcnt lgkmcnt(14)\n" \
    "v_mfma_f32_16x16x32_bf16 v[32:35], v[88:91], v[136:139], 0\n" \
    "ds_read_b128 v[156:159], %[hp] offset:576\n" \
    "s_waitcnt lgkmcnt(12)\n" \
    "v_mfma_f32_16x16x32_bf16 v[28:31], v[88:91], v[132:135], 0\n" \
    "ds_read_b128 v[152:155], %[hp] offset:64\n" \
    "s_waitcnt lgkmcnt(10)\n" \
    "v_mfma_f32_16x16x32_bf16 v[24:27], v[88:91], v[128:131], 0\n" \
    "ds_write_b128 %[scr], v[60:63] offset:2048\n" \
    "v_mfma_f32_16x16x32_bf16 v[20:23], v[88:91], v[124:127], 0\n" \
    "v_mfma_f32_16x16x32_bf16 v[16:19], v[88:91], v[120:123], 0\n" \
    "ds_read2_b32 v[88:89], v163 offset0:160 offset1:161\n" \
    "v_mfma_f32_16x16x32_bf16 v[44:47], v[92:95], v[144:147], v[44:47]\n" \
    "ds_read2_b32 v[90:91], v163 offset0:162 offset1:163\n" \
    "v_mfma_f32_16x16x32_bf16 v[40:43], v[92:95], v[140:143], v[40:43]\n" \
    "ds_read_b128 v[144:147], %[hp] offset:3136\n" \
    "v_mfma_f32_16x16x32_bf16 v[36:39], v[92:95], v[136:139], v[36:39]\n" \
    "v_fmac_f32 v16, %[dd], v84\n" \
    "v_fmac_f32 v17, %[dd], v85\n" \
    "v_mfma_f32_16x16x32_bf16 v[32:35], v[92:95], v[132:135], v[32:35]\n" \
    "v_fmac_f32 v18, %[dd], v86\n" \
    "v_fmac_f32 v19, %[dd], v87\n" \
    "v_mfma_f32_16x16x32_bf16 v[28:31], v[92:95], v[128:131], v[28:31]\n" \
    "ds_read_b64 v[86:87], %[kvr] offset:512\n" \
    "v_mfma_f32_16x16x32_bf16 v[24:27], v[92:95], v[124:127], v[24:27]\n" \
    "ds_write_b128 %[scr], v[64:67] offset:3072\n" \
    "v_mfma_f32_16x16x32_bf16 v[20:23], v[92:95], v[120:123], v[20:23]\n" \
    "ds_read2_b32 v[92:93], v163 offset0:16 offset1:17\n" \
    "v_mfma_f32_16x16x32_bf16 v[44:47], v[96:99], v[140:143], v[44:47]\n" \
    "ds_read_b128 v[140:143], %[hp] offset:2624\n" \
    "v_mfma_f32_16x16x32_bf16 v[40:43], v[96:99], v[136:139], v[40:43]\n" \
    "ds_read2_b32 v[94:95], v163 offset0:18 offset1:19\n" \
    "v_mfma_f32_16x16x32_bf16 v[36:39], v[96:99], v[132:135], v[36:39]\n" \
    "ds_write_b128 %[scr], v[68:71] offset:4096\n" \
    "v_mfma_f32_16x16x32_bf16 v[32:35], v[96:99], v[128:131], v[32:35]\n" \
    "v_mfma_f32_16x16x32_bf16 v[28:31], v[96:99], v[124:127], v[28:31]\n" \
    "v_mfma_f32_16x16x32_bf16 v[24:27], v[96:99], v[120:123], v[24:27]\n" \
    "ds_read2_b32 v[96:97], v162 offset0:160 offset1:161\n" \
    "v_mfma_f32_16x16x32_bf16 v[44:47], v[100:103], v[136:139], v[44:47]\n" \
    "ds_read_b128 v[136:139], %[hp] offset:2112\n" \
    "v_mfma_f32_16x16x32_bf16 v[40:43], v[100:103], v[132:135], v[40:43]\n" \
    "ds_read2_b32 v[98:99], v162 offset0:162 offset1:163\n" \
    "v_mfma_f32_16x16x32_bf16 v[36:39], v[100:103], v[128:131], v[36:39]\n" \
    "ds_write_b128 %[scr], v[72:75] offset:5120\n" \
    "v_mfma_f32_16x16x32_bf16 v[32:35], v[100:103], v[124:127], v[32:35]\n" \
    "s_waitcnt lgkmcnt(9)\n" \
    "v_lshlrev_b32 v84, 16, v86\n" \
    "v_mfma_f32_16x16x32_bf16 v[28:31], v[100:103], v[120:123], v[28:31]\n" \
    "ds_read2_b32 v[100:101], v162 offset0:16 offset1:17\n" \
    "v_mfma_f32_16x16x32_bf16 v[44:47], v[104:107], v[132:135], v[44:47]\n" \
    "ds_read_b128 v[132:135], %[hp] offset:1600\n" \
    "v_and_b32 v85, 0xffff0000, v86\n" \
    "v_mfma_f32_16x16x32_bf16 v[40:43], v[104:107], v[128:131], v[40:43]\n" \
    "ds_read2_b32 v[102:103], v162 offset0:18 offset1:19\n" \
    "v_mfma_f32_16x16x32_bf16 v[36:39], v[104:107], v[124:127], v[36:39]\n" \
    "v_lshlrev_b32 v86, 16, v87\n" \
    "v_mfma_f32_16x16x32_bf16 v[32:35], v[104:107], v[120:123], v[32:35]\n" \
    "ds_read2_b32 v[104:105], v161 offset0:160 offset1:161\n" \
    "v_mfma_f32_16x16x32_bf16 v[44:47], v[108:111], v[128:131], v[44:47]\n" \
    "ds_read_b128 v[128:131], %[hp] offset:1088\n" \
    "v_and_b32 v87, 0xffff0000, v87\n" \
    "v_mfma_f32_16x16x32_bf16 v[40:43], v[108:111], v[124:127], v[40:43]\n" \
    "ds_read2_b32 v[106:107], v161 offset0:162 offset1:163\n" \
    "v_mfma_f32_16x16x32_bf16 v[36:39], v[108:111], v[120:123], v[36:39]\n" \
    "ds_read2_b32 v[108:109], v161 offset0:16 offset1:17\n" \
    "v_mfma_f32_16x16x32_bf16 v[44:47], v[112:115], v[124:127], v[44:47]\n" \
    "ds_read2_b32 v[110:111], v161 offset0:18 offset1:19\n" \
    "v_mfma_f32_16x16x32_bf16 v[40:43], v[112:115], v[120:123], v[40:43]\n" \
    "ds_read2_b32 v[112:113], v160 offset0:160 offset1:161\n" \
    "v_mfma_f32_16x16x32_bf16 v[44:47], v[116:119], v[120:123], v[44:47]\n" \
    "ds_read2_b32 v[114:115], v160 offset0:162 offset1:163\n" \
    "v_mfma_f32_16x16x32_bf16 v[44:47], v[88:91], v[148:151], v[44:47]\n" \
    "ds_read2_b32 v[116:117], v160 offset0:16 offset1:17\n" \
    "v_mfma_f32_16x16x32_bf16 v[40:43], v[88:91], v[144:147], v[40:43]\n" \
    "ds_read2_b32 v[118:119], v160 offset0:18 offset1:19\n" \
    "s_waitcnt lgkmcnt(15)\n" \
    "v_mfma_f32_16x16x32_bf16 v[36:39], v[88:91], v[140:143], v[36:39]\n" \
    "ds_read_b128 v[148:151], %[hp] offset:3712\n" \
    "s_waitcnt lgkmcnt(15)\n" \
    "v_mfma_f32_16x16x32_bf16 v[32:35], v[88:91], v[136:139], v[32:35]\n" \
    "ds_read_b128 v[124:127], %[hp] offset:640\n" \
    "s_waitcnt lgkmcnt(12)\n" \
    "v_mfma_f32_16x16x32_bf16 v[28:31], v[88:91], v[132:135], v[28:31]\n" \
    "ds_read_b128 v[120:123], %[hp] offset:128\n" \
    "s_waitcnt lgkmcnt(10)\n" \
    "v_mfma_f32_16x16x32_bf16 v[24:27], v[88:91], v[128:131], v[24:27]\n" \
    "ds_write_b128 %[scr], v[76:79] offset:6144\n" \
    "v_mfma_f32_16x16x32_bf16 v[20:23], v[88:91], v[156:159], v[20:23]\n" \
    "v_mfma_f32_16x16x32_bf16 v[16:19], v[88:91], v[152:155], v[16:19]\n" \
    "ds_read2_b32 v[88:89], v163 offset0:176 offset1:177\n" \
    "v_mfma_f32_16x16x32_bf16 v[44:47], v[92:95], v[144:147], v[44:47]\n" \
    "ds_read2_b32 v[90:91], v163 offset0:178 offset1:179\n" \
    "v_mfma_f32_16x16x32_bf16 v[40:43], v[92:95], v[140:143], v[40:43]\n" \
    "ds_read_b128 v[144:147], %[hp] offset:3200\n" \
    "v_fmac_f32 v20, %[dd], v84\n" \
    "v_fmac_f32 v21, %[dd], v85\n" \
    "v_mfma_f32_16x16x32_bf16 v[36:39], v[92:95], v[136:139], v[36:39]\n" \
    "v_fmac_f32 v22, %[dd], v86\n" \
    "v_fmac_f32 v23, %[dd], v87\n" \
    "v_mfma_f32_16x16x32_bf16 v[32:35], v[92:95], v[132:135], v[32:35]\n" \
    "ds_write_b128 %[scr], v[80:83] offset:7168\n" \
    "v_mov_b32 v50, 1\n" \
    "s_mov_b64 exec, 1\n" \
    "ds_add_u32 %[ctl], v50 offset:84\n" \
    "s_mov_b64 exec, -1\n" \
    "v_mfma_f32_16x16x32_bf16 v[28:31], v[92:95], v[128:131], v[28:31]\n" \
    "ds_read_b64 v[86:87], %[kvr] offset:1024\n" \
    "v_mfma_f32_16x16x32_bf16 v[24:27], v[92:95], v[156:159], v[24:27]\n" \
    "v_mfma_f32_16x16x32_bf16 v[20:23], v[92:95], v[152:155], v[20:23]\n" \
    "ds_read2_b32 v[92:93], v163 offset0:32 offset1:33\n" \
    "v_mfma_f32_16x16x32_bf16 v[44:47], v[96:99], v[140:143], v[44:47]\n" \
    "ds_read_b128 v[140:143], %[hp] offset:2688\n" \
    "v_mfma_f32_16x16x32_bf16 v[40:43], v[96:99], v[136:139], v[40:43]\n" \
    "ds_read2_b32 v[94:95], v163 offset0:34 offset1:35\n" \
    "v_mfma_f32_16x16x32_bf16 v[36:39], v[96:99], v[132:135], v[36:39]\n" \
    "v_mfma_f32_16x16x32_bf16 v[32:35], v[96:99], v[128:131], v[32:35]\n" \
    "v_mfma_f32_16x16x32_bf16 v[28:31], v[96:99], v[156:159], v[28:31]\n" \
    "v_mfma_f32_16x16x32_bf16 v[24:27], v[96:99], v[152:155], v[24:27]\n" \
    "ds_read2_b32 v[96:97], v162 offset0:176 offset1:177\n" \
    "v_mfma_f32_16x16x32_bf16 v[44:47], v[100:103], v[136:139], v[44:47]\n" \
    "ds_read_b128 v[136:139], %[hp] offset:2176\n" \
    "v_mfma_f32_16x16x32_bf16 v[40:43], v[100:103], v[132:135], v[40:43]\n" \
    "ds_read2_b32 v[98:99], v162 offset0:178 offset1:179\n" \
    "v_mfma_f32_16x16x32_bf16 v[36:39], v[100:103], v[128:131], v[36:39]\n" \
    "v_mfma_f32_16x16x32_bf16 v[32:35], v[100:103], v[156:159], v[32:35]\n" \
    "s_waitcnt lgkmcnt(6)\n" \
    "v_lshlrev_b32 v84, 16, v86\n" \
    "v_mfma_f32_16x16x32_bf16 v[28:31], v[100:103], v[152:155], v[28:31]\n" \
    "ds_read2_b32 v[100:101], v162 offset0:32 offset1:33\n" \
    "v_mfma_f32_16x16x32_bf16 v[44:47], v[104:107], v[132:135], v[44:47]\n" \
    "ds_read_b128 v[132:135], %[hp] offset:1664\n" \
    "v_and_b32 v85, 0xffff0000, v86\n" \
    "v_mfma_f32_16x16x32_bf16 v[40:43], v[104:107], v[128:131], v[40:43]\n" \
    "ds_read2_b32 v[102:103], v162 offset0:34 offset1:35\n" \
    "v_mfma_f32_16x16x32_bf16 v[36:39], v[104:107], v[156:159], v[36:39]\n" \
    "v_lshlrev_b32 v86, 16, v87\n" \
    "v_mfma_f32_16x16x32_bf16 v[32:35], v[104:107], v[152:155], v[32:35]\n" \
    "ds_read2_b32 v[104:105], v161 offset0:176 offset1:177\n" \
    "v_mfma_f32_16x16x32_bf16 v[44:47], v[108:111], v[128:131], v[44:47]\n" \
    "ds_read_b128 v[128:131], %[hp] offset:1152\n" \
    "v_and_b32 v87, 0xffff0000, v87\n" \
    "v_mfma_f32_16x16x32_bf16 v[40:43], v[108:111], v[156:159], v[40:43]\n" \
    "ds_read2_b32 v[106:107], v161 offset0:178 offset1:179\n" \
    "v_mfma_f32_16x16x32_bf16 v[36:39], v[108:111], v[152:155], v[36:39]\n" \
    "ds_read2_b32 v[108:109], v161 offset0:32 offset1:33\n" \
    "v_mfma_f32_16x16x32_bf16 v[44:47], v[112:115], v[156:159], v[44:47]\n" \
    "ds_read2_b32 v[110:111], v161 offset0:34 offset1:35\n" \
    "v_mfma_f32_16x16x32_bf16 v[40:43], v[112:115], v[152:155], v[40:43]\n" \
    "ds_read2_b32 v[112:113], v160 offset0:176 offset1:177\n" \
    "v_mfma_f32_16x16x32_bf16 v[44:47], v[116:119], v[152:155], v[44:47]\n" \
    "ds_read2_b32 v[114:115], v160 offset0:178 offset1:179\n" \
    "v_mfma_f32_16x16x32_bf16 v[44:47], v[88:91], v[148:151], v[44:47]\n" \
    "ds_read2_b32 v[116:117], v160 offset0:32 offset1:33\n" \
    "v_mfma_f32_16x16x32_bf16 v[40:43], v[88:91], v[144:147], v[40:43]\n" \
    "ds_read2_b32 v[118:119], v160 offset0:34 offset1:35\n" \
    "s_waitcnt lgkmcnt(15)\n" \
    "v_mfma_f32_16x16x32_bf16 v[36:39], v[88:91], v[140:143], v[36:39]\n" \
    "ds_read_b128 v[148:151], %[hp] offset:3776\n" \
    "s_waitcnt lgkmcnt(14)\n" \
    "v_mfma_f32_16x16x32_bf16 v[32:35], v[88:91], v[136:139], v[32:35]\n" \
    "ds_read_b128 v[156:159], %[hp] offset:704\n" \
    "s_waitcnt lgkmcnt(12)\n" \
    "v_mfma_f32_16x16x32_bf16 v[28:31], v[88:91], v[132:135], v[28:31]\n" \
    "ds_read_b128 v[152:155], %[hp] offset:192\n" \
    "s_waitcnt lgkmcnt(10)\n" \
    "v_mfma_f32_16x16x32_bf16 v[24:27], v[88:91], v[128:131], v[24:27]\n" \
    "v_mfma_f32_16x16x32_bf16 v[20:23], v[88:91], v[124:127], v[20:23]\n" \
    "v_mfma_f32_16x16x32_bf16 v[16:19], v[88:91], v[120:123], v[16:19]\n" \
    "ds_read2_b32 v[88:89], v163 offset0:192 offset1:193\n" \
    "v_mfma_f32_16x16x32_bf16 v[44:47], v[92:95], v[144:147], v[44:47]\n" \
    "ds_read2_b32 v[90:91], v163 offset0:194 offset1:195\n" \
    "v_fmac_f32 v24, %[dd], v84\n" \
    "v_fmac_f32 v25, %[dd], v85\n" \
    "v_mfma_f32_16x16x32_bf16 v[40:43], v[92:95], v[140:143], v[40:43]\n" \
    "ds_read_b128 v[144:147], %[hp] offset:3264\n" \
    "v_fmac_f32 v26, %[dd], v86\n" \
    "v_fmac_f32 v27, %[dd], v87\n" \
    "v_mfma_f32_16x16x32_bf16 v[36:39], v[92:95], v[136:139], v[36:39]\n" \
    "v_mfma_f32_16x16x32_bf16 v[32:35], v[92:95], v[132:135], v[32:35]\n" \
    "v_mfma_f32_16x16x32_bf16 v[28:31], v[92:95], v[128:131], v[28:31]\n" \
    "ds_read_b64 v[86:87], %[kvr] offset:1536\n" \
    "v_mfma_f32_16x16x32_bf16 v[24:27], v[92:95], v[124:127], v[24:27]\n" \
    "v_mfma_f32_16x16x32_bf16 v[20:23], v[92:95], v[120:123], v[20:23]\n" \
    "ds_read2_b32 v[92:93], v163 offset0:48 offset1:49\n" \
    "v_mfma_f32_16x16x32_bf16 v[44:47], v[96:99], v[140:143], v[44:47]\n" \
    "ds_read_b128 v[140:143], %[hp] offset:2752\n" \
    "v_mfma_f32_16x16x32_bf16 v[40:43], v[96:99], v[136:139], v[40:43]\n" \
    "ds_read2_b32 v[94:95], v163 offset0:50 offset1:51\n" \
    "v_mfma_f32_16x16x32_bf16 v[36:39], v[96:99], v[132:135], v[36:39]\n" \
    "v_mfma_f32_16x16x32_bf16 v[32:35], v[96:99], v[128:131], v[32:35]\n" \
    "v_mfma_f32_16x16x32_bf16 v[28:31], v[96:99], v[124:127], v[28:31]\n" \
    "v_mfma_f32_16x16x32_bf16 v[24:27], v[96:99], v[120:123], v[24:27]\n" \
    "ds_read2_b32 v[96:97], v162 offset0:192 offset1:193\n" \
    "v_mfma_f32_16x16x32_bf16 v[44:47], v[100:103], v[136:139], v[44:47]\n" \
    "ds_read_b128 v[136:139], %[hp] offset:2240\n" \
    "v_mfma_f32_16x16x32_bf16 v[40:43], v[100:103], v[132:135], v[40:43]\n" \
    "ds_read2_b32 v[98:99], v162 offset0:194 offset1:195\n" \
    "v_mfma_f32_16x16x32_bf16 v[36:39], v[100:103], v[128:131], v[36:39]\n" \
    "v_mfma_f32_16x16x32_bf16 v[32:35], v[100:103], v[124:127], v[32:35]\n" \
    "s_waitcnt lgkmcnt(6)\n" \
    "v_lshlrev_b32 v84, 16, v86\n" \
    "v_mfma_f32_16x16x32_bf16 v[28:31], v[100:103], v[120:123], v[28:31]\n" \
    "ds_read2_b32 v[100:101], v162 offset0:48 offset1:49\n" \
    "v_mfma_f32_16x16x32_bf16 v[44:47], v[104:107], v[132:135], v[44:47]\n" \
    "ds_read_b128 v[132:135], %[hp] offset:1728\n" \
    "v_and_b32 v85, 0xffff0000, v86\n" \
    "v_mfma_f32_16x16x32_bf16 v[40:43], v[104:107], v[128:131], v[40:43]\n" \
    "ds_read2_b32 v[102:103], v162 offset0:50 offset1:51\n" \
    "v_mfma_f32_16x16x32_bf16 v[36:39], v[104:107], v[124:127], v[36:39]\n" \
    "v_lshlrev_b32 v86, 16, v87\n" \
    "v_mfma_f32_16x16x32_bf16 v[32:35], v[104:107], v[120:123], v[32:35]\n" \
    "ds_read2_b32 v[104:105], v161 offset0:192 offset1:193\n" \
    "v_mfma_f32_16x16x32_bf16 v[44:47], v[108:111], v[128:131], v[44:47]\n" \
    "ds_read_b128 v[128:131], %[hp] offset:1216\n" \
    "v_and_b32 v87, 0xffff0000, v87\n" \
    "v_mfma_f32_16x16x32_bf16 v[40:43], v[108:111], v[124:127], v[40:43]\n" \
    "ds_read2_b32 v[106:107], v161 offset0:194 offset1:195\n" \
    "v_mfma_f32_16x16x32_bf16 v[36:39], v[108:111], v[120:123], v[36:39]\n" \
    "ds_read2_b32 v[108:109], v161 offset0:48 offset1:49\n" \
    "v_mfma_f32_16x16x32_bf16 v[44:47], v[112:115], v[124:127], v[44:47]\n" \
    "ds_read2_b32 v[110:111], v161 offset0:50 offset1:51\n" \
    "v_mfma_f32_16x16x32_bf16 v[40:43], v[112:115], v[120:123], v[40:43]\n" \
    "ds_read2_b32 v[112:113], v160 offset0:192 offset1:193\n" \
    "v_mfma_f32_16x16x32_bf16 v[44:47], v[116:119], v[120:123], v[44:47]\n" \
    "ds_read2_b32 v[114:115], v160 offset0:194 offset1:195\n" \
    "v_mfma_f32_16x16x32_bf16 v[44:47], v[88:91], v[148:151], v[44:47]\n" \
    "ds_read2_b32 v[116:117], v160 offset0:48 offset1:49\n" \
    "v_mfma_f32_16x16x32_bf16 v[40:43], v[88:91], v[144:147], v[40:43]\n" \
    "ds_read2_b32 v[118:119], v160 offset0:50 offset1:51\n" \
    "s_waitcnt lgkmcnt(15)\n" \
    "v_mfma_f32_16x16x32_bf16 v[36:39], v[88:91], v[140:143], v[36:39]\n" \
    "ds_read_b128 v[148:151], %[hp] offset:3840\n" \
    "s_waitcnt lgkmcnt(14)\n" \
    "v_mfma_f32_16x16x32_bf16 v[32:35], v[88:91], v[136:139], v[32:35]\n" \
    "ds_read_b128 v[124:127], %[hp] offset:768\n" \
    "s_waitcnt lgkmcnt(12)\n" \
    "v_mfma_f32_16x16x32_bf16 v[28:31], v[88:91], v[132:135], v[28:31]\n" \
    "ds_read_b128 v[120:123], %[hp] offset:256\n" \
    "s_waitcnt lgkmcnt(10)\n" \
    "v_mfma_f32_16x16x32_bf16 v[24:27], v[88:91], v[128:131], v[24:27]\n" \
    "v_mfma_f32_16x16x32_bf16 v[20:23], v[88:91], v[156:159], v[20:23]\n" \
    "v_mfma_f32_16x16x32_bf16 v[16:19], v[88:91], v[152:155], v[16:19]\n" \
    "ds_read2_b32 v[88:89], v163 offset0:216 offset1:217\n" \
    "v_fmac_f32 v28, %[dd], v84\n" \
    "v_fmac_f32 v29, %[dd], v85\n" \
    "v_mfma_f32_16x16x32_bf16 v[44:47], v[92:95], v[144:147], v[44:47]\n" \
    "ds_read2_b32 v[90:91], v163 offset0:218 offset1:219\n" \
    "v_fmac_f32 v30, %[dd], v86\n" \
    "v_fmac_f32 v31, %[dd], v87\n" \
    "v_mfma_f32_16x16x32_bf16 v[40:43], v[92:95], v[140:143], v[40:43]\n" \
    "ds_read_b128 v[144:147], %[hp] offset:3328\n" \
    "v_mfma_f32_16x16x32_bf16 v[36:39], v[92:95], v[136:139], v[36:39]\n" \
    "v_mfma_f32_16x16x32_bf16 v[32:35], v[92:95], v[132:135], v[32:35]\n" \
    "v_mfma_f32_16x16x32_bf16 v[28:31], v[92:95], v[128:131], v[28:31]\n" \
    "ds_read_b64 v[86:87], %[kvr] offset:2048\n" \
    "v_mfma_f32_16x16x32_bf16 v[24:27], v[92:95], v[156:159], v[24:27]\n" \
    "v_mfma_f32_16x16x32_bf16 v[20:23], v[92:95], v[152:155], v[20:23]\n" \
    "ds_read2_b32 v[92:93], v163 offset0:72 offset1:73\n" \
    "v_mfma_f32_16x16x32_bf16 v[44:47], v[96:99], v[140:143], v[44:47]\n" \
    "ds_read_b128 v[140:143], %[hp] offset:2816\n" \
    "v_mfma_f32_16x16x32_bf16 v[40:43], v[96:99], v[136:139], v[40:43]\n" \
    "ds_read2_b32 v[94:95], v163 offset0:74 offset1:75\n" \
    "v_mfma_f32_16x16x32_bf16 v[36:39], v[96:99], v[132:135], v[36:39]\n" \
    "v_mfma_f32_16x16x32_bf16 v[32:35], v[96:99], v[128:131], v[32:35]\n" \
    "v_mfma_f32_16x16x32_bf16 v[28:31], v[96:99], v[156:159], v[28:31]\n" \
    "v_mfma_f32_16x16x32_bf16 v[24:27], v[96:99], v[152:155], v[24:27]\n" \
    "ds_read2_b32 v[96:97], v162 offset0:216 offset1:217\n" \
    "v_mfma_f32_16x16x32_bf16 v[44:47], v[100:103], v[136:139], v[44:47]\n" \
    "ds_read_b128 v[136:139], %[hp] offset:2304\n" \
    "v_mfma_f32_16x16x32_bf16 v[40:43], v[100:103], v[132:135], v[40:43]\n" \
    "ds_read2_b32 v[98:99], v162 offset0:218 offset1:219\n" \
    "v_mfma_f32_16x16x32_bf16 v[36:39], v[100:103], v[128:131], v[36:39]\n" \
    "v_mfma_f32_16x16x32_bf16 v[32:35], v[100:103], v[156:159], v[32:35]\n" \
    "s_waitcnt lgkmcnt(6)\n" \
    "v_lshlrev_b32 v84, 16, v86\n" \
    "v_mfma_f32_16x16x32_bf16 v[28:31], v[100:103], v[152:155], v[28:31]\n" \
    "ds_read2_b32 v[100:101], v162 offset0:72 offset1:73\n" \
    "v_mfma_f32_16x16x32_bf16 v[44:47], v[104:107], v[132:135], v[44:47]\n" \
    "ds_read_b128 v[132:135], %[hp] offset:1792\n" \
    "v_and_b32 v85, 0xffff0000, v86\n" \
    "v_mfma_f32_16x16x32_bf16 v[40:43], v[104:107], v[128:131], v[40:43]\n" \
    "ds_read2_b32 v[102:103], v162 offset0:74 offset1:75\n" \
    "v_mfma_f32_16x16x32_bf16 v[36:39], v[104:107], v[156:159], v[36:39]\n" \
    "v_lshlrev_b32 v86, 16, v87\n" \
    "v_mfma_f32_16x16x32_bf16 v[32:35], v[104:107], v[152:155], v[32:35]\n" \
    "ds_read2_b32 v[104:105], v161 offset0:216 offset1:217\n" \
    "v_mfma_f32_16x16x32_bf16 v[44:47], v[108:111], v[128:131], v[44:47]\n" \
    "ds_read_b128 v[128:131], %[hp] offset:1280\n" \
    "v_and_b32 v87, 0xffff0000, v87\n" \
    "v_mfma_f32_16x16x32_bf16 v[40:43], v[108:111], v[156:159], v[40:43]\n" \
    "ds_read2_b32 v[106:107], v161 offset0:218 offset1:219\n" \
    "v_mfma_f32_16x16x32_bf16 v[36:39], v[108:111], v[152:155], v[36:39]\n" \
    "ds_read2_b32 v[108:109], v161 offset0:72 offset1:73\n" \
    "v_mfma_f32_16x16x32_bf16 v[44:47], v[112:115], v[156:159], v[44:47]\n" \
    "ds_read2_b32 v[110:111], v161 offset0:74 offset1:75\n" \
    "v_mfma_f32_16x16x32_bf16 v[40:43], v[112:115], v[152:155], v[40:43]\n" \
    "ds_read2_b32 v[112:113], v160 offset0:216 offset1:217\n" \
    "v_mfma_f32_16x16x32_bf16 v[44:47], v[116:119], v[152:155], v[44:47]\n" \
    "ds_read2_b32 v[114:115], v160 offset0:218 offset1:219\n" \
    "v_add_u32 v160, 288, v160\n" \
    "v_add_u32 v161, 288, v161\n" \
    "v_add_u32 v162, 288, v162\n" \
    "v_add_u32 v163, 288, v163\n" \
    "v_mfma_f32_16x16x32_bf16 v[44:47], v[88:91], v[148:151], v[44:47]\n" \
    "ds_read2_b32 v[116:117], v160 offset0:0 offset1:1\n" \
    "v_mfma_f32_16x16x32_bf16 v[40:43], v[88:91], v[144:147], v[40:43]\n" \
    "ds_read2_b32 v[118:119], v160 offset0:2 offset1:3\n" \
    "s_waitcnt lgkmcnt(15)\n" \
    "v_mfma_f32_16x16x32_bf16 v[36:39], v[88:91], v[140:143], v[36:39]\n" \
    "ds_read_b128 v[148:151], %[hp] offset:3904\n" \
    "s_waitcnt lgkmcnt(14)\n" \
    "v_mfma_f32_16x16x32_bf16 v[32:35], v[88:91], v[136:139], v[32:35]\n" \
    "ds_read_b128 v[156:159], %[hp] offset:832\n" \
    "s_waitcnt lgkmcnt(12)\n" \
    "v_mfma_f32_16x16x32_bf16 v[28:31], v[88:91], v[132:135], v[28:31]\n" \
    "ds_read_b128 v[152:155], %[hp] offset:320\n" \
    "s_waitcnt lgkmcnt(10)\n" \
    "v_mfma_f32_16x16x32_bf16 v[24:27], v[88:91], v[128:131], v[24:27]\n" \
    "v_mfma_f32_16x16x32_bf16 v[20:23], v[88:91], v[124:127], v[20:23]\n" \
    "v_fmac_f32 v32, %[dd], v84\n" \
    "v_fmac_f32 v33, %[dd], v85\n" \
    "v_mfma_f32_16x16x32_bf16 v[16:19], v[88:91], v[120:123], v[16:19]\n" \
    "ds_read2_b32 v[88:89], v163 offset0:160 offset1:161\n" \
    "v_fmac_f32 v34, %[dd], v86\n" \
    "v_fmac_f32 v35, %[dd], v87\n" \
    "v_mfma_f32_16x16x32_bf16 v[44:47], v[92:95], v[144:147], v[44:47]\n" \
    "ds_read2_b32 v[90:91], v163 offset0:162 offset1:163\n" \
    "v_mfma_f32_16x16x32_bf16 v[40:43], v[92:95], v[140:143], v[40:43]\n" \
    "ds_read_b128 v[144:147], %[hp] offset:3392\n" \
    "v_mfma_f32_16x16x32_bf16 v[36:39], v[92:95], v[136:139], v[36:39]\n" \
    "v_mfma_f32_16x16x32_bf16 v[32:35], v[92:95], v[132:135], v[32:35]\n" \
    "v_mfma_f32_16x16x32_bf16 v[28:31], v[92:95], v[128:131], v[28:31]\n" \
    "ds_read_b64 v[86:87], %[kvr] offset:2560\n" \
    "v_mfma_f32_16x16x32_bf16 v[24:27], v[92:95], v[124:127], v[24:27]\n" \
    "v_mfma_f32_16x16x32_bf16 v[20:23], v[92:95], v[120:123], v[20:23]\n" \
    "ds_read2_b32 v[92:93], v163 offset0:16 offset1:17\n" \
    "v_mfma_f32_16x16x32_bf16 v[44:47], v[96:99], v[140:143], v[44:47]\n" \
    "ds_read_b128 v[140:143], %[hp] offset:2880\n" \
    "v_mfma_f32_16x16x32_bf16 v[40:43], v[96:99], v[136:139], v[40:43]\n" \
    "ds_read2_b32 v[94:95], v163 offset0:18 offset1:19\n" \
    "v_mfma_f32_16x16x32_bf16 v[36:39], v[96:99], v[132:135], v[36:39]\n" \
    "v_mfma_f32_16x16x32_bf16 v[32:35], v[96:99], v[128:131], v[32:35]\n" \
    "v_mfma_f32_16x16x32_bf16 v[28:31], v[96:99], v[124:127], v[28:31]\n" \
    "v_mfma_f32_16x16x32_bf16 v[24:27], v[96:99], v[120:123], v[24:27]\n" \
    "ds_read2_b32 v[96:97], v162 offset0:160 offset1:161\n" \
    "v_mfma_f32_16x16x32_bf16 v[44:47], v[100:103], v[136:139], v[44:47]\n" \
    "ds_read_b128 v[136:139], %[hp] offset:2368\n" \
    "v_mfma_f32_16x16x32_bf16 v[40:43], v[100:103], v[132:135], v[40:43]\n" \
    "ds_read2_b32 v[98:99], v162 offset0:162 offset1:163\n" \
    "v_mfma_f32_16x16x32_bf16 v[36:39], v[100:103], v[128:131], v[36:39]\n" \
    "v_mfma_f32_16x16x32_bf16 v[32:35], v[100:103], v[124:127], v[32:35]\n" \
    "s_waitcnt lgkmcnt(6)\n" \
    "v_lshlrev_b32 v84, 16, v86\n" \
    "v_mfma_f32_16x16x32_bf16 v[28:31], v[100:103], v[120:123], v[28:31]\n" \
    "ds_read2_b32 v[100:101], v162 offset0:16 offset1:17\n" \
    "v_mfma_f32_16x16x32_bf16 v[44:47], v[104:107], v[132:135], v[44:47]\n" \
    "ds_read_b128 v[132:135], %[hp] offset:1856\n" \
    "v_and_b32 v85, 0xffff0000, v86\n" \
    "v_mfma_f32_16x16x32_bf16 v[40:43], v[104:107], v[128:131], v[40:43]\n" \
    "ds_read2_b32 v[102:103], v162 offset0:18 offset1:19\n" \
    "v_mfma_f32_16x16x32_bf16 v[36:39], v[104:107], v[124:127], v[36:39]\n" \
    "v_lshlrev_b32 v86, 16, v87\n" \
    "v_mfma_f32_16x16x32_bf16 v[32:35], v[104:107], v[120:123], v[32:35]\n" \
    "ds_read2_b32 v[104:105], v161 offset0:160 offset1:161\n" \
    "v_mfma_f32_16x16x32_bf16 v[44:47], v[108:111], v[128:131], v[44:47]\n" \
    "ds_read_b128 v[128:131], %[hp] offset:1344\n" \
    "v_and_b32 v87, 0xffff0000, v87\n" \
    "v_mfma_f32_16x16x32_bf16 v[40:43], v[108:111], v[124:127], v[40:43]\n" \
    "ds_read2_b32 v[106:107], v161 offset0:162 offset1:163\n" \
    "v_mfma_f32_16x16x32_bf16 v[36:39], v[108:111], v[120:123], v[36:39]\n" \
    "ds_read2_b32 v[108:109], v161 offset0:16 offset1:17\n" \
    "v_mfma_f32_16x16x32_bf16 v[44:47], v[112:115], v[124:127], v[44:47]\n" \
    "ds_read2_b32 v[110:111], v161 offset0:18 offset1:19\n" \
    "v_mfma_f32_16x16x32_bf16 v[40:43], v[112:115], v[120:123], v[40:43]\n" \
    "ds_read2_b32 v[112:113], v160 offset0:160 offset1:161\n" \
    "v_mfma_f32_16x16x32_bf16 v[44:47], v[116:119], v[120:123], v[44:47]\n" \
    "ds_read2_b32 v[114:115], v160 offset0:162 offset1:163\n" \
    "v_mfma_f32_16x16x32_bf16 v[44:47], v[88:91], v[148:151], v[44:47]\n" \
    "ds_read2_b32 v[116:117], v160 offset0:16 offset1:17\n" \
    "v_mfma_f32_16x16x32_bf16 v[40:43], v[88:91], v[144:147], v[40:43]\n" \
    "ds_read2_b32 v[118:119], v160 offset0:18 offset1:19\n" \
    "s_waitcnt lgkmcnt(15)\n" \
    "v_mfma_f32_16x16x32_bf16 v[36:39], v[88:91], v[140:143], v[36:39]\n" \
    "ds_read_b128 v[148:151], %[hp] offset:3968\n" \
    "s_waitcnt lgkmcnt(14)\n" \
    "v_mfma_f32_16x16x32_bf16 v[32:35], v[88:91], v[136:139], v[32:35]\n" \
    "ds_read_b128 v[124:127], %[hp] offset:896\n" \
    "s_waitcnt lgkmcnt(12)\n" \
    "v_mfma_f32_16x16x32_bf16 v[28:31], v[88:91], v[132:135], v[28:31]\n" \
    "ds_read_b128 v[120:123], %[hp] offset:384\n" \
    "s_waitcnt lgkmcnt(10)\n" \
    "v_mfma_f32_16x16x32_bf16 v[24:27], v[88:91], v[128:131], v[24:27]\n" \
    "v_fmac_f32 v36, %[dd], v84\n" \
    "v_fmac_f32 v37, %[dd], v85\n" \
    "v_mfma_f32_16x16x32_bf16 v[20:23], v[88:91], v[156:159], v[20:23]\n" \
    "v_fmac_f32 v38, %[dd], v86\n" \
    "v_fmac_f32 v39, %[dd], v87\n" \
    "v_mfma_f32_16x16x32_bf16 v[16:19], v[88:91], v[152:155], v[16:19]\n" \
    "ds_read2_b32 v[88:89], v163 offset0:176 offset1:177\n" \
    "v_mfma_f32_16x16x32_bf16 v[44:47], v[92:95], v[144:147], v[44:47]\n" \
    "ds_read2_b32 v[90:91], v163 offset0:178 offset1:179\n" \
    "v_mfma_f32_16x16x32_bf16 v[40:43], v[92:95], v[140:143], v[40:43]\n" \
    "ds_read_b128 v[144:147], %[hp] offset:3456\n" \
    "v_mfma_f32_16x16x32_bf16 v[36:39], v[92:95], v[136:139], v[36:39]\n" \
    "v_mfma_f32_16x16x32_bf16 v[32:35], v[92:95], v[132:135], v[32:35]\n" \
    "v_mfma_f32_16x16x32_bf16 v[28:31], v[92:95], v[128:131], v[28:31]\n" \
    "ds_read_b64 v[86:87], %[kvr] offset:3072\n" \
    "v_mfma_f32_16x16x32_bf16 v[24:27], v[92:95], v[156:159], v[24:27]\n" \
    "v_mfma_f32_16x16x32_bf16 v[20:23], v[92:95], v[152:155], v[20:23]\n" \
    "ds_read2_b32 v[92:93], v163 offset0:32 offset1:33\n" \
    "v_mfma_f32_16x16x32_bf16 v[44:47], v[96:99], v[140:143], v[44:47]\n" \
    "ds_read_b128 v[140:143], %[hp] offset:2944\n" \
    "v_mfma_f32_16x16x32_bf16 v[40:43], v[96:99], v[136:139], v[40:43]\n" \
    "ds_read2_b32 v[94:95], v163 offset0:34 offset1:35\n" \
    "v_mfma_f32_16x16x32_bf16 v[36:39], v[96:99], v[132:135], v[36:39]\n" \
    "v_mfma_f32_16x16x32_bf16 v[32:35], v[96:99], v[128:131], v[32:35]\n" \
    "v_mfma_f32_16x16x32_bf16 v[28:31], v[96:99], v[156:159], v[28:31]\n" \
    "v_mfma_f32_16x16x32_bf16 v[24:27], v[96:99], v[152:155], v[24:27]\n" \
    "ds_read2_b32 v[96:97], v162 offset0:176 offset1:177\n" \
    "v_mfma_f32_16x16x32_bf16 v[44:47], v[100:103], v[136:139], v[44:47]\n" \
    "ds_read_b128 v[136:139], %[hp] offset:2432\n" \
    "v_mfma_f32_16x16x32_bf16 v[40:43], v[100:103], v[132:135], v[40:43]\n" \
    "ds_read2_b32 v[98:99], v162 offset0:178 offset1:179\n" \
    "v_mfma_f32_16x16x32_bf16 v[36:39], v[100:103], v[128:131], v[36:39]\n" \
    "v_mfma_f32_16x16x32_bf16 v[32:35], v[100:103], v[156:159], v[32:35]\n" \
    "s_waitcnt lgkmcnt(6)\n" \
    "v_lshlrev_b32 v84, 16, v86\n" \
    "v_mfma_f32_16x16x32_bf16 v[28:31], v[100:103], v[152:155], v[28:31]\n" \
    "ds_read2_b32 v[100:101], v162 offset0:32 offset1:33\n" \
    "v_mfma_f32_16x16x32_bf16 v[44:47], v[104:107], v[132:135], v[44:47]\n" \
    "ds_read_b128 v[132:135], %[hp] offset:1920\n" \
    "v_and_b32 v85, 0xffff0000, v86\n" \
    "v_mfma_f32_16x16x32_bf16 v[40:43], v[104:107], v[128:131], v[40:43]\n" \
    "ds_read2_b32 v[102:103], v162 offset0:34 offset1:35\n" \
    "v_mfma_f32_16x16x32_bf16 v[36:39], v[104:107], v[156:159], v[36:39]\n" \
    "v_lshlrev_b32 v86, 16, v87\n" \
    "v_mfma_f32_16x16x32_bf16 v[32:35], v[104:107], v[152:155], v[32:35]\n" \
    "ds_read2_b32 v[104:105], v161 offset0:176 offset1:177\n" \
    "v_mfma_f32_16x16x32_bf16 v[44:47], v[108:111], v[128:131], v[44:47]\n" \
    "ds_read_b128 v[128:131], %[hp] offset:1408\n" \
    "v_and_b32 v87, 0xffff0000, v87\n" \
    "v_mfma_f32_16x16x32_bf16 v[40:43], v[108:111], v[156:159], v[40:43]\n" \
    "ds_read2_b32 v[106:107], v161 offset0:178 offset1:179\n" \
    "v_mfma_f32_16x16x32_bf16 v[36:39], v[108:111], v[152:155], v[36:39]\n" \
    "ds_read2_b32 v[108:109], v161 offset0:32 offset1:33\n" \
    "v_mfma_f32_16x16x32_bf16 v[44:47], v[112:115], v[156:159], v[44:47]\n" \
    "ds_read2_b32 v[110:111], v161 offset0:34 offset1:35\n" \
    "v_mfma_f32_16x16x32_bf16 v[40:43], v[112:115], v[152:155], v[40:43]\n" \
    "ds_read2_b32 v[112:113], v160 offset0:176 offset1:177\n" \
    "v_mfma_f32_16x16x32_bf16 v[44:47], v[116:119], v[152:155], v[44:47]\n" \
    "ds_read2_b32 v[114:115], v160 offset0:178 offset1:179\n" \
    "v_mfma_f32_16x16x32_bf16 v[44:47], v[88:91], v[148:151], v[44:47]\n" \
    "ds_read2_b32 v[116:117], v160 offset0:32 offset1:33\n" \
    "v_mfma_f32_16x16x32_bf16 v[40:43], v[88:91], v[144:147], v[40:43]\n" \
    "ds_read2_b32 v[118:119], v160 offset0:34 offset1:35\n" \
    "s_waitcnt lgkmcnt(15)\n" \
    "v_mfma_f32_16x16x32_bf16 v[36:39], v[88:91], v[140:143], v[36:39]\n" \
    "ds_read_b128 v[152:155], %[hp] offset:448\n" \
    "s_waitcnt lgkmcnt(14)\n" \
    "v_mfma_f32_16x16x32_bf16 v[32:35], v[88:91], v[136:139], v[32:35]\n" \
    "ds_read_b128 v[156:159], %[hp] offset:960\n" \
    "s_waitcnt lgkmcnt(12)\n" \
    "v_mfma_f32_16x16x32_bf16 v[28:31], v[88:91], v[132:135], v[28:31]\n" \
    "ds_read_b128 v[148:151], %[hp] offset:4032\n" \
    "v_fmac_f32 v40, %[dd], v84\n" \
    "v_fmac_f32 v41, %[dd], v85\n" \
    "s_waitcnt lgkmcnt(10)\n" \
    "v_mfma_f32_16x16x32_bf16 v[24:27], v[88:91], v[128:131], v[24:27]\n" \
    "v_fmac_f32 v42, %[dd], v86\n" \
    "v_fmac_f32 v43, %[dd], v87\n" \
    "v_mfma_f32_16x16x32_bf16 v[20:23], v[88:91], v[124:127], v[20:23]\n" \
    "v_mfma_f32_16x16x32_bf16 v[16:19], v[88:91], v[120:123], v[16:19]\n" \
    "ds_read2_b32 v[88:89], v163 offset0:192 offset1:193\n" \
    "v_mfma_f32_16x16x32_bf16 v[44:47], v[92:95], v[144:147], v[44:47]\n" \
    "ds_read2_b32 v[90:91], v163 offset0:194 offset1:195\n" \
    "v_mfma_f32_16x16x32_bf16 v[40:43], v[92:95], v[140:143], v[40:43]\n" \
    "ds_read_b128 v[144:147], %[hp] offset:3520\n" \
    "v_mfma_f32_16x16x32_bf16 v[36:39], v[92:95], v[136:139], v[36:39]\n" \
    "v_mfma_f32_16x16x32_bf16 v[32:35], v[92:95], v[132:135], v[32:35]\n" \
    "v_mfma_f32_16x16x32_bf16 v[28:31], v[92:95], v[128:131], v[28:31]\n" \
    "ds_read_b64 v[86:87], %[kvr] offset:3584\n" \
    "v_mfma_f32_16x16x32_bf16 v[24:27], v[92:95], v[124:127], v[24:27]\n" \
    "v_mfma_f32_16x16x32_bf16 v[20:23], v[92:95], v[120:123], v[20:23]\n" \
    "ds_read2_b32 v[92:93], v163 offset0:48 offset1:49\n" \
    "v_mfma_f32_16x16x32_bf16 v[44:47], v[96:99], v[140:143], v[44:47]\n" \
    "ds_read2_b32 v[94:95], v163 offset0:50 offset1:51\n" \
    "v_mfma_f32_16x16x32_bf16 v[40:43], v[96:99], v[136:139], v[40:43]\n" \
    "ds_read_b128 v[140:143], %[hp] offset:3008\n" \
    "v_mfma_f32_16x16x32_bf16 v[36:39], v[96:99], v[132:135], v[36:39]\n" \
    "v_mfma_f32_16x16x32_bf16 v[32:35], v[96:99], v[128:131], v[32:35]\n" \
    "v_mfma_f32_16x16x32_bf16 v[28:31], v[96:99], v[124:127], v[28:31]\n" \
    "v_mfma_f32_16x16x32_bf16 v[24:27], v[96:99], v[120:123], v[24:27]\n" \
    "ds_read2_b32 v[96:97], v162 offset0:192 offset1:193\n" \
    "v_mfma_f32_16x16x32_bf16 v[44:47], v[100:103], v[136:139], v[44:47]\n" \
    "ds_read2_b32 v[98:99], v162 offset0:194 offset1:195\n" \
    "v_mfma_f32_16x16x32_bf16 v[40:43], v[100:103], v[132:135], v[40:43]\n" \
    "ds_read_b128 v[136:139], %[hp] offset:2496\n" \
    "v_mfma_f32_16x16x32_bf16 v[36:39], v[100:103], v[128:131], v[36:39]\n" \
    "v_mfma_f32_16x16x32_bf16 v[32:35], v[100:103], v[124:127], v[32:35]\n" \
    "s_waitcnt lgkmcnt(6)\n" \
    "v_lshlrev_b32 v84, 16, v86\n" \
    "v_mfma_f32_16x16x32_bf16 v[28:31], v[100:103], v[120:123], v[28:31]\n" \
    "ds_read2_b32 v[100:101], v162 offset0:48 offset1:49\n" \
    "v_mfma_f32_16x16x32_bf16 v[44:47], v[104:107], v[132:135], v[44:47]\n" \
    "ds_read_b128 v[132:135], %[hp] offset:1984\n" \
    "v_and_b32 v85, 0xffff0000, v86\n" \
    "v_mfma_f32_16x16x32_bf16 v[40:43], v[104:107], v[128:131], v[40:43]\n" \
    "ds_read2_b32 v[102:103], v162 offset0:50 offset1:51\n" \
    "v_mfma_f32_16x16x32_bf16 v[36:39], v[104:107], v[124:127], v[36:39]\n" \
    "v_lshlrev_b32 v86, 16, v87\n" \
    "v_mfma_f32_16x16x32_bf16 v[32:35], v[104:107], v[120:123], v[32:35]\n" \
    "ds_read2_b32 v[104:105], v161 offset0:192 offset1:193\n" \
    "v_mfma_f32_16x16x32_bf16 v[44:47], v[108:111], v[128:131], v[44:47]\n" \
    "ds_read_b128 v[128:131], %[hp] offset:1472\n" \
    "v_and_b32 v87, 0xffff0000, v87\n" \
    "v_mfma_f32_16x16x32_bf16 v[40:43], v[108:111], v[124:127], v[40:43]\n" \
    "ds_read2_b32 v[106:107], v161 offset0:194 offset1:195\n" \
    "v_mfma_f32_16x16x32_bf16 v[36:39], v[108:111], v[120:123], v[36:39]\n" \
    "ds_read2_b32 v[108:109], v161 offset0:48 offset1:49\n" \
    "v_mfma_f32_16x16x32_bf16 v[44:47], v[112:115], v[124:127], v[44:47]\n" \
    "ds_read2_b32 v[110:111], v161 offset0:50 offset1:51\n" \
    "v_mfma_f32_16x16x32_bf16 v[40:43], v[112:115], v[120:123], v[40:43]\n" \
    "ds_read2_b32 v[112:113], v160 offset0:192 offset1:193\n" \
    "v_mfma_f32_16x16x32_bf16 v[44:47], v[116:119], v[120:123], v[44:47]\n" \
    "ds_read2_b32 v[114:115], v160 offset0:194 offset1:195\n" \
    "v_mfma_f32_16x16x32_bf16 v[16:19], v[88:91], v[152:155], v[16:19]\n" \
    "ds_read2_b32 v[116:117], v160 offset0:48 offset1:49\n" \
    "v_mfma_f32_16x16x32_bf16 v[20:23], v[88:91], v[156:159], v[20:23]\n" \
    "ds_read2_b32 v[118:119], v160 offset0:50 offset1:51\n" \
    "s_waitcnt lgkmcnt(15)\n" \
    "v_mfma_f32_16x16x32_bf16 v[20:23], v[92:95], v[152:155], v[20:23]\n" \
    "ds_read_b32 v50, %[ctl] offset:88\n" \
    "s_waitcnt lgkmcnt(8)\n" \
    "v_mfma_f32_16x16x32_bf16 v[24:27], v[88:91], v[128:131], v[24:27]\n" \
    "v_fmac_f32 v44, %[dd], v84\n" \
    "v_fmac_f32 v45, %[dd], v85\n" \
    "v_mfma_f32_16x16x32_bf16 v[24:27], v[92:95], v[156:159], v[24:27]\n" \
    "v_fmac_f32 v46, %[dd], v86\n" \
    "v_fmac_f32 v47, %[dd], v87\n" \
    "v_mfma_f32_16x16x32_bf16 v[24:27], v[96:99], v[152:155], v[24:27]\n" \
    "v_mfma_f32_16x16x32_bf16 v[28:31], v[88:91], v[132:135], v[28:31]\n" \
    "v_cvt_pk_bf16_f32 v52, v16, v17\n" \
    "v_cvt_pk_bf16_f32 v54, v20, v21\n" \
    "v_mfma_f32_16x16x32_bf16 v[28:31], v[92:95], v[128:131], v[28:31]\n" \
    "v_cvt_pk_bf16_f32 v53, v18, v19\n" \
    "v_cvt_pk_bf16_f32 v55, v22, v23\n" \
    "v_mfma_f32_16x16x32_bf16 v[28:31], v[96:99], v[156:159], v[28:31]\n" \
    "v_min_u32 v52, -2, v52\n" \
    "v_min_u32 v54, -2, v54\n" \
    "v_mfma_f32_16x16x32_bf16 v[28:31], v[100:103], v[152:155], v[28:31]\n" \
    "v_min_u32 v53, -2, v53\n" \
    "v_min_u32 v55, -2, v55\n" \
    "v_mfma_f32_16x16x32_bf16 v[32:35], v[88:91], v[136:139], v[32:35]\n" \
    "v_permlane16_swap_b32 v52, v54\n" \
    "v_add_u32 v68, 0, %[yo]\n" \
    "v_mfma_f32_16x16x32_bf16 v[32:35], v[92:95], v[132:135], v[32:35]\n" \
    "v_permlane16_swap_b32 v53, v55\n" \
    "v_mfma_f32_16x16x32_bf16 v[32:35], v[96:99], v[128:131], v[32:35]\n" \
    "s_waitcnt lgkmcnt(0)\n" \
    "v_readfirstlane_b32 %[tmp], v50\n" \
    "s_mov_b32 %[cnt], 0\n" \
    "s_cmp_lg_u32 %[tmp], 0\n" \
    "s_cbranch_scc1 Lok_%=\n" \
    "Ls2_%=:\n" \
    "ds_read_b32 v50, %[ctl] offset:88\n" \
    "s_add_u32 %[cnt], %[cnt], 1\n" \
    "s_waitcnt lgkmcnt(0)\n" \
    "v_readfirstlane_b32 %[tmp], v50\n" \
    "s_cmp_ge_u32 %[cnt], %[cap]\n" \
    "s_cbranch_scc1 Lok_%=\n" \
    "s_cmp_eq_u32 %[tmp], 0\n" \
    "s_cbranch_scc1 Ls2_%=\n" \
    "Lok_%=:\n" \
    "global_store_dwordx4 v68, v[52:55], %[wsp] sc1\n" \
    "v_mfma_f32_16x16x32_bf16 v[32:35], v[100:103], v[156:159], v[32:35]\n" \
    "v_cvt_pk_bf16_f32 v56, v24, v25\n" \
    "v_cvt_pk_bf16_f32 v58, v28, v29\n" \
    "v_mfma_f32_16x16x32_bf16 v[32:35], v[104:107], v[152:155], v[32:35]\n" \
    "v_cvt_pk_bf16_f32 v57, v26, v27\n" \
    "v_cvt_pk_bf16_f32 v59, v30, v31\n" \
    "v_mfma_f32_16x16x32_bf16 v[36:39], v[88:91], v[140:143], v[36:39]\n" \
    "v_min_u32 v56, -2, v56\n" \
    "v_min_u32 v58, -2, v58\n" \
    "v_mfma_f32_16x16x32_bf16 v[36:39], v[92:95], v[136:139], v[36:39]\n" \
    "v_min_u32 v57, -2, v57\n" \
    "v_min_u32 v59, -2, v59\n" \
    "v_mfma_f32_16x16x32_bf16 v[36:39], v[96:99], v[132:135], v[36:39]\n" \
    "v_permlane16_swap_b32 v56, v58\n" \
    "v_add_u32 v69, 655360, %[yo]\n" \
    "v_mfma_f32_16x16x32_bf16 v[36:39], v[100:103], v[128:131], v[36:39]\n" \
    "v_permlane16_swap_b32 v57, v59\n" \
    "v_mfma_f32_16x16x32_bf16 v[36:39], v[104:107], v[156:159], v[36:39]\n" \
    "global_store_dwordx4 v69, v[56:59], %[wsp] sc1\n" \
    "v_mfma_f32_16x16x32_bf16 v[36:39], v[108:111], v[152:155], v[36:39]\n" \
    "v_mfma_f32_16x16x32_bf16 v[40:43], v[88:91], v[144:147], v[40:43]\n" \
    "v_mfma_f32_16x16x32_bf16 v[40:43], v[92:95], v[140:143], v[40:43]\n" \
    "v_mfma_f32_16x16x32_bf16 v[40:43], v[96:99], v[136:139], v[40:43]\n" \
    "v_mfma_f32_16x16x32_bf16 v[40:43], v[100:103], v[132:135], v[40:43]\n" \
    "v_cvt_pk_bf16_f32 v60, v32, v33\n" \
    "v_cvt_pk_bf16_f32 v62, v36, v37\n" \
    "v_mfma_f32_16x16x32_bf16 v[40:43], v[104:107], v[128:131], v[40:43]\n" \
    "v_cvt_pk_bf16_f32 v61, v34, v35\n" \
    "v_cvt_pk_bf16_f32 v63, v38, v39\n" \
    "v_mfma_f32_16x16x32_bf16 v[40:43], v[108:111], v[156:159], v[40:43]\n" \
    "v_min_u32 v60, -2, v60\n" \
    "v_min_u32 v62, -2, v62\n" \
    "v_mfma_f32_16x16x32_bf16 v[40:43], v[112:115], v[152:155], v[40:43]\n" \
    "v_min_u32 v61, -2, v61\n" \
    "v_min_u32 v63, -2, v63\n" \
    "v_mfma_f32_16x16x32_bf16 v[44:47], v[88:91], v[148:151], v[44:47]\n" \
    "v_permlane16_swap_b32 v60, v62\n" \
    "v_add_u32 v70, 1310720, %[yo]\n" \
    "v_mfma_f32_16x16x32_bf16 v[44:47], v[92:95], v[144:147], v[44:47]\n" \
    "v_permlane16_swap_b32 v61, v63\n" \
    "v_mfma_f32_16x16x32_bf16 v[44:47], v[96:99], v[140:143], v[44:47]\n" \
    "global_store_dwordx4 v70, v[60:63], %[wsp] sc1\n" \
    "v_mfma_f32_16x16x32_bf16 v[44:47], v[100:103], v[136:139], v[44:47]\n" \
    "v_mfma_f32_16x16x32_bf16 v[44:47], v[104:107], v[132:135], v[44:47]\n" \
    "v_mfma_f32_16x16x32_bf16 v[44:47], v[108:111], v[128:131], v[44:47]\n" \
    "v_mfma_f32_16x16x32_bf16 v[44:47], v[112:115], v[156:159], v[44:47]\n" \
    "v_mfma_f32_16x16x32_bf16 v[44:47], v[116:119], v[152:155], v[44:47]\n" \
    "s_nop 7\n" \
    "v_cvt_pk_bf16_f32 v64, v40, v41\n" \
    "v_cvt_pk_bf16_f32 v66, v44, v45\n" \
    "v_cvt_pk_bf16_f32 v65, v42, v43\n" \
    "v_cvt_pk_bf16_f32 v67, v46, v47\n" \
    "v_min_u32 v64, -2, v64\n" \
    "v_min_u32 v66, -2, v66\n" \
    "v_min_u32 v65, -2, v65\n" \
    "v_min_u32 v67, -2, v67\n" \
    "v_permlane16_swap_b32 v64, v66\n" \
    "v_add_u32 v71, 1966080, %[yo]\n" \
    "v_permlane16_swap_b32 v65, v67\n" \
    "global_store_dwordx4 v71, v[64:67], %[wsp] sc1\n" \
    "s_nop 1\n"
#define CONV_CLOB "v16", "v17", "v18", "v19", "v20", "v21", "v22", "v23", "v24", "v25", "v26", "v27", "v28", "v29", "v30", "v31", "v32", "v33", "v34", "v35", "v36", "v37", "v38", "v39", "v40", "v41", "v42", "v43", "v44", "v45", "v46", "v47", "v50", "v51", "v52", "v53", "v54", "v55", "v56", "v57", "v58", "v59", "v60", "v61", "v62", "v63", "v64", "v65", "v66", "v67", "v68", "v69", "v70", "v71", "v72", "v73", "v74", "v75", "v76", "v77", "v78", "v79", "v80", "v81", "v82", "v83", "v84", "v85", "v86", "v87", "v88", "v89", "v90", "v91", "v92", "v93", "v94", "v95", "v96", "v97", "v98", "v99", "v100", "v101", "v102", "v103", "v104", "v105", "v106", "v107", "v108", "v109", "v110", "v111", "v112", "v113", "v114", "v115", "v116", "v117", "v118", "v119", "v120", "v121", "v122", "v123", "v124", "v125", "v126", "v127", "v128", "v129", "v130", "v131", "v132", "v133", "v134", "v135", "v136", "v137", "v138", "v139", "v140", "v141", "v142", "v143", "v144", "v145", "v146", "v147", "v148", "v149", "v150", "v151", "v152", "v153", "v154", "v155", "v156", "v157", "v158", "v159", "v160", "v161", "v162", "v163", "scc", "memory"

static_assert(C_HR - L_CTRL == 144 && C_RED - L_CTRL == 84 && C_XD - L_CTRL == 80 && C_OWN - L_CTRL == 88, "offsets in the conv stream");
typedef __attribute__((ext_vector_type(16))) unsigned u32x16;
typedef __attribute__((address_space(3))) const unsigned lds_cu32;
typedef __attribute__((address_space(3))) u32x2 lds_u32x2;
typedef __attribute__((address_space(3))) u32x4 lds_u32x4;
typedef __attribute__((address_space(3))) f32x4 lds_f32x4;
#define GPTR(T, lo, hi) ((T*)(__attribute__((address_space(1))) T*)(((u64)(hi) << 32) | (u64)(lo)))
#if defined(__HIP_DEVICE_COMPILE__)
#define NOPK __attribute__((target("no-packed-fp32-ops")))
#else
#define NOPK
#endif
__global__ NOPK __launch_bounds__(768, 3) void fused_kernel(const u16* x_, const u16* wqkv_, const u16* kin_, const u16* vin_, const u16* shift_k_,
                                                       const u16* ssm_k_, const u16* ssm_D_, const u16* wo_,
                                                       const u16* gw_, char* ws_, u16* out_) {
    u32x16 ka0;
    u32x4 ka1;
    u32x2 ka2;
    asm volatile("s_load_dwordx16 %0, %3, 0\n\ts_load_dwordx4 %1, %3, 64\n\ts_load_dwordx2 %2, %3, 80"
                 : "=&s"(ka0), "=&s"(ka1), "=&s"(ka2) : "s"(__builtin_amdgcn_kernarg_segment_ptr()));
#define x GPTR(const u16, ka0[0], ka0[1])
#define wqkv GPTR(const u16, ka0[2], ka0[3])
#define kin GPTR(const u16, ka0[4], ka0[5])
#define vin GPTR(const u16, ka0[6], ka0[7])
#define shift_k GPTR(const u16, ka0[8], ka0[9])
#define ssm_k GPTR(const u16, ka0[10], ka0[11])
#define ssm_D GPTR(const u16, ka0[12], ka0[13])
#define wo GPTR(const u16, ka0[14], ka0[15])
#define gw GPTR(const u16, ka1[0], ka1[1])
#define ws GPTR(char, ka1[2], ka1[3])
#define out GPTR(u16, ka2[0], ka2[1])
#define wsr __builtin_amdgcn_make_buffer_rsrc(ws, 0, 0x4000000, 0x00020000)
    __shared__ __attribute__((aligned(16))) char smem[LDS_TOTAL];
    const int bid = __builtin_amdgcn_workgroup_id_x();
    const int tid = __builtin_amdgcn_workitem_id_x();
    const int lane = tid & 63;
    const int wave = __builtin_amdgcn_readfirstlane(tid >> 6);
    const int xcd = bid & 7, r5 = bid >> 3, r = r5 & 15;
    const bool isG = r5 >= 16;
    const int hg = (xcd & 1) * 4 + (r & 3);
    const int rb2 = (xcd >> 1) * 4 + (r >> 2);
    const int cq = (xcd & 1) * 2 + (r & 1), rbv = (xcd >> 1) * 8 + (r >> 1);
    const int vid = (cq ^ CQ_X) * 32 + rbv;
    const int sid = rbv * 4 + cq;
    const int rbo = ((xcd + X_SHIFT) & 7) * 4 + (r >> 3) * 2 + (r5 >> 4), oc = r5 & 7;
    const int rto = (rbo >> 1) * 8 + (rbo & 1) * 2;
#define ROWT(i) (rto + ((i) & 1) + ((i) >> 1) * 4)
#define DUE_H ph
    const int w = wave & 3;
    const int g = lane >> 4, n16 = lane & 15, q4 = n16 >> 2, p4 = lane & 3;
    const unsigned lb = (unsigned)(size_t)(lds_char*)smem;
    u64 tag;
    {
        u64 id = llvm_dispatch_id();
        u64 dp = (u64)(size_t)__builtin_amdgcn_dispatch_ptr();
        tag = ((u64)((unsigned)id ^ 0x5bd1e995u) << 32) | (u64)((unsigned)(dp >> 6) ^ (unsigned)(id >> 32) ^ 0x9e3779b9u);
    }
    const unsigned tlo = (unsigned)tag, thi = (unsigned)(tag >> 32);
    u64 code0;
    asm volatile("s_getpc_b64 %0" : "=s"(code0));
    code0 &= ~255ull;
    if (wave == 0) {
        const unsigned cl = lane * 32 + r5;
        if (cl < CODE_LINES) GLT(cl * 128, code0);
    }
    const u32x4 ones = {0xffffffffu, 0xffffffffu, 0xffffffffu, 0xffffffffu};

#define WO_OFF(c) ({ const int rho_ = (c) / 10, ch_ = (c) - rho_ * 10; (unsigned)((rho_ ^ ((rho_ & 8) >> 1)) * (D_DIM * 2) + oc * 160 + ch_ * 16); })
    const int kr = KROT;
#define T_C(i) __builtin_amdgcn_readfirstlane((kr + (i)) >= 10 ? kr + (i) - 10 : kr + (i))
#define T_WV() ({ const int j_ = min(n16, 14); (unsigned)((r * 4 + g) * (NQKV * 2) + j_ * 128 + ((j_ >= 5) + (j_ >= 10) + (xcd & 1)) * 640); })
#define T_XV() ((unsigned)(((xcd >> 1) * 512 + r * 32 + (lane & 31)) * (D_DIM * 2)))
#define T_W(wv, i) GLT(wv, (const char*)wqkv + (size_t)T_C(i) * (64 * NQKV * 2))
#define T_X(xv, i) { const int c0_ = T_C(i), c1_ = T_C((i) + 1); GLT((xv) + (lane < 32 ? c0_ : c1_) * 128, x); }
#define T_PAIR(i) { T_W(wv, i); T_X(xv_, i); T_W(wv, (i) + 1); }

    unsigned e0;
    if (isG) {
        const unsigned t0_ = T_WV() + (kr + (w >> 1)) * (64 * NQKV * 2);
        const unsigned t1_ = ((xcd >> 1) * 512 + r * 32 + (lane & 31)) * (D_DIM * 2) + (kr + (lane >> 5)) * 128;
        e0 = (w & 1) ? t1_ : t0_;
    } else {
        const unsigned c = (wave >= 5 ? wave - 5 : wave) * 64 + lane, row = (c * 3277) >> 16;
        e0 = rbv * (64 * D_DIM * 2) + cq * 320 + row * (D_DIM * 2 - 320) + c * 16;
    }
    asm volatile("" : "+v"(e0));
    asm volatile("s_waitcnt lgkmcnt(0)" : "+s"(ka0), "+s"(ka1), "+s"(ka2) :: "memory");
    if (isG) {
        if (wave >= 4 && wave < 7) GLT(e0, (w & 1) ? x : wqkv);
        BARRIER();
    }
    u32x2 skw;
    if (!isG && wave < 10) {
        const int tens = wave >= 5, i5 = wave - 5 * tens;
        const unsigned lw = lb + (tens ? V_VST : V_KST) + i5 * 1024;
        const char* kb = (const char*)(tens ? vin : kin);
        __builtin_amdgcn_s_sleep(V_DELAY);
        GLS(e0, kb, lw, 0);
        GLS(e0, kb + 16 * D_DIM * 2, lw, 5152);
        GLS(e0, kb + 32 * D_DIM * 2, lw, 2 * 5152);
        GLS(e0, kb + 48 * D_DIM * 2, lw, 3 * 5152);
        asm volatile("global_load_dwordx2 %0, %1, %2" : "=v"(skw) : "v"((cq * 160 + wave * 16 + n16) * 8), "s"(shift_k) : "memory");
        if (wave == 0) { if (lane < 60) GLS(e0, kb - (rbv ? 3 * D_DIM * 2 : 0), lb, V_KST - 32 - 960); }
    }
    if (wave == 11) {
        if (lane < 16) *(u32x4*)(smem + L_CTRL + lane * 16) = (u32x4){0u, 0u, 0u, 0u};
        asm volatile("s_waitcnt lgkmcnt(0)" ::: "memory");
    }
    STAMP(1);
#define CF_OFF() ((unsigned)(lane < 16 ? WS_HF + ROWT(lane & 3) * 128 : lane < 32 ? WS_SF + (oc * 16 + (lane & 15)) * 128 : WS_HN + ((rbo >> 1) * 8 + (lane & 7)) * 128))
#define NOW() ((unsigned)__builtin_amdgcn_s_memrealtime())
#define TICKS_PER_CYCLE(now, c0, r0) ((float)((now) - (r0)) * __builtin_amdgcn_rcpf((float)((unsigned)__builtin_amdgcn_s_memtime() - (c0))))
#define CF_LOOK(v) { const bool k_ = v[0] == tlo && v[1] == thi; cfm |= __builtin_amdgcn_ballot_w64(k_); \
            if (k_) { tv = v[2] | 1; tu = v[3] | 1; } \
            if (!(~cfm & 0xffff) && lane >= 16 && lane < 40 && (lane < 32 ? !(~cfm & 0xffff0000u) : ((cfm >> (lane & ~1)) & 3) == 3)) \
                asm volatile("ds_max_u32 %0, %1\n\tds_max_u32 %0, %2 offset:16" ::"v"(lb + (lane < 32 ? C_TY : C_TM + (lane & 6) * 2)), "v"(tv), "v"(lane < 32 ? 0u : tu) : "memory"); }
#define CF_LOOP(off, need) _Pragma("nounroll") for (int it = 0; it < SPIN_CAP && (~cfm & (need)); ++it) { \
            if (it) __builtin_amdgcn_s_sleep(CF_SLEEP); \
            asm volatile("" ::: "memory"); \
            u32x4 v = {0u, 0u, 0u, 0u}; \
            if (!((cfm >> lane) & 1)) v = __builtin_amdgcn_raw_buffer_load_b128(wsr, off, 0, 16); \
            CF_LOOK(v); }
#define G_LD(n) __builtin_amdgcn_raw_buffer_load_b32(wsr, (n) * 4, WS_G + ROWT(w) * 64, 16)
#define G_CHK(v, n) { if (__builtin_expect(__any(v == 0xffffffffu), 0)) { _Pragma("nounroll") for (int it = 0; it < SPIN_CAP; ++it) { \
            asm volatile("" ::: "memory"); v = G_LD(n); if (!__any(v == 0xffffffffu)) break; __builtin_amdgcn_s_sleep(4); } } }
#define XCH_AT(rt) (lb + (isG ? ((rt) < 2 ? G_Q : G_XCH - 20480) : V_XCH) + (rt) * 10240)
#define Y_OFF (WS_Y + rbo * (D_DIM * 128) + oc * 10240 + w * 1024)
#define Y_LD(i) asm volatile("global_load_dwordx4 %0, %1, %2 sc1" : "=v"(yv[i]) : "v"(l16y), "s"(ws + Y_OFF + ((i) < 2 ? (i) * 4096 : w < 2 ? 8192 : 0)) : "memory")
#define Y_TIME(t) { _Pragma("nounroll") for (int it = 0; it < 20000 && (int)(NOW() - (t)) < 0; ++it) {} }
#define TY_GET() ({ unsigned t_ = 0; _Pragma("nounroll") for (int it = 0; it < SPIN_CAP; ++it) { t_ = __builtin_amdgcn_readfirstlane(lds_ld32(lb + C_TY)); if (t_) break; __builtin_amdgcn_s_sleep(3); } t_; })
#define G_FIRST() unsigned g1 = G_LD(lane & 15)
#define Y_FIRST() { Y_LD(0); Y_LD(1); Y_LD(2); }
#define G_MUL() { G_CHK(g1, lane & 15); _Pragma("unroll") for (int ct = 0; ct < 5; ++ct) gq[ct] *= __uint_as_float(g1); \
        asm volatile("" : "+v"(gq[0]), "+v"(gq[1]), "+v"(gq[2]), "+v"(gq[3]), "+v"(gq[4])); }
#define RELANE() const int lane_ = opaque(lane); const int g = lane_ >> 4, n16 = lane_ & 15, q4 = n16 >> 2, p4 = lane_ & 3
#define SENT4(v) __any(max(max((v)[0], (v)[1]), max((v)[2], (v)[3])) == 0xffffffffu)
#define Q_READ(base) f32x4 gq[5]; { const int l_ = opaque(lane); _Pragma("unroll") for (int ct = 0; ct < 5; ++ct) { \
            const u32x2 q2 = *(const lds_u32x2*)(size_t)(lb + (base) + w * 2560 + (l_ & 15) * 160 + 8 * (l_ >> 4) + ct * 32); \
            gq[ct] = (f32x4){lo_f(q2[0]), hi_f(q2[0]), lo_f(q2[1]), hi_f(q2[1])}; } \
        asm volatile("" : "+v"(gq[0]), "+v"(gq[1]), "+v"(gq[2]), "+v"(gq[3]), "+v"(gq[4]) :: "memory"); }
#define P_STAGE(YE) { \
        RELANE(); \
        FLUSH_LO(); \
        const unsigned l_y = lb + (isG ? G_Y : V_Y); \
        const unsigned yw = opaque(l_y + (w * 8 + (lane_ >> 3)) * Y_PITCH + (lane_ & 7) * 16), yr = opaque(l_y + (4 * g + q4) * Y_PITCH + w * 32 + p4 * 8), xa = opaque(XCH_AT(w) + lane_ * 16); \
        u16* ob = out + (size_t)(ROWT(w) * 16 + n16) * D_DIM + oc * 80; \
        const float g0 = 1.f - __uint_as_float(g1); \
        asm volatile("" : "+v"(ob)); \
_Pragma("nounroll") \
        for (int it = 0; it < SPIN_CAP; ++it) { \
            asm volatile("s_cmp_eq_u32 %3, 0\n\ts_cbranch_scc0 1f\n\ts_waitcnt vmcnt(0)\n1:\n\ts_waitcnt vmcnt(3)" : "+v"(yv[0]), "+v"(yv[1]), "+v"(yv[2]) : "s"(__builtin_amdgcn_readfirstlane(it == 0 ? (unsigned)(YE) : 0u)) : "memory", "scc"); \
            const bool b0 = SENT4(yv[0]), b1 = SENT4(yv[1]), b2 = SENT4(yv[2]); \
            if (!(b0 | b1 | b2)) break; \
            if (it || Y_SL0) __builtin_amdgcn_s_sleep(Y_SLEEP); \
            if (b0) Y_LD(0); \
            if (b1) Y_LD(1); \
            if (b2) Y_LD(2); \
        } \
        STAMP(8); \
        *(lds_u32x4*)(size_t)yw = yv[0]; \
        *(lds_u32x4*)(size_t)(yw + 32 * Y_PITCH) = yv[1]; \
        if (w < 2) *(lds_u32x4*)(size_t)(yw + 64 * Y_PITCH) = yv[2]; \
        lds_add(lb + C_Y, lane_); \
        spin_ge(lb + C_Y, 4); \
        u32x2 y2[5]; \
        const unsigned xc = lds_v32(lb + C_XCH + w * 4); \
_Pragma("unroll") \
        for (int ct = 0; ct < 5; ++ct) y2[ct] = __builtin_bit_cast(u32x2, TRRD((lds_char*)(size_t)(yr + ct * 16 * Y_PITCH))); \
        SB(); \
        f32x4 pa[5], pb[5]; \
        const bool early = __builtin_amdgcn_readfirstlane(xc) >= 2; \
        if (early) { _Pragma("unroll") for (int ct = 0; ct < 5; ++ct) { pa[ct] = *(const lds_f32x4*)(size_t)(xa + ct * 1024); pb[ct] = *(const lds_f32x4*)(size_t)(xa + 5120 + ct * 1024); } SB(); } \
_Pragma("unroll") \
        for (int ct = 0; ct < 5; ++ct) gq[ct] *= (f32x4){lo_f(y2[ct][0]), hi_f(y2[ct][0]), lo_f(y2[ct][1]), hi_f(y2[ct][1])}; \
        asm volatile("" : "+v"(gq[0]), "+v"(gq[1]), "+v"(gq[2]), "+v"(gq[3]), "+v"(gq[4])); \
        STAMP(9); \
        if (!early) { \
            spin_ge(lb + C_XCH + w * 4, 2); \
            _Pragma("unroll") for (int ct = 0; ct < 5; ++ct) { pa[ct] = *(const lds_f32x4*)(size_t)(xa + ct * 1024); pb[ct] = *(const lds_f32x4*)(size_t)(xa + 5120 + ct * 1024); } \
        } \
        STAMP(13); \
        unsigned pk[5][2]; \
_Pragma("unroll") \
        for (int ct = 0; ct < 5; ++ct) { \
            const f32x4 fin = gq[ct] + g0 * (pa[ct] + pb[ct]); \
            pk[ct][0] = pack2(fin[0], fin[1]); \
            pk[ct][1] = pack2(fin[2], fin[3]); \
        } \
_Pragma("unroll") \
        for (int p = 0; p < 2; ++p) { \
            const auto r0 = __builtin_amdgcn_permlane16_swap(pk[2 * p][0], pk[2 * p + 1][0], false, false); \
            const auto r1 = __builtin_amdgcn_permlane16_swap(pk[2 * p][1], pk[2 * p + 1][1], false, false); \
            asm volatile("global_store_dwordx4 %0, %1, off " OUT_POL "\n\ts_nop 1" :: "v"(ob + (2 * p + (g & 1)) * 16 + (g >> 1) * 8), "v"((u32x4){r0[0], r1[0], r0[1], r1[1]}) : "memory"); \
        } \
        asm volatile("global_store_dwordx2 %0, %1, off " OUT_POL :: "v"(ob + 64 + 4 * g), "v"((u32x2){pk[4][0], pk[4][1]}) : "memory"); \
        FLUSH(); \
        return; }

    const int ph = isG ? wave >> 2 : 1 - (wave >> 2), kh = w >> 1, trt = (1 - ph) * 2 + (w & 1);
    unsigned ofo, pc, pd, xa;
    bool wo_open = isG && wave < 4;
#define TAIL_ADDR() { RELANE(); const int hsw = (g & 1) * 4; \
        ofo = opaque(WS_O + (ROWT(trt) * 20 + kh * 10) * 1024 + 2048 + lane_ * 16); \
        pc = opaque(lb + (8 * g + hsw + q4) * 160 + p4 * 8 + (isG ? (kh ? G_WO1 : G_WO0) : V_WO + kh * 51200)); \
        pd = opaque(pc + (4 - 2 * hsw) * 160); \
        xa = opaque(XCH_AT(trt) + kh * 5120 + lane_ * 16); }

    if (__builtin_expect(isG, 1)) {
      if (__builtin_expect(wave >= 4, 0)) {
        const int half = (wave >> 2) - 1;
        if (half) __builtin_amdgcn_s_setprio(C1_PRIO); else __builtin_amdgcn_s_setprio(C0_PRIO);
        const int hf = 1 - half, rb = rb2 * 2 + hf;
        f32x4 acc[4][5];
        unsigned aofs[2];
#pragma unroll
        for (int sub = 0; sub < 2; ++sub) aofs[sub] = lb + L_A + hf * 8192 + n16 * 128 + (((sub * 4 + g) ^ ((n16 >> 1) & 7)) * 16);
        unsigned wofs[4];
#pragma unroll
        for (int i = 0; i < 4; ++i) {
            const int part = i < 3 ? i : (p4 < 3 ? p4 : 2);
            const int Q = i < 3 ? 5 * w + (w & 1) + p4 : 5 * w + ((w & 1) ? 0 : 4);
            const int c2 = (Q >> 1) - 2 * (hg & 1);
            const int cc = ((unsigned)c2 < 8u ? part * 8 + c2 : 24 + part * 2 + (c2 & 1)) ^ (2 * ((g & 1) * 4 + q4));
            wofs[i] = lb + L_W + (8 * g + q4) * W_PITCH + cc * 16 + (Q & 1) * 8;
        }
        bf16x8 af[5], bA[4], bB[4];
        lds_char* wp[2][4];
        lds_char* ap[2];
#pragma unroll
        for (int i = 0; i < 4; ++i) { wp[0][i] = (lds_char*)(size_t)opaque(wofs[i]); wp[1][i] = (lds_char*)(size_t)opaque(wofs[i] + 3 * W_SLOT); }
        ap[0] = (lds_char*)(size_t)opaque(aofs[0]);
        ap[1] = (lds_char*)(size_t)opaque(aofs[1]);
        const unsigned ctl = opaque(lb + C_FREE), ctf = opaque(lb + C_FREE + (wave - 4) * 4);
#define WSL(J, i) (wp[((J) % NW) / 3][i] + (((J) % NW) % 3) * W_SLOT)
#define MFMA16A(c, a, b) asm volatile("v_mfma_f32_16x16x32_bf16 %0, %1, %2, %0" : "+v"(c) : "v"(a), "v"(b))
#define MFMA16Z(c, a, b) asm volatile("v_mfma_f32_16x16x32_bf16 %0, %1, %2, 0" : "=&v"(c) : "v"(a), "v"(b))
#define MFMA16(c, a, b) do { if (first_) MFMA16Z(c, a, b); else MFMA16A(c, a, b); } while (0)
#define BORD(rt) (((rt) + 1 - R0_) & 3)
#define FR0 (JL < 16 ? JL + 1 : 16)
#define STEP(J, bc, bn, R0) { \
            const bool first_ = (J) == 0; \
            constexpr int R0_ = R0; \
            unsigned fl_ = 4, fs_ = 4; \
            _Pragma("unroll") for (int rt = R0; rt < 5; ++rt) { \
                s16x4 b0_, b1_; \
                if ((J) > JL && (J) <= 17 && rt == 2) { fl_ = lds_v32(ctl + (C_FULL - C_FREE) + ((J) - 13) * 4); SB(); } \
                if ((J) >= FR0 && (J) < 19 && rt == 4) { if ((J) > JL && (J) <= 17) fs_ = __builtin_amdgcn_readfirstlane(fl_); asm volatile("v_mov_b32 %0, %1" : "=v"(fl_) : "n"(J)); SB(); } \
                if (rt > 0) MFMA16(acc[0][rt], bc[0], af[rt]); SB(); \
                if ((J) < 19 && rt < 4 + R0) { b0_ = TRRD(WSL((J) + 1, BORD(rt - R0))); SB(); } \
                MFMA16(acc[1][rt], bc[1], af[rt]); SB(); \
                if ((J) < 19 && rt < 4 + R0) { b1_ = TRRD(WSL((J) + 1, BORD(rt - R0)) + 4 * W_PITCH); bn[BORD(rt - R0)] = cat8(b0_, b1_); SB(); } \
                MFMA16(acc[2][rt], af[rt], bc[2]); SB(); \
                MFMA16(acc[3][rt], bc[3], af[rt]); SB(); \
                if ((J) < 19) { af[rt] = *(lds_cbf16x8*)(ap[((J) + 1) & 1] + ((((J) + 1) >> 1) % 3) * A_SLOT + rt * 2048); SB(); } \
            } \
            if ((J) >= FR0 && (J) < 19) lds_st32(ctf, fl_);        \
            if ((J) == JL && JL < 16) asm volatile("s_waitcnt lgkmcnt(0)" ::: "memory"); \
            if ((J) <= JL) BARRIER(); \
            if ((J) > JL && (J) <= 17) { if (__builtin_expect(fs_ < 4, 0)) spin_ge(lb + C_FULL + ((J) - 13) * 4, 4); } \
            SB(); }
#define GEMM(R0) { \
        _Pragma("unroll") for (int i = 0; i < 4; ++i) bA[i] = cat8(TRRD(wp[0][i]), TRRD(wp[0][i] + 4 * W_PITCH)); \
        _Pragma("unroll") for (int rt = R0; rt < 5; ++rt) af[rt] = *(lds_cbf16x8*)(ap[0] + rt * 2048); \
        SB(); \
        GT(0) STEP(0, bA, bB, R0) GT(1) STEP(1, bB, bA, R0) STAMP(5); GT(2) STEP(2, bA, bB, R0) GT(3) STEP(3, bB, bA, R0) GT(4) STEP(4, bA, bB, R0) GT(5) STEP(5, bB, bA, R0) \
        STAMP(6); GT(6) STEP(6, bA, bB, R0) GT(7) STEP(7, bB, bA, R0) GT(8) STEP(8, bA, bB, R0) GT(9) STEP(9, bB, bA, R0) STAMP(14); GT(10) STEP(10, bA, bB, R0) GT(11) STEP(11, bB, bA, R0) \
        GT(12) STEP(12, bA, bB, R0) GT(13) STEP(13, bB, bA, R0) GT(14) STEP(14, bA, bB, R0) GT(15) STEP(15, bB, bA, R0) GT(16) STEP(16, bA, bB, R0) GT(17) STEP(17, bB, bA, R0) \
        TS(0); GT(18) STEP(18, bA, bB, R0) GT(19) sent0 = lds_v32(lb + C_OWN); SB(); STEP(19, bB, bA, R0) GT(20) }
#define GT(j)
        f32x4 cm0, cm1;
        u32x4 psel, vxa;
        unsigned scw, oo, ox;
#define ATT_CONST() { \
        const int l_ = opaque(lane), g = l_ >> 4, qi = l_ & 15, q4 = qi >> 2, p4 = l_ & 3, par = l_ & 1; \
        _Pragma("unroll") for (int e = 0; e < 4; ++e) { \
            const bool own = 4 * g + e <= qi, pm = (e & 1) == par; \
            cm0[e] = pm && !own ? 0.f : -3.0e38f; \
            cm1[e] = pm && own ? 0.f : -3.0e38f; \
            const bool ow = 4 * g + par + 2 * (e & 1) <= qi;         \
            const unsigned by = (e & 1) ? 0x0302u : 0x0100u; \
            psel[e] = (ow == (e >= 2)) ? (par ? (by << 16) | 0x0c0cu : 0x0c0c0000u | by) : 0x0c0c0c0cu; \
            vxa[e] = p4 == e ? lb + G_VX + w * 1152 + (hf * 4 + e) * 128 + (4 * g + q4) * 8 : lb + L_CTRL; \
        } \
        const int cc = (hg * 80 + w * 20 + (w & 1) * 4) / 8 + (g >> 1), colx = hg * 80 + w * 20 + ((w & 1) ? 0 : 16); \
        oo = WS_O + ((rb * 4 + (g & 1)) * 20 + (cc >> 2)) * 1024 + ((cc & 3) * 16 + qi) * 16; \
        ox = (g * 20 + (colx >> 5)) * 1024 + (((colx >> 3) & 3) * 16 + qi) * 16 + (colx & 4) * 2; \
        asm volatile("" : "+v"(cm0), "+v"(cm1), "+v"(psel), "+v"(vxa), "+v"(oo), "+v"(ox)); SB(); }
        scw = g == 2 ? lb + G_VX + w * 1152 + hf * 512 + n16 * 8 : lb + G_VXD;
        asm volatile("" : "+v"(scw));
        ATT_CONST();
        unsigned sent0;
        unsigned hxo = opaque(WS_HX + ((rb2 + hf) * 8 + hg) * 5120 + w * 1280 + lane * 16), hxp = opaque(hxo + 1024 - lane * 12);
        unsigned hxw = opaque(g >= 2 ? lb + G_VX + w * 1152 + n16 * 8 + (g & 1) * 4 : lb + G_VXD);
        BARRIER();
        STAMP(7);
        CSTAMP(0);
        GEMM(1)
        TS(1);
        asm volatile("s_nop 7\n\ts_nop 7" ::: "memory");
        STAMP(2);
        CSTAMP(1);

        __builtin_amdgcn_s_setprio(T_PRIO);
        typedef __attribute__((address_space(3))) volatile unsigned lds_vu32;
#define HINT(at)
        u32x4 kop[5], qop[5];
        u32x16 vv;
        const unsigned hx = lb + G_HX + w * 1152;
        u32x4 ha0 = {0u, 0u, 0u, 0u};
        unsigned hb0 = 0;
        const unsigned far = half * rb2;
#define HX_LD() asm volatile("s_cmp_eq_u32 %5, 0\n\ts_cbranch_scc1 1f\n\tglobal_load_dwordx4 %0, %2, %4 sc1\n\tglobal_load_dword %1, %3, %4 sc1\n1:" \
            : "+v"(ha0), "+v"(hb0) : "v"(hxo), "v"(hxp), "s"(ws), "s"(far) : "memory", "scc")
        if (__builtin_expect(!__builtin_amdgcn_readfirstlane(sent0), 0)) spin_ge(lb + C_OWN, 1);
        if (HX_AT == 5) { HX_LD(); SB(); }
#pragma unroll
        for (int rt = 4; rt >= 1; --rt) {
            const unsigned d0 = pack2(acc[3][rt][0], acc[3][rt][1]), d1 = pack2(acc[3][rt][2], acc[3][rt][3]);
            asm volatile("ds_write2_b32 %0, %1, %2 offset0:%3 offset1:%4" ::"v"(scw), "v"(d0), "v"(d1), "n"(rt * 32), "n"(rt * 32 + 1) : "memory");
            const auto sw2 = __builtin_amdgcn_permlane16_swap(d0, d1, false, false);
            kop[rt][0] = pack2(acc[1][rt][0], acc[1][rt][1]);
            kop[rt][1] = pack2(acc[1][rt][2], acc[1][rt][3]);
            kop[rt][2] = sw2[1];
            kop[rt][3] = 0u;
            vv[2 * rt] = pack2(acc[2][rt][0], acc[2][rt][1]);
            vv[2 * rt + 1] = pack2(acc[2][rt][2], acc[2][rt][3]);
            if (rt == 4) {
                if (half) {
                    asm volatile("ds_write2_b64 %0, %1, %2 offset1:1" ::"v"(hx + lane * 16), "v"((u32x2){kop[4][0], kop[4][1]}), "v"((u32x2){vv[8], vv[9]}) : "memory");
                    asm volatile("ds_write_b32 %0, %1" ::"v"(g < 2 ? hx + 1024 + lane * 4 : lb + L_TOUCH), "v"(kop[4][2]) : "memory");
                    lds_st32(lb + C_HX + w * 4, 1);
                } else if (rb2 < 15) {
                    __builtin_amdgcn_raw_buffer_store_b128((u32x4){min(kop[4][0], 0xfffffffeu), min(kop[4][1], 0xfffffffeu), min(vv[8], 0xfffffffeu), min(vv[9], 0xfffffffeu)}, wsr, hxo, 0, 16);
                    __builtin_amdgcn_raw_buffer_store_b32(min(g < 2 ? sw2[1] : sw2[0], 0xfffffffeu), wsr, hxp, 0, 16);
                }
                if (!half) lds_add(lb + C_HS, lane);
            }
            qop[rt][0] = pack2(acc[0][rt][0], acc[0][rt][1]);
            qop[rt][1] = pack2(acc[0][rt][2], acc[0][rt][3]);
            qop[rt][2] = g < 2 ? sw2[0] : 0u;
            qop[rt][3] = 0u;
            if (rt == HX_AT) { SB(); HX_LD(); if (half) lds_add(lb + C_HS, lane); SB(); }
        }
        TS(2);
        bf16x8 vx[5];
#pragma unroll
        for (int rt = 4; rt >= 2; --rt) vx[rt] = cat8(TRRD((lds_char*)(size_t)vxa[rt - 1]), TRRD((lds_char*)(size_t)(vxa[rt - 1] + 128)));
        TS(3);
        STAMP(3);
        const float scale = 0.22360679774997896f * 1.4426950408889634f;
        const f32x4 z4 = {0.f, 0.f, 0.f, 0.f};
        float l0[5], l1[5], mx[5];
        u32x4 hl4;
        unsigned hl1, hx0;
#pragma unroll
        for (int rt = 4; rt >= 1; --rt) {
            if (rt == HL_AT) {
                SB();
                hx0 = lds_v32(lb + C_HX + w * 4);
                hl4 = lds_v128(hx + lane * 16);
                hl1 = lds_v32(hx + 1024 + (lane & 31) * 4);
                SB();
            }
            if (rt == 1) {
                SB();
                asm volatile("s_cmp_eq_u32 %2, 0\n\ts_cbranch_scc1 1f\n\ts_waitcnt vmcnt(0)\n1:" : "+v"(ha0), "+v"(hb0) : "s"(far) : "memory", "scc");
                if (half) {
                    if (__builtin_expect(__any(max(max(max(ha0[0], ha0[1]), max(ha0[2], ha0[3])), hb0) == 0xffffffffu), 0)) {
#pragma nounroll
                        for (int it = 0; it < SPIN_CAP; ++it) {
                            NHRETRY();
                            __builtin_amdgcn_s_sleep(HX_SLEEP);
                            HX_LD();
                            asm volatile("s_waitcnt vmcnt(0)" : "+v"(ha0), "+v"(hb0) :: "memory");
                            if (!__any(max(max(max(ha0[0], ha0[1]), max(ha0[2], ha0[3])), hb0) == 0xffffffffu)) break;
                        }
                    }
                    asm volatile("ds_write_b32 %0, %1" ::"v"(hxw), "v"(hb0) : "memory");
                } else {
                    if (__builtin_expect(!__builtin_amdgcn_readfirstlane(hx0), 0)) {
                        spin_ge(lb + C_HX + w * 4, 1);
                        hl4 = lds_v128(hx + lane * 16);
                        hl1 = lds_v32(hx + 1024 + (lane & 31) * 4);
                    }
                    ha0 = hl4;
                    hb0 = hl1;
                }
                kop[0] = (u32x4){ha0[0], ha0[1], hb0, 0u};
                vv[0] = ha0[2];
                vv[1] = ha0[3];
                vx[1] = cat8(TRRD((lds_char*)(size_t)vxa[0]), TRRD((lds_char*)(size_t)(vxa[0] + 128)));
                SB();
            }
            const f32x4 s0 = __builtin_amdgcn_mfma_f32_16x16x32_bf16(__builtin_bit_cast(bf16x8, kop[rt - 1]), __builtin_bit_cast(bf16x8, qop[rt]), cm0, 0, 0, 0);
            const f32x4 s1 = __builtin_amdgcn_mfma_f32_16x16x32_bf16(__builtin_bit_cast(bf16x8, kop[rt]), __builtin_bit_cast(bf16x8, qop[rt]), cm1, 0, 0, 0);
            if (rt == 1 && rb == 0) {
                l0[rt] = fmax_raw(s1[0], s1[1]);
                l1[rt] = fmax_raw(s1[2], s1[3]);
            } else {
                l0[rt] = fmax_raw(fmax3_raw(s0[0], s0[1], s1[0]), s1[1]);
                l1[rt] = fmax_raw(fmax3_raw(s0[2], s0[3], s1[2]), s1[3]);
            }
            mx[rt] = fmax_raw(l0[rt], l1[rt]);
        }
#define FU(v) __float_as_uint(v)
#define UF(v) __uint_as_float(v)
#define RED4(dst, src, OP) { \
            const auto ra_ = __builtin_amdgcn_permlane16_swap(FU(src[1]), FU(src[2]), false, false); \
            const auto rb_ = __builtin_amdgcn_permlane16_swap(FU(src[3]), FU(src[4]), false, false); \
            const auto rc_ = __builtin_amdgcn_permlane32_swap(FU(OP(UF(ra_[0]), UF(ra_[1]))), FU(OP(UF(rb_[0]), UF(rb_[1]))), false, false); \
            dst = OP(UF(rc_[0]), UF(rc_[1])); }
#define BC4(dst, src) { \
            const auto ra_ = __builtin_amdgcn_permlane32_swap(FU(src), FU(src), false, false); \
            const auto rb_ = __builtin_amdgcn_permlane16_swap(ra_[0], ra_[0], false, false); \
            const auto rc_ = __builtin_amdgcn_permlane16_swap(ra_[1], ra_[1], false, false); \
            dst[1] = UF(rb_[0]); dst[2] = UF(rb_[1]); dst[3] = UF(rc_[0]); dst[4] = UF(rc_[1]); }
#define FADD(a, b) ((a) + (b))
        SB(); TS(4); HINT(1); SB();
        float red;
        RED4(red, mx, fmax_raw);
        red *= scale;
        BC4(mx, red);
        f32x2 pp[5];
        float sm[5];
#pragma unroll
        for (int rt = 4; rt >= 1; --rt) {
            pp[rt] = (f32x2){__builtin_amdgcn_exp2f(__builtin_fmaf(l0[rt], scale, -mx[rt])), __builtin_amdgcn_exp2f(__builtin_fmaf(l1[rt], scale, -mx[rt]))};
            sm[rt] = pp[rt][0] + pp[rt][1];
        }
        RED4(red, sm, FADD);
        red = __builtin_amdgcn_rcpf(red);
        BC4(sm, red);
        SB(); TS(5);
        const unsigned ty0 = *(lds_vu32*)(size_t)(lb + C_TY);
        SB();
        f32x4 o1[5], o2 = z4;
#pragma unroll
        for (int rt = 4; rt >= 1; --rt) {
            const unsigned pk = pack2(pp[rt][0] * sm[rt], pp[rt][1] * sm[rt]);
            const u32x4 pop = {__builtin_amdgcn_perm(pk, pk, psel[0]), __builtin_amdgcn_perm(pk, pk, psel[1]), __builtin_amdgcn_perm(pk, pk, psel[2]), __builtin_amdgcn_perm(pk, pk, psel[3])};
            const u32x4 vop = {vv[2 * rt - 2], vv[2 * rt - 1], vv[2 * rt], vv[2 * rt + 1]};
            o1[rt] = __builtin_amdgcn_mfma_f32_16x16x32_bf16(__builtin_bit_cast(bf16x8, vop), __builtin_bit_cast(bf16x8, pop), z4, 0, 0, 0);
            o2 = __builtin_amdgcn_mfma_f32_16x16x32_bf16(vx[rt], __builtin_bit_cast(bf16x8, pop), o2, 0, 0, 0);
        }
        SB(); TS(6);
        u32x4 yv[3];
        unsigned g1;
        const unsigned l16y = lane * 16;
#define G_ASK() asm volatile("global_load_dword %0, %1, %2 sc1" : "=v"(g1) : "v"((lane & 15) * 4), "s"(ws + WS_G + ROWT(w) * 64) : "memory")
        unsigned ty = half ? __builtin_amdgcn_readfirstlane(ty0) : 0;
        const unsigned yearly = __builtin_amdgcn_readfirstlane(YEARLY && ty && (int)(NOW() - ty) >= 0);
        if (yearly) { G_ASK(); Y_FIRST(); }
        SB();
#pragma unroll
        for (int rt = 3; rt >= 1; rt -= 2) {
            const auto r0 = __builtin_amdgcn_permlane16_swap(min(pack2(o1[rt][0], o1[rt][1]), 0xfffffffeu), min(pack2(o1[rt + 1][0], o1[rt + 1][1]), 0xfffffffeu), false, false);
            const auto r1 = __builtin_amdgcn_permlane16_swap(min(pack2(o1[rt][2], o1[rt][3]), 0xfffffffeu), min(pack2(o1[rt + 1][2], o1[rt + 1][3]), 0xfffffffeu), false, false);
            __builtin_amdgcn_raw_buffer_store_b128((u32x4){r0[0], r1[0], r0[1], r1[1]}, wsr, oo + (rt - 1) * 20480, 0, 16);
        }
        __builtin_amdgcn_raw_buffer_store_b64((u32x2){min(pack2(o2[0], o2[1]), 0xfffffffeu), min(pack2(o2[2], o2[3]), 0xfffffffeu)}, wsr, ox, WS_O + rb * 4 * 20480, 16);
        HINT(4);
        TS(7);
        TSFLUSH();
        if (!half || yearly) lds_add(lb + C_OS1, lane);
        if (half) {
            STAMP(4);
            if (!yearly) {
                const bool soon = ty && (int)(NOW() + Y_SOON - ty) >= 0;
                if (!soon) {
                    lds_add(lb + C_OS1, lane);
                    if (w == 0) {
                        spin_mid(lb + C_LK, 1);
                        if (!__builtin_amdgcn_readfirstlane(lds_ld32(lb + C_TY))) {
                            u64 cfm = ~0xffff0000ull;
                            unsigned tv = 0, tu = 0;
                            const unsigned cfo = CF_OFF();
                            CF_LOOP(cfo, ~0ull);
                        }
                    }
                    ty = TY_GET();
                }
                G_ASK();
                Y_TIME(ty);
                Y_FIRST();
                if (soon) lds_add(lb + C_OS1, lane);
            }
            spin_all(lb + C_FREE + (lane & 7) * 4, 18);
            spin_ge(lb + C_WO, 4);
            Q_READ(G_Q);
            spin_ge(lb + C_OST, 4);
            lds_add(lb + C_QR, lane);
            asm volatile("s_cmp_eq_u32 %1, 0\n\ts_cbranch_scc0 1f\n\ts_waitcnt vmcnt(3)\n1:\n\ts_waitcnt vmcnt(6)" : "+v"(g1) : "s"(yearly) : "memory", "scc");
            G_MUL();
            P_STAGE(yearly);
        }
        lds_add(lb + C_OST, lane);
        STAMP(4);
        TAIL_ADDR();
      } else {
        __builtin_amdgcn_s_setprio(L_PRIO);
        unsigned ev[9];
        {
            const int hi = lane >> 5, pos = lane & 31;
#pragma unroll
            for (int v = 0; v < 2; ++v) {
                const int row = 2 * w + hi + 8 * v;
                int L = pos ^ (2 * (v * 4 + (row & 3)));
                L = L > 29 ? L - 2 : L;
                const int part = L < 24 ? L >> 3 : (L - 24) >> 1;
                const int chunk = L < 24 ? (L & 7) + 2 * (hg & 1) : (L & 1) + ((hg & 1) ? 0 : 8);
                ev[v] = row * (NQKV * 2) + chunk * 16 + part * 1280 + hg * 160;
                ev[v + 2] = ev[v] + 16 * NQKV * 2;
            }
            const int ch = (lane & 7) ^ ((4 * (w & 1) + g) & 7);
#pragma unroll
            for (int j = 0; j < 4; ++j) ev[4 + j] = (rb2 * 128 + 8 * w + (lane >> 3) + 32 * j) * (D_DIM * 2) + ch * 16;
            ev[8] = 0;
        }
        ring_issue<4, 0, ring_cum(-1, 4)>(ev, x, wqkv, kr, lb + w * 1024);
        SB();
        STAMP(5);
        unsigned wf[2][3], qf[3];
#pragma unroll
        for (int v = 0; v < 2; ++v)
#pragma unroll
            for (int j = 0; j < 3; ++j) wf[v][j] = opaque(WO_OFF((((w + 2 * v) & 3) + 4 * j) * 64 + lane));
#pragma unroll
        for (int j = 0; j < 3; ++j) { const int c = min(j * 64 + lane, 159); const int row = c / 10, ch = c - row * 10; qf[j] = opaque(row * (D_DIM * 2) + ch * 16); }
        const unsigned sfo = opaque(CF_OFF());
        TAIL_ADDR();
        const unsigned fra = opaque(lb + C_FREE + (lane & 7) * 4);
        SB();
        unsigned c0, r0;
        ring_run17<4, -1>(ev, x, wqkv, kr, lb + w * 1024, c0, r0);
        STAMP(7);
#define WO_PART(chunk, v) { const char* wb_ = (const char*)wo + (size_t)(chunk) * (64 * D_DIM * 2); const unsigned sl_ = lb + ((w + 2 * (v)) & 3) * 1024; \
            constexpr int pos_ = (chunk) < 5 ? G_WO0 + (chunk) * 10240 : G_WO1 + ((chunk) - 5) * 10240; \
            GLS(wf[v][0], wb_, sl_, pos_); GLS(wf[v][1], wb_, sl_, pos_ + 4096); if (((w + 2 * (v)) & 3) < 2) GLS(wf[v][2], wb_, sl_, pos_ + 8192); }
#define FREED(J) { spin_all(fra, J); }
#define Q_PART() { _Pragma("unroll") for (int j = 0; j < 3; ++j) \
            if (j < 2 || lane < 32) GLS(qf[j], (const char*)x + (size_t)__builtin_amdgcn_readfirstlane(ROWT(w) * 16 * (D_DIM * 2) + oc * 160), lb + w * 2560, G_Q + j * 1024); }
        if (w == 2) {
            const unsigned t_ = NOW();
            const float f_ = TICKS_PER_CYCLE(t_, c0, r0);
            if (lane == 0) __builtin_amdgcn_raw_buffer_store_b128((u32x4){tlo, thi, t_ + (unsigned)(f_ * D0), t_ + (unsigned)(f_ * D1)}, wsr, WS_HN + (rb2 * 8 + hg) * 128, 0, 16);
            lds_st32(lb + C_TR + 4, t_ + (unsigned)(f_ * D0) + FB_T);
            lds_st32(lb + C_TR + 8, t_ + (unsigned)(f_ * D1) + FB_T);
        }
        u32x2 ov;
        const unsigned owo = WS_HF + (lane < 8 ? rb2 * 8 + lane : min(rb2 + 1, 15) * 8 + hg) * 128;
        if (w == 1) asm volatile("s_mov_b64 exec, 0x1ff\n\tglobal_load_dwordx2 %0, %1, %2 sc1\n\ts_mov_b64 exec, -1" : "=v"(ov) : "v"(owo), "s"(ws) : "memory");
        WO_PART(0, 0);
        WO_PART(5, 1);
        WAIT_VM(9);
        BARRIER();
        WO_PART(1, 0);
        WO_PART(2, 1);
        asm volatile("s_waitcnt vmcnt(10)" : "+v"(ov) :: "memory");
        const bool own_ok = w != 1 || !(__builtin_amdgcn_ballot_w64(ov[0] != tlo || ov[1] != thi) & 0x1ff);
        if (w == 1 && own_ok) lds_st32(lb + C_OWN, 1);
        BARRIER();
        if (!own_ok) {
            poll_flags(wsr, owo, tlo, thi);
            lds_st32(lb + C_OWN, 1);
        }
        __builtin_amdgcn_s_setprio(WO_PRIO);
        STAMP(2);
        if (HS_POS == 0) spin_mid(lb + C_HS, HS_N);
        WO_PART(3, 0);
        WO_PART(4, 1);
        if (HS_POS == 1) spin_mid(lb + C_HS, HS_N);
        u32x4 fv;
        u64 cfm = ~0ull << 40;
        unsigned tv = 0, tu = 0;
#define LOOK_LD() if (w == 3) asm volatile("s_mov_b32 exec_hi, 0xff\n\tglobal_load_dwordx4 %0, %1, %2 sc1\n\ts_mov_b32 exec_hi, -1" : "=v"(fv) : "v"(sfo), "s"(ws) : "memory")
        if (LOOK_AT == 0) LOOK_LD();
        WO_PART(6, 0);
        WO_PART(7, 1);
        if (HS_POS == 2) spin_mid(lb + C_HS, HS_N);
        if (LOOK_AT == 1) LOOK_LD();
        spin_all(fra, 17);
        WO_PART(8, 0);
        WO_PART(9, 1);
        if (LOOK_AT == 2) LOOK_LD();
        Q_PART();
        STAMP(3);
        if (w == 3) {
            tv = lds_ld32(lb + C_TR + 4) | 1;
            tu = lds_ld32(lb + C_TR + 8) | 1;
            cfm |= 0xffull << 32;
            asm volatile("s_waitcnt vmcnt(%1)" : "+v"(fv) : "n"(13 - 5 * LOOK_AT) : "memory");
            CF_LOOK(fv);
        }
        spin_all(fra, 18);
        if (w == 3 && (~cfm & 0xffff)) {
            WAIT_VM(0);
            lds_add(lb + C_WO, lane);
            wo_open = false;
            CF_LOOP(sfo, 0xffffull);
        }
        if (w == 3) lds_st32(lb + C_LK, 1);
        STAMP(14);
        STAMP(4);
      }
    } else {
        __builtin_amdgcn_s_setprio(3);
        const int c5 = wave < 4 ? wave : 4;
        const int d = vid * 5 + c5;
        unsigned Dw = *(const __attribute__((address_space(4))) unsigned*)(size_t)(ssm_D + (d & ~1));
        unsigned hp0 = *(const __attribute__((address_space(4))) unsigned*)(size_t)(ssm_k + (vid * 5 + 4) * S_LEN + w * 512 - 2);
        BARRIER();
        if (wave <= 4) hp0 = 0;
        asm volatile("" : "+s"(Dw), "+s"(hp0));
#define WO_CHUNK(chunk, lds) { const char* wb_ = (const char*)wo + (size_t)(chunk) * (64 * D_DIM * 2); \
            _Pragma("unroll") for (int p = 0; p < 10; ++p) GLS(wof[p], wb_, lds, p * 1024); }
#define KV_DONE() do { if (lds_add_rtn(lb + C_KVD, lane) == 9) { \
            if (lane == 0) __hip_atomic_store((u64*)(ws + WS_XF + (cq * 32 + rbv) * 128), tag, __ATOMIC_RELAXED, __HIP_MEMORY_SCOPE_AGENT); lds_add(lb + C_KVD, lane); } } while (0)
        u32x4 hv[4];
        const unsigned l16 = lane * 16;
        const char* hsrc = (const char*)ssm_k + (size_t)d * (S_LEN * 2) + (wave < 4 ? 0 : w * 1024);
#define H_LD(i) asm volatile("global_load_dwordx4 %0, %1, %2 offset:%3" : "=v"(hv[i]) : "v"(l16), "s"(hsrc), "n"((i) * 1024) : "memory")
        if (wave < 10) {
            const int m0 = (g & 1) * 2 + (g >> 1);
            const unsigned kp = opaque(lb + V_KST + (8 * m0 + q4) * 320 + (m0 >> 1) * 32 + wave * 32 + p4 * 8);
            const unsigned km = opaque(kp - 1280 - ((m0 & 1) ? 0 : 32));
            const unsigned sw_ = opaque(lb + V_STG + wave * 2048 + n16 * 128 + ((m0 + n16) & 7) * 16);
            const unsigned sr_ = opaque(lb + V_STG + wave * 2048 + (lane >> 3) * 128 + ((lane + (lane >> 3)) & 7) * 16);
            const unsigned e1 = opaque(WS_KV + (cq * 160 + wave * 16 + (lane >> 3)) * KV_PITCH + rbv * 128 + (lane & 7) * 16), e2 = opaque(e1 + 8 * KV_PITCH);
            asm volatile("s_waitcnt vmcnt(0)" : "+v"(skw) :: "memory");
            lds_add(lb + C_KV, lane);
            const f32x2 s0 = {lo_f(skw[0]), lo_f(skw[0])}, s1 = {hi_f(skw[0]), hi_f(skw[0])}, s2 = {lo_f(skw[1]), lo_f(skw[1])}, s3 = {hi_f(skw[1]), hi_f(skw[1])};
            if (H_EARLY == 1 && wave < 8) { H_LD(0); if (wave < 4) { H_LD(1); H_LD(2); H_LD(3); } }
            spin_ge(lb + C_KV, 10);
            if (H_EARLY == 2 && wave < 8) { H_LD(0); if (wave < 4) { H_LD(1); H_LD(2); H_LD(3); } }
            STAMP(2);
            u32x2 kq[2][3], vq[2][2];
#pragma unroll
            for (int i = 0; i < 2; ++i) {
                kq[i][0] = __builtin_bit_cast(u32x2, TRRD((lds_char*)(size_t)(km + i * 10304)));
#pragma unroll
                for (int e = 0; e < 2; ++e) kq[i][e + 1] = __builtin_bit_cast(u32x2, TRRD((lds_char*)(size_t)(kp + i * 10304 + e * 1280)));
#pragma unroll
                for (int e = 0; e < 2; ++e) vq[i][e] = __builtin_bit_cast(u32x2, TRRD((lds_char*)(size_t)(kp + i * 10304 + (V_VST - V_KST) + e * 1280)));
            }
            if (rbv == 0 && m0 == 0) kq[0][0] = (u32x2){0u, 0u};
            f32x2 kf[12], res[8];
#pragma unroll
            for (int e = 1; e < 12; ++e) {
                const unsigned u0 = kq[0][e >> 2][(e >> 1) & 1], u1 = kq[1][e >> 2][(e >> 1) & 1];
                kf[e] = (e & 1) ? (f32x2){hi_f(u0), hi_f(u1)} : (f32x2){lo_f(u0), lo_f(u1)};
            }
#pragma unroll
            for (int e = 0; e < 8; ++e) {
                const unsigned u0 = vq[0][e >> 2][(e >> 1) & 1], u1 = vq[1][e >> 2][(e >> 1) & 1];
                f32x2 a = kf[e + 1] * s3;
                a = kf[e + 2] * s2 + a;
                a = kf[e + 3] * s1 + a;
                a = kf[e + 4] * s0 + a;
                res[e] = a * ((e & 1) ? (f32x2){hi_f(u0), hi_f(u1)} : (f32x2){lo_f(u0), lo_f(u1)});
            }
#pragma unroll
            for (int i = 0; i < 2; ++i) {
                u32x4 pk;
#pragma unroll
                for (int e = 0; e < 4; ++e) pk[e] = pack2(res[2 * e][i], res[2 * e + 1][i]);
                *(lds_vu32x4*)(size_t)(sw_ ^ (i * 64)) = pk;
            }
            const u32x4 pk0 = lds_v128(sr_), pk1 = lds_v128(sr_ + 1024);
            __builtin_amdgcn_raw_buffer_store_b128(pk0, wsr, e1, 0, 16);
            __builtin_amdgcn_raw_buffer_store_b128(pk1, wsr, e2, 0, 16);
            lds_add(lb + C_KVST, lane);
            STAMP(3);
        }
        if (wave >= 8) {
            const unsigned wv = T_WV(), xv_ = T_XV();
            unsigned wof[10], qf[3];
#define AUX_ADDR() { _Pragma("unroll") for (int p = 0; p < 10; ++p) wof[p] = opaque(WO_OFF(p * 64 + lane)); \
                _Pragma("unroll") for (int j = 0; j < 3; ++j) { const int c = min(j * 64 + lane, 159); const int row = c / 10, ch = c - row * 10; qf[j] = opaque(row * (D_DIM * 2) + ch * 16); } \
                SB(); }
            if (wave == 10) {
                T_PAIR(2);
                AUX_ADDR();
                if (T4_W) spin_ge(lb + T4_W, T4_N);
                T_PAIR(4);
                if (WO_MALL && lane < 50) GLT((unsigned)((vid * 50 + lane) * 128), wo);
            } else if (wave == 11) {
                if (lane < 56) {
#pragma unroll
                    for (int c5 = 0; c5 < 5; ++c5)
                        *(lds_u32x4*)(size_t)(lb + (lane < 16 ? V_KV(c5) + lane * 16 : V_F + c5 * XC_BYTES + 15 * XC_ROW + 256 + (lane < 36 ? XC_COPY + (lane - 16) * 16 : XC_B + 4 + (lane - 36) * 16))) = (u32x4){0u, 0u, 0u, 0u};
                }
                const unsigned x4o = opaque(WS_KV + d * KV_PITCH + lane * 16);
                AUX_ADDR();
                spin_ge(lb + C_KVD, 11);
                if (POLL_DELAY > 0) __builtin_amdgcn_s_sleep(POLL_DELAY);
                const unsigned xfo = WS_XF + ((cq ^ CQ_X) * 32 + (lane & 31)) * 128;
                STAMP(3);
                const u32x2 fv = __builtin_amdgcn_raw_buffer_load_b64(wsr, xfo, 0, XF_AUX);
                const bool ok0_ = __all(fv[0] == tlo && fv[1] == thi);
                STAMP(4);
                if (!ok0_) {
                    lds_st32(lb + C_GO, 1);
#pragma nounroll
                    for (int i = 0; i < SPIN_CAP; ++i) {
                        asm volatile("" ::: "memory");
                        const u32x2 v = __builtin_amdgcn_raw_buffer_load_b64(wsr, xfo, 0, XF_AUX);
                        if (__all(v[0] == tlo && v[1] == thi)) break;
                        __builtin_amdgcn_s_sleep(POLL_SLEEP);
                    }
                }
                asm volatile("s_mov_b32 m0, %2\n\ts_nop 0\n\tglobal_load_lds_dwordx4 %0, %1 sc1\n\tglobal_load_lds_dwordx4 %0, %1 offset:1024 sc1\n\t"
                             "global_load_lds_dwordx4 %0, %1 offset:2048 sc1\n\tglobal_load_lds_dwordx4 %0, %1 offset:3072 sc1" ::"v"(x4o), "s"(ws), "s"(lb + V_KV4 + 256) : "memory");
                lds_add(lb + C_KVR, lane);
                STAMP(2);
                WAIT_VM(0);
                lds_st32(lb + C_TR, NOW());
                lds_st32(lb + C_TR + 4, (unsigned)__builtin_amdgcn_s_memtime());
                lds_st32(lb + C_HR, 4);
                STAMP(12);
                spin_ge(lb + C_XD, 4);
                lds_st32(lb + C_GO, 1);
            } else {
                WAIT_VM(0);
                KV_DONE();
                STAMP(11);
                const unsigned c8 = (wave - 8) * 80 + (lane >> 3), so = opaque(WS_Y + vid * 640 + (lane & 7) * 16);
                unsigned sy[10];
#pragma unroll
                for (int i = 0; i < 10; ++i) { const unsigned c = c8 + i * 8, b = c / 5; sy[i] = opaque(so + b * (D_DIM * 128) + (c - b * 5) * 128); }
#define O_SENT() { _Pragma("unroll") for (int i = 0; i < (X_NOOS ? 0 : 10); ++i) __builtin_amdgcn_raw_buffer_store_b128(ones, wsr, WS_O + sid * 20480 + (wave - 8) * 10240 + lane * 16 + i * 1024, 0, 16); \
                if (lane < 2) __builtin_amdgcn_raw_buffer_store_b128(ones, wsr, WS_G + sid * 64 + (wave - 8) * 32 + lane * 16, 0, 16); \
                _Pragma("unroll") for (int i = 0; i < 3; ++i) if (i < 2 || lane < 32) __builtin_amdgcn_raw_buffer_store_b128(ones, wsr, WS_HX + sid * 5120 + (wave - 8) * 2560 + lane * 16 + i * 1024, 0, 16); }
#define O_FLAG() { if (lds_add_rtn(lb + C_OSC, lane) == 1) { if (lane == 0) __hip_atomic_store((u64*)(ws + WS_HF + sid * 128), tag, __ATOMIC_RELAXED, __HIP_MEMORY_SCOPE_AGENT); } }
                if (OS_EARLY) { spin_ge(lb + C_KVD, 11); if (OS_DELAY > 0) __builtin_amdgcn_s_sleep(OS_DELAY); O_SENT(); }
                AUX_ADDR();
                if (OS_EARLY) { WAIT_VM(0); O_FLAG(); }
                spin_ge(lb + SENT_W, SENT_N);
                if (!OS_EARLY) O_SENT();
#pragma unroll
                for (int i = 0; i < (X_NOYS ? 0 : 10); ++i) __builtin_amdgcn_raw_buffer_store_b128(ones, wsr, sy[i], 0, 16);
                STAMP(4);
                if (!OS_EARLY) { WAIT_VM(10); O_FLAG(); }
                if (WO2_FIRST) { WO_CHUNK(2 + w, lb + V_WO + (2 + w) * 10240); WAIT_VM(10); } else WAIT_VM(0);
                if (lds_add_rtn(lb + C_OS1, lane) == 1) {
                    lds_st32(lb + C_OWN, 1);
                    spin_ge(lb + C_HR, 4);
                    const unsigned r0 = lds_ld32(lb + C_TR), c0 = lds_ld32(lb + C_TR + 4);
#pragma nounroll
                    for (int it = 0; it < 1000 && (int)(NOW() - __builtin_amdgcn_readfirstlane(r0)) < 60; ++it) __builtin_amdgcn_s_sleep(2);
                    const unsigned t_ = NOW();
                    const float f_ = TICKS_PER_CYCLE(t_, c0, r0);
                    if (lane == 0) __builtin_amdgcn_raw_buffer_store_b128((u32x4){tlo, thi, r0 + (unsigned)(f_ * DY), 0u}, wsr, WS_SF + vid * 128, 0, 16);
                }
                STAMP(12);
            }
            __builtin_amdgcn_s_setprio(AUX_PRIO);
            if (wave == 10) {
                if (!X_TALL) spin_ge(lb + GO_W, GO_N);
                T_PAIR(6);
                if (T8_AT == 1) WAIT_VM(1); else if (T8_AT == 2) spin_ge(lb + C_XD, 4);
                T_PAIR(8);
                spin_mid(lb + WOT_W, WOT_N);
#pragma unroll
                for (int i = 0; i < 4; ++i) {
                    const int l0 = r * 240 + i * 60 + lane;
                    waitvm<WOT_PACE>();
                    if (lane < 60) GLT((unsigned)((l0 < 1280 ? l0 : l0 + 2560) * 128), wo);
                }
                {
                    const unsigned cl = lane * 16 + r;
                    if (cl < CODE_LINES) GLT(cl * 128, code0);
                }
                WAIT_VM(0);
                lds_st32(lb + C_WT, 1);
                STAMP(4);
                WO_CHUNK(4, lb + V_WO + 4 * 10240);
            }
            if (wave == 11 || (wave < 10 && !WO2_FIRST)) {
                spin_mid(lb + WO2_W, WO2_N);
                WO_CHUNK(2 + w, lb + V_WO + (2 + w) * 10240);
            }
#pragma unroll
            for (int j = 0; j < 3; ++j)
                if (j < 2 || lane < 32) GLS(qf[j], (const char*)x + (size_t)(ROWT(w) * 16) * (D_DIM * 2) + oc * 160, lb + w * 2560, V_QS + j * 1024);
            WAIT_VM(0);
            STAMP(5);
            Q_READ(V_QS);
            lds_add(lb + C_QR, lane);
            if (w < 2) {
                spin_mid(lb + (w ? C_QR : C_RED2), 4);
                spin_mid(lb + C_WT, 1);
                WO_CHUNK(w, lb + V_WO + w * 10240);
            }
            WAIT_VM(0);
            lds_add(lb + C_WO, lane);
            STAMP(6);
            if (wave == 10) {
                u64 cfm = ~0ull << 40;
                unsigned tv = 0, tu = 0;
                const unsigned cfo = CF_OFF();
                if (VL_DELAY > 0) __builtin_amdgcn_s_sleep(VL_DELAY);
                CF_LOOP(cfo, ~0ull);
                STAMP(10);
            }
            const unsigned ty = TY_GET();
            G_FIRST();
            G_MUL();
            spin_mid(lb + C_CONVN, 8);
            __builtin_amdgcn_s_setprio(3);
            u32x4 yv[3];
            const unsigned l16y = lane * 16;
            Y_TIME(ty);
            Y_FIRST();
            STAMP(7);
            P_STAGE(false);
        } else {
            const unsigned xb = lb + V_F + c5 * XC_BYTES;
            const unsigned hb = lb + V_KV(c5) + 256;
            const float Dd = (d & 1) ? hi_f(Dw) : lo_f(Dw);
            if (H_EARLY) WAIT_VM(0);
            else {
                spin_ge(lb + C_KVST, 10);
                H_LD(0);
                if (wave < 4) { H_LD(1); H_LD(2); H_LD(3); WAIT_VM(4); } else WAIT_VM(1);
            }
            KV_DONE();
            STAMP(4);
            {
                const unsigned da = xb + (3 - g) * XC_ROW + (15 - n16) * 16 + (wave < 4 ? 0 : -w * (4 * XC_ROW));
                u32x4 ra[4], rb[4];
                asm volatile("s_waitcnt vmcnt(0)" : "+v"(hv[0]), "+v"(hv[1]), "+v"(hv[2]), "+v"(hv[3]) :: "memory");
#define H_PREV(i) __builtin_amdgcn_update_dpp((i) == 0 ? hp0 : __builtin_amdgcn_readlane(hv[(i) == 0 ? 0 : (i) - 1][3], 63), hv[i][3], 0x138, 0xf, 0xf, false)
#define BFI16(lo, hi) (((lo) & 0xffffu) | ((hi) & 0xffff0000u))
#define H_PREP(i) { ra[i] = (u32x4){__builtin_amdgcn_alignbit(hv[i][3], hv[i][3], 16), __builtin_amdgcn_alignbit(hv[i][2], hv[i][2], 16), __builtin_amdgcn_alignbit(hv[i][1], hv[i][1], 16), __builtin_amdgcn_alignbit(hv[i][0], hv[i][0], 16)}; \
                    rb[i] = (u32x4){BFI16(hv[i][3], hv[i][2]), BFI16(hv[i][2], hv[i][1]), BFI16(hv[i][1], hv[i][0]), BFI16(hv[i][0], H_PREV(i))}; \
                    *(lds_u32x4*)(size_t)(da + (3 - (i)) * (4 * XC_ROW) + XC_COPY) = ra[i]; *(lds_u32x4*)(size_t)(da + (3 - (i)) * (4 * XC_ROW) + XC_B + 4) = rb[i]; }
#define H_DUP(i) { *(lds_u32x4*)(size_t)(da + (3 - (i)) * (4 * XC_ROW) + XC_COPY - 32) = ra[i]; *(lds_u32x4*)(size_t)(da + (3 - (i)) * (4 * XC_ROW) + XC_B + 4 - 32) = rb[i]; }
                H_PREP(0)
                if (wave < 4) {
                    H_PREP(1) H_PREP(2) H_PREP(3)
                    if (n16 == 15) { H_DUP(0) H_DUP(1) H_DUP(2) if (g < 3) H_DUP(3) }
                } else {
                    if (n16 == 15 && (w < 3 || g < 3)) H_DUP(0)
                }
            }
            STAMP(9);
            const int j0 = wave >= 4 ? 2 * w : 0;
            const int m16 = HYB ? n16 : (n16 & 3) | ((n16 & 4) << 1) | ((n16 & 8) >> 1);
            const unsigned kp = hb + 2 * (256 * j0 + 128 * (g >> 1) + 8 * m16 + 4 * (g & 1));
            const int ts = 256 * (j0 + (g & 1)) + 128 * (g >> 1) + 8 * m16;
            const unsigned yo = WS_Y + d * 128 + (HYB ? (ts >> 6) * (D_DIM * 128) + (ts & 63) * 2 : ((ts >> 7) * 2 + ((ts >> 5) & 1)) * (D_DIM * 128) + (((ts >> 6) & 1) * 32 + (ts & 31)) * 2);
            if (wave < 4) {
                const int b1 = n16 >> 3, phi = n16 & 7;
                const unsigned xp = xb + ((phi & 1) ? XC_COPY - 4 : XC_B) + (1 - b1) * XC_ROW + 2 * (8 - 2 * (phi >> 1) + 8 * g);
                const unsigned hp = hb + 2 * (8 * (m16 + g) - 120);
                const int sx4 = (4 - w) * XC_BYTES + (w >> 1) * XC_ROW + (w & 1) * 128, sh4 = V_KV4 - w * KV_BYTES + w * 128;
                unsigned c0_, c1_;
                __builtin_amdgcn_s_setprio(CONV_PRIO);
                spin_ge(lb + C_KVR, 5);
                STAMP(6);
                CSTAMP(0);
                TS(11);
                asm volatile(CONV_ASM
                             : [cnt] "=&s"(c0_), [tmp] "=&s"(c1_)
                             : [xp] "v"(xp), [hp] "v"(hp), [sx4] "s"(sx4), [sh4] "s"(sh4), [kvl] "s"(hb), [kvr] "v"(kp), [scr] "v"(lb + V_SCR + w * 8192 + l16),
                               [ctl] "v"(lb + L_CTRL), [dd] "s"(Dd), [cap] "s"(SPIN_CAP), [xo] "v"(WS_KV + d * KV_PITCH + l16), [wsp] "s"(ws), [yo] "v"(yo)
                             : CONV_CLOB);
                __builtin_amdgcn_s_setprio(3);
                TAIL_ADDR();
                CSTAMP(1);
            } else {
                lds_add(lb + C_KVR, lane);
                const int grow = sid * 16 + w * 4 + g;
                const unsigned go = opaque(grow * (D_DIM * 2) + n16 * 16);
                u32x4 gx[5], gv[5];
#pragma unroll
                for (int i = 0; i < 5; ++i) gv[i] = *(const u32x4*)((const char*)gw + n16 * 16 + i * 256);
                unsigned wof[10];
#pragma unroll
                for (int p = 0; p < 10; ++p) wof[p] = opaque(WO_OFF(p * 64 + lane));
                TAIL_ADDR();
                SB();
                __builtin_amdgcn_s_setprio(AUX_PRIO);
                {
                    if (GATE_LATE) spin_mid(lb + GATE_W, GATE_N);
#pragma unroll
                    for (int i = 0; i < 5; ++i) gx[i] = X_NOG ? gv[i] : *(const u32x4*)((const char*)x + go + i * 256);
                    float sa[4] = {0.f, 0.f, 0.f, 0.f};
#pragma unroll
                    for (int i = 0; i < 5; ++i)
#pragma unroll
                        for (int e = 0; e < 4; ++e) sa[e] = __builtin_fmaf(hi_f(gx[i][e]), hi_f(gv[i][e]), __builtin_fmaf(lo_f(gx[i][e]), lo_f(gv[i][e]), sa[e]));
                    float sacc = (sa[0] + sa[1]) + (sa[2] + sa[3]);
                    sacc += __builtin_amdgcn_update_dpp(0.f, sacc, 0x128, 0xf, 0xf, true);
                    sacc += __builtin_amdgcn_update_dpp(0.f, sacc, 0x124, 0xf, 0xf, true);
                    sacc += __builtin_amdgcn_update_dpp(0.f, sacc, 0x122, 0xf, 0xf, true);
                    sacc += __builtin_amdgcn_update_dpp(0.f, sacc, 0x121, 0xf, 0xf, true);
                    const float gres = rbf(__builtin_amdgcn_rcpf(1.f + __expf(-rbf(sacc))));
                    spin_ge(lb + C_OSC, 2);
                    if (n16 == 0) __builtin_amdgcn_raw_buffer_store_b32(__float_as_uint(gres), wsr, WS_G + grow * 4, 0, 16);
                    STAMP(6);
                }
                spin_ge(lb + C_RED, 4);
                STAMP(5);
                f32x4 acc[2];
#pragma unroll
                for (int e = 0; e < 2; ++e) {
                    const u32x2 kk = *(const lds_u32x2*)(size_t)(kp + e * 512);
                    acc[e] = (f32x4){lo_f(kk[0]) * Dd, hi_f(kk[0]) * Dd, lo_f(kk[1]) * Dd, hi_f(kk[1]) * Dd};
#pragma unroll
                    for (int o = 0; o < 4; ++o) acc[e] += *(const lds_f32x4*)(size_t)(lb + V_SCR + o * 8192 + (2 * w + e) * 1024 + l16);
                }
                lds_add(lb + C_RED2, lane);
                const auto r0 = __builtin_amdgcn_permlane16_swap(min(pack2(acc[0][0], acc[0][1]), 0xfffffffeu), min(pack2(acc[1][0], acc[1][1]), 0xfffffffeu), false, false);
                const auto r1 = __builtin_amdgcn_permlane16_swap(min(pack2(acc[0][2], acc[0][3]), 0xfffffffeu), min(pack2(acc[1][2], acc[1][3]), 0xfffffffeu), false, false);
                spin_mid(lb + C_OWN, 1);
                __builtin_amdgcn_raw_buffer_store_b128((u32x4){r0[0], r1[0], r0[1], r1[1]}, wsr, yo, 0, 16);
                STAMP(7);
                spin_ge(lb + C_RED2, 4);
                spin_mid(lb + C_WT, 1);
                WO_CHUNK(6 + w, lb + V_WO + (6 + w) * 10240);
                WAIT_VM(0);
                lds_add(lb + C_WO, lane);
            }
            lds_add(lb + C_CONVN, lane);
            STAMP(12);

        }
    }

#define NPOLL(ph, it)
#define NRETRY()
#define V_WAITC 4
#define W0_DELAY 0
#define W1_DELAY 0
#define H_DELAY 0
#define WA0 2
#define WA1 7
#define PRE_PRIO 0
#define PASS1_PRIO 2
    {
        FLUSH_LO();
        const f32x4 z4 = {0.f, 0.f, 0.f, 0.f};
        STAMP(12);
        __builtin_amdgcn_s_setprio(ENT_PRIO);
        if (wo_open) { WAIT_VM(0); lds_add(lb + C_WO, lane); }
        unsigned ta, tb;
#pragma nounroll
        for (int it = 0; it < SPIN_CAP; ++it) {
            u32x2 t_;
            asm volatile("ds_read_b64 %0, %1\n\ts_waitcnt lgkmcnt(0)" : "=v"(t_) : "v"(lb + C_TM + DUE_H * 16 + kh * 8) : "memory");
            ta = __builtin_amdgcn_readfirstlane(t_[0]); tb = __builtin_amdgcn_readfirstlane(t_[1]);
            if (ta && tb) break;
            __builtin_amdgcn_s_sleep(3);
        }
        const bool sw = (int)(ta - tb) > 0;
        const unsigned t0 = (sw ? tb : ta) - (isG ? G_LEAD : 0), t1 = (sw ? ta : tb) - (isG ? G_LEAD : 0);
        const int k0 = sw * 5120, k1 = 5120 - k0;
        spin_mid(lb + C_WO, isG ? 4 : 8);
        lds_char* bc[2] = {(lds_char*)(size_t)(pc + k0 * 5), (lds_char*)(size_t)(pc + k1 * 5)};
        lds_char* bd[2] = {(lds_char*)(size_t)(pd + k0 * 5), (lds_char*)(size_t)(pd + k1 * 5)};
#define WO_RD(s, ct) cat8(TRRD(bc[(s) / 5] + ((s) % 5) * 5120 + (ct) * 32), TRRD(bd[(s) / 5] + ((s) % 5) * 5120 + (ct) * 32))
#define WO_PRE(s0) { _Pragma("unroll") for (int s = s0; s < s0 + NBUF; ++s) _Pragma("unroll") for (int ct = 0; ct < 5; ++ct) wB[s % NBUF][ct] = WO_RD(s, ct); SB(); }
        bf16x8 wB[NBUF][5];
        WO_PRE(0);
        STAMP(14);
#define T_WAIT(t) { _Pragma("nounroll") for (int it = 0; it < 20000 && (int)(NOW() - (t)) < 0; ++it) {} }
        T_WAIT(t0);
        if (isG) spin_ge(lb + C_OS1, 8);
        __builtin_amdgcn_s_setprio(T_PRIO);
        const unsigned m = __builtin_amdgcn_readfirstlane((int)(NOW() + T_JOIN - t1) >= 0 ? 15 : 3);
        STAMP(8);
        u32x4 of[10];
        const char *ws0 = ws + k0, *ws1 = ws + k1;
#define O_LD(s) asm volatile("global_load_dwordx4 %0, %1, %2 offset:%3 sc1" : "=v"(of[s]) : "v"(ofo), "s"((s) < 5 ? ws0 : ws1), "n"(((s) % 5) * 1024 - 2048) : "memory")
#define O_WAIT(s) { if ((s) >= 5) asm volatile("s_waitcnt vmcnt(%1)" : "+v"(of[s]) : "n"(9 - (s)) : "memory"); \
            else asm volatile("s_cmp_eq_u32 %1, 15\n\ts_cbranch_scc1 1f\n\ts_waitcnt vmcnt(%2)\n1:\n\ts_waitcnt vmcnt(%3)" : "+v"(of[s]) : "s"(m), "n"(4 - (s) % 5), "n"(9 - (s)) : "memory", "scc"); }
#pragma unroll
        for (int s = 0; s < 5; ++s) O_LD(s);
        if (m == 15) {
#pragma unroll
            for (int s = 5; s < 10; ++s) O_LD(s);
        }
        __builtin_amdgcn_s_waitcnt(0xc07f);
        SB();
        if (ph) __builtin_amdgcn_s_setprio(PASS1_PRIO); else __builtin_amdgcn_s_setprio(PASS_PRIO);
        STAMP(9);
        f32x4 acc[2][5];
#define GROUP(i) { _Pragma("unroll") for (int s = 5 * i; s < 5 * i + 5; ++s) _Pragma("unroll") for (int ct = 0; ct < 5; ++ct) { \
                if (ct == 0) O_WAIT(s); \
                acc[i][ct] = __builtin_amdgcn_mfma_f32_16x16x32_bf16(wB[s % NBUF][ct], __builtin_bit_cast(bf16x8, of[s]), s % 5 ? acc[i][ct] : z4, 0, 0, 0); SB(); \
                if (s + NBUF < 10) { wB[s % NBUF][ct] = WO_RD(s + NBUF, ct); SB(); } } }
#define AGAIN(i) if (__builtin_expect(__any(acc[i][0][0] != acc[i][0][0]), 0)) { \
            NRETRY(); \
            _Pragma("unroll") for (int s = 5 * i; s < 5 * i + 5; ++s) O_LD(s); \
            WO_PRE(5 * i); \
            _Pragma("nounroll") \
            for (int it = 0; it < SPIN_CAP; ++it) { \
                asm volatile("s_waitcnt vmcnt(0)" : "+v"(of[5 * i]), "+v"(of[5 * i + 1]), "+v"(of[5 * i + 2]), "+v"(of[5 * i + 3]), "+v"(of[5 * i + 4]) :: "memory"); \
                bool any_ = false; \
                _Pragma("unroll") for (int s = 5 * i; s < 5 * i + 5; ++s) if (SENT4(of[s])) { \
                    asm volatile("global_load_dwordx4 %0, %1, %2 offset:%3 sc1" : "+v"(of[s]) : "v"(ofo), "s"((s) < 5 ? ws0 : ws1), "n"(((s) % 5) * 1024 - 2048) : "memory"); any_ = true; } \
                if (!any_) break; \
            } \
            _Pragma("unroll") for (int s = 5 * i; s < 5 * i + 5; ++s) _Pragma("unroll") for (int ct = 0; ct < 5; ++ct) { \
                acc[i][ct] = __builtin_amdgcn_mfma_f32_16x16x32_bf16(wB[s % NBUF][ct], __builtin_bit_cast(bf16x8, of[s]), s % 5 ? acc[i][ct] : z4, 0, 0, 0); SB(); \
                if (s + NBUF < 5 * i + 5) { wB[s % NBUF][ct] = WO_RD(s + NBUF, ct); SB(); } } }
        GROUP(0);
        if (m != 15) {
            T_WAIT(t1);
#pragma unroll
            for (int s = 5; s < 10; ++s) O_LD(s);
            __builtin_amdgcn_s_waitcnt(0xc07f);
            SB();
        }
        STAMP(10);
        unsigned fr = 4;
        if (!isG || ph) fr = lds_v32(lb + (isG ? C_QR : C_CONVN));
        SB();
        GROUP(1);
        AGAIN(0);
        AGAIN(1);
        __builtin_amdgcn_s_setprio(T_PRIO);
        STAMP(11);
        if (__builtin_amdgcn_readfirstlane(fr) < (isG ? 4 : 8)) spin_ge(lb + (isG ? C_QR : C_CONVN), isG ? 4 : 8);
#pragma unroll
        for (int ct = 0; ct < 5; ++ct) *(lds_f32x4*)(size_t)(xa + ct * 1024) = acc[0][ct] + acc[1][ct];
        lds_add(lb + C_XCH + trt * 4, lane);
        FLUSH();
    }
}

#undef x
#undef kin
#undef vin
#undef shift_k
#undef ssm_k
#undef ssm_D
#undef wqkv
#undef wo
#undef gw
#undef ws
#undef out
#undef wsr
extern "C" void kernel_launch(void* const* d_in, const int* in_sizes, int n_in, void* d_out, int out_size,
                              void* d_ws, size_t ws_size, hipStream_t stream) {
    const u16* q = (const u16*)d_in[0];
    const u16* k = (const u16*)d_in[1];
    const u16* v = (const u16*)d_in[2];
    const u16* shift_k = (const u16*)d_in[3];
    const u16* ssm_k = (const u16*)d_in[4];
    const u16* ssm_D = (const u16*)d_in[5];
    const u16* wqkv = (const u16*)d_in[7];
    const u16* wo = (const u16*)d_in[8];
    const u16* gw = (const u16*)d_in[9];
    fused_kernel<<<256, 768, 0, stream>>>(q, wqkv, k, v, shift_k, ssm_k, ssm_D, wo, gw, (char*)d_ws, (u16*)d_out);
}
```

```cpp
#define CODE_LINES 374
#define V_DELAY 6
#define MAP1 1
#define HINT_AT -1
#define VY_DELAY 10
#define GY_DELAY 4
#include <hip/hip_runtime.h>
#include <hip/hip_bf16.h>
#include <stdint.h>

#define S_LEN 2048
#define D_DIM 640
#define NQKV 1920

typedef unsigned short u16;
typedef unsigned long long u64;
typedef __attribute__((ext_vector_type(8))) short bf16x8;
typedef __attribute__((ext_vector_type(4))) short s16x4;
typedef __attribute__((ext_vector_type(4))) float f32x4;
typedef __attribute__((ext_vector_type(2))) float f32x2;
typedef __attribute__((ext_vector_type(4))) unsigned u32x4;
typedef __attribute__((ext_vector_type(2))) unsigned u32x2;
typedef __attribute__((ext_vector_type(2))) __bf16 bf16x2_t;
typedef __attribute__((address_space(3))) s16x4 lds_s16x4;
typedef __attribute__((address_space(3))) const s16x4 lds_cs16x4;
typedef __attribute__((address_space(3))) const bf16x8 lds_cbf16x8;
typedef __attribute__((address_space(3))) char lds_char;

#define __float_as_uint(f) __builtin_bit_cast(unsigned, (float)(f))
#define __uint_as_float(u) __builtin_bit_cast(float, (unsigned)(u))
#define __any(c) (__builtin_amdgcn_ballot_w64(c) != 0)
#define __all(c) (__builtin_amdgcn_ballot_w64(!(c)) == 0)
extern "C" __device__ unsigned long long llvm_dispatch_id() __asm("llvm.amdgcn.dispatch.id");

__device__ __forceinline__ float bf2f(u16 x) { return __uint_as_float(((unsigned)x) << 16); }
__device__ __forceinline__ u16 f2bf(float f) {
    __hip_bfloat16 b = __float2bfloat16(f);
    return *reinterpret_cast<u16*>(&b);
}
__device__ __forceinline__ float rbf(float f) { return bf2f(f2bf(f)); }
__device__ __forceinline__ float lo_f(unsigned u) { return __uint_as_float(u << 16); }
__device__ __forceinline__ float hi_f(unsigned u) { return __uint_as_float(u & 0xffff0000u); }
__device__ __forceinline__ unsigned pack2(float a, float b) {
    return __builtin_bit_cast(unsigned, __builtin_convertvector((f32x2){a, b}, bf16x2_t));
}
__device__ __forceinline__ float dot2(unsigned a, unsigned b, float c) {
    return __builtin_amdgcn_fdot2_f32_bf16(__builtin_bit_cast(bf16x2_t, a), __builtin_bit_cast(bf16x2_t, b), c, false);
}

#define SB() __builtin_amdgcn_sched_barrier(0)
#define WAIT_VM(n) asm volatile("s_waitcnt vmcnt(" #n ")" ::: "memory")
#define RAW_BARRIER() do { asm volatile("s_waitcnt lgkmcnt(0)" ::: "memory"); __builtin_amdgcn_s_barrier(); } while (0)

#define KV_PITCH 4224
#define WS_KV 4096
#define WS_O (WS_KV + D_DIM * KV_PITCH)
#define WS_Y (WS_O + D_DIM * S_LEN * 2)
#define WS_G (WS_Y + D_DIM * S_LEN * 2)
#define WS_XF (WS_G + S_LEN * 4)
#define WS_SF (WS_XF + 128 * 128)
#define WS_HF (WS_SF + 128 * 128)
#define WS_HN (WS_HF + 128 * 128)
#define WS_HG (WS_HN + 128 * 128)
#define WS_HX (WS_HG + 128 * 128)
#define WS_END (WS_HX + 128 * 5120)
#define WS_DBG (WS_END + 4096)

#define L_A 0
#define A_SLOT 18432
#define L_W 55296
#define W_SLOT 16384
#define W_PITCH 512
#define NW 6
#define G_XCH 69632
#define G_Y 0
#define G_WO1 18432
#define G_WO0 (G_XCH + 20480)
#define G_Q (G_WO0 + 51200)
#define G_VX 153600
#define G_HX 158208
#define G_VXD (G_HX + 4608)
#define KV_BYTES 4352
#define V_F (4 * KV_BYTES)
#define XC_ROW 288
#define XC_B 12
#define XC_COPY 4944
#define XC_BYTES (XC_COPY + 17 * XC_ROW)
#define V_KV4 (V_F + 5 * XC_BYTES)
#define V_KV(c) ((c) < 4 ? (c) * KV_BYTES : V_KV4)
#define V_WO (V_KV4 + KV_BYTES - 10240)
#define V_KST (V_WO + 10240 + 1312)
#define V_VST (V_KST + 20608)
#define V_STG 113536
#define V_QS (V_WO + 10240)
#define V_SCR 130560
#define V_XCH 0
#define V_Y 40960
#define Y_PITCH 160
#define L_TOUCH 163328
#define L_CTRL 163584
#define LDS_TOTAL 163840
static_assert(V_VST + 20608 <= V_STG && V_STG % 128 == 0 && V_STG + 20480 <= L_TOUCH && V_WO >= V_F + 4 * XC_BYTES && V_WO + 10 * 10240 <= L_TOUCH &&
              V_WO + 6 * 10240 <= V_SCR && V_SCR + 32768 <= L_TOUCH && V_Y + 80 * Y_PITCH <= V_WO && V_WO % 16 == 0, "lds");
static_assert(G_Q + 10240 <= G_VX && G_WO1 + 51200 <= G_XCH && L_W + NW * W_SLOT <= G_VX && G_VX + 4608 <= G_HX && G_VXD + 520 <= L_CTRL && G_Y + 80 * Y_PITCH <= A_SLOT && G_Q + 20480 <= G_VXD, "lds");
#define C_FREE (L_CTRL + 16)
#define C_OST (L_CTRL + 48)
#define C_SENT (L_CTRL + 52)
#define C_WO (L_CTRL + 56)
#define C_KV (L_CTRL + 60)
#define C_KVST (L_CTRL + 64)
#define C_CONVN (L_CTRL + 68)
#define C_Y (L_CTRL + 72)
#define C_KVR (L_CTRL + 76)
#define C_XD (L_CTRL + 80)
#define C_RED (L_CTRL + 84)
#define C_OWN (L_CTRL + 88)
#define C_QR (L_CTRL + 92)
#define C_TY (L_CTRL + 96)
#define C_TR (L_CTRL + 112)
#define C_OS1 (L_CTRL + 104)
#define C_OSC (L_CTRL + 108)
#define C_SFC (L_CTRL + 136)
#define C_GO (L_CTRL + 140)
#define C_HR (L_CTRL + 144)
#define C_RED2 (L_CTRL + 148)
#define C_KVD (L_CTRL + 152)
#define C_WT (L_CTRL + 156)
#define C_HX (L_CTRL + 160)
#define C_XCH (L_CTRL + 176)
#define C_FULL (L_CTRL + 208)
#define C_HS (L_CTRL + 100)
#define C_OW2 (L_CTRL + 224)
#define C_HGC (L_CTRL + 228)
#define C_LK (L_CTRL + 52)
#define C_TM (L_CTRL + 192)

#define KNOB(n, v)
#define SPIN_CAP 4000000
#define GH 0
#define WO_PRIO 0
#define T_PRIO 3
#define C0_PRIO 2
#define C1_PRIO 1
#define L_PRIO 3
#define O_SLEEP 4
#define O_DELAY 0
#define POLL_SLEEP 2
#define Y_SLEEP 8
#define W_SLEEP 4
#define X_SLEEP 2
#define X_DELAY 0
#define CONV_PRIO 2
#define NBUF 4
#define WATCH_RT 2
#define JL 17
#define RING_E0 70
#define RING_F 105
#define WOT_SPLIT 0
#define GO_W C_KVD
#define GO_N 11
#define SENT_PACE 10
#define GATE_LATE 0
#define WOT_PACE 7
#define SENT_W C_XD
#define SENT_N 4
#define O_AHEAD 4
#define PASS_PRIO 1
#define G_OST 8
#define V_ODELAY 0
#define W_LATE 1
#define Y_SL0 0
#define SF_DELAY 8
#define WO_MALL 1
#define T4_EARLY 0
#define POLL_DELAY 24
#define T4_W C_KV
#define T4_N 10
#define SENT_DELAY 0
#define CF_W C_RED2
#define CF_N 4
#define CF_DELAY 0
#define NB3 6
#define GW0_DELAY 4
#define GW1_DELAY 4
#define VW0_DELAY 0
#define VW1_DELAY 6
#define ROT_OC 3
#define AUX_PRIO 1
#define RING_T 5
#define H_EARLY 0
#define T8_AT 1
#define RING_J 7
#define RING_F2 105
#define LATE_W C_RED
#define LATE_N 4
#define WOT_W C_XD
#define WOT_N 4
#define GATE_W C_XD
#define GATE_N 4
#define WO2_W C_XD
#define WO2_N 4
#define V_BOTH true
#define NRES 4
#define OS_EARLY 1
#define OS_DELAY 0
#define WO2_FIRST 0
#define HINT_W 3
#define D0 5400
#define D1 6000
#define DY 7400
#define CF_SLEEP 6
#define V_MORE 2
#define G_MORE 1
#define SELF_AT 0
#define LOOK_AT 2
#define VL_DELAY 0
#define ENT_PRIO 1
#define G_GATE0 8
#define G_GATE1 8
#define YEARLY 1
#define Y_SOON 10
#define T_JOIN 0
#define X_NOOS 0
#define X_NOYS 0
#define X_NOG 0
#define X_TALL 0
#define L_FIRST 1
#define X_SHIFT 0
#define D_AHEAD 9000
#define HALO_EARLY 1
#define OUT_P 0
#define OUT_POL ""
#define G_LEAD 0
#define XF_AUX 16
#define CQ_X 2
#define HYB 0
#define RETOUCH 1
#define HX_AT 3
#define HX_SLEEP 2
#define NHRETRY()
#define HL_AT 2
#define HS_N 4
#define HS_POS 1
#define FB_T 30
#define TAILM 1
#define L19_PRIO 3
#define E_PRIO 3
#define GATE_EARLY 0
#define PRIO_EARLY 0
#define HG_AT 6
#define OW2_AT 0
#define PF_SLEEP 6
#define OS_PRE 10
#define YS_N 5
#define KROT ((0x27057250u >> (xcd * 4)) & 15)

#define STAMP(id)
#define FLUSH()
#define CSTAMP(i)

#define FLUSH_LO()
#define TS(i)
#define TSFLUSH()
__device__ __forceinline__ unsigned lds_ld32(unsigned a) {
    unsigned v;
    asm volatile("ds_read_b32 %0, %1\n\ts_waitcnt lgkmcnt(0)" : "=v"(v) : "v"(a) : "memory");
    return v;
}
__device__ __forceinline__ unsigned lds_min4(unsigned a) {
    u32x4 v;
    asm volatile("ds_read_b128 %0, %1\n\ts_waitcnt lgkmcnt(0)" : "=v"(v) : "v"(a) : "memory");
    return min(min(v[0], v[1]), min(v[2], v[3]));
}
__device__ __forceinline__ unsigned lds_min8(unsigned a) {
    u32x4 v, u;
    asm volatile("ds_read_b128 %0, %2\n\tds_read_b128 %1, %2 offset:16\n\ts_waitcnt lgkmcnt(0)" : "=&v"(v), "=&v"(u) : "v"(a) : "memory");
    return min(min(min(v[0], v[1]), min(v[2], v[3])), min(min(u[0], u[1]), min(u[2], u[3])));
}
__device__ __forceinline__ void lds_st32(unsigned a, unsigned v) {
    asm volatile("ds_write_b32 %0, %1" ::"v"(a), "v"(v) : "memory");
}
__device__ __forceinline__ unsigned lds_add_rtn(unsigned a, int lane) {
    unsigned old = 0;
    if (lane == 0) {
        unsigned one = 1;
        asm volatile("ds_add_rtn_u32 %0, %1, %2\n\ts_waitcnt lgkmcnt(0)" : "=v"(old) : "v"(a), "v"(one) : "memory");
    }
    return __builtin_amdgcn_readfirstlane(old);
}
__device__ __forceinline__ void lds_add(unsigned a, int lane, unsigned n = 1) {
    if (lane == 0) asm volatile("ds_add_u32 %0, %1" ::"v"(a), "v"(n) : "memory");
}
__device__ __forceinline__ void spin_min4(unsigned a, unsigned target) {
#pragma nounroll
    for (int i = 0; i < SPIN_CAP; ++i) {
        if (__builtin_amdgcn_readfirstlane(lds_min4(a)) >= target) break;
        __builtin_amdgcn_s_sleep(1);
    }
}
__device__ __forceinline__ void spin_min8(unsigned a, unsigned target) {
#pragma nounroll
    for (int i = 0; i < SPIN_CAP; ++i) {
        if (__builtin_amdgcn_readfirstlane(lds_min8(a)) >= target) break;
        __builtin_amdgcn_s_sleep(1);
    }
}
__device__ __forceinline__ void spin_ge(unsigned a, unsigned target) {
#pragma nounroll
    for (int i = 0; i < SPIN_CAP; ++i) {
        if (__builtin_amdgcn_readfirstlane(lds_ld32(a)) >= target) break;
        __builtin_amdgcn_s_sleep(1);
    }
}
typedef __attribute__((address_space(3))) volatile u32x4 lds_vu32x4;
typedef __attribute__((address_space(3))) volatile unsigned lds_vu32_t;
__device__ __forceinline__ u32x4 lds_v128(unsigned a) { return *(lds_vu32x4*)(size_t)a; }
__device__ __forceinline__ unsigned lds_v32(unsigned a) { return *(lds_vu32_t*)(size_t)a; }
__device__ __forceinline__ float fmax_raw(float a, float b) { return __builtin_elementwise_maximum(a, b); }
__device__ __forceinline__ float fmax3_raw(float a, float b, float c) { return __builtin_elementwise_maximum(__builtin_elementwise_maximum(a, b), c); }
__device__ __forceinline__ unsigned opaque(unsigned v) { asm volatile("" : "+v"(v)); return v; }
__device__ __forceinline__ bf16x8 cat8(s16x4 a, s16x4 b) { return __builtin_shufflevector(a, b, 0, 1, 2, 3, 4, 5, 6, 7); }

__device__ __forceinline__ void poll_flags(__amdgpu_buffer_rsrc_t rs, unsigned off, unsigned tlo, unsigned thi) {
#pragma nounroll
    for (int i = 0; i < SPIN_CAP; ++i) {
        __builtin_amdgcn_s_sleep(PF_SLEEP);
        asm volatile("" ::: "memory");
        const u32x2 v = __builtin_amdgcn_raw_buffer_load_b64(rs, off, 0, 16);
        if (__all(v[0] == tlo && v[1] == thi)) break;
        __builtin_amdgcn_s_sleep(POLL_SLEEP);
    }
    __builtin_amdgcn_fence(__ATOMIC_ACQUIRE, "wavefront");
}

__device__ __forceinline__ void spin_slow(unsigned a, unsigned target) {
#pragma nounroll
    for (int i = 0; i < SPIN_CAP; ++i) {
        if (__builtin_amdgcn_readfirstlane(lds_ld32(a)) >= target) break;
        __builtin_amdgcn_s_sleep(6);
    }
}
__device__ __forceinline__ void spin_mid(unsigned a, unsigned target) {
#pragma nounroll
    for (int i = 0; i < SPIN_CAP; ++i) {
        if (__builtin_amdgcn_readfirstlane(lds_ld32(a)) >= target) break;
        __builtin_amdgcn_s_sleep(3);
    }
}
__device__ __forceinline__ void spin_all(unsigned a, unsigned target) {
#pragma nounroll
    for (int i = 0; i < SPIN_CAP; ++i) {
        if (__all(lds_ld32(a) >= target)) break;
        __builtin_amdgcn_s_sleep(1);
    }
}

template <int N> __device__ __forceinline__ void waitvm() { asm volatile("s_waitcnt vmcnt(%0)" ::"n"(N) : "memory"); }
#define BARRIER() asm volatile("s_barrier" ::: "memory")
#define GLS(voff, sbase, lw, ldsoff) asm volatile("s_add_u32 m0, %2, %3\n\ts_nop 0\n\tglobal_load_lds_dwordx4 %0, %1" ::"v"(voff), "s"(sbase), "s"(lw), "n"(ldsoff) : "memory", "scc")
#define GLT(voff, sbase) asm volatile("s_mov_b32 m0, %2\n\ts_nop 0\n\tglobal_load_lds_dword %0, %1" ::"v"(voff), "s"(sbase), "s"(lb + L_TOUCH) : "memory")
#define TRRD(p) __builtin_amdgcn_ds_read_tr16_b64_v4i16((lds_s16x4*)(p))

constexpr int ring_total(int NA) { return 10 * (8 + NA); }
constexpr int ring_need(int J, int NA) { return J < 0 ? NA + 8 : (J & 1) ? (NA + 8) * ((J + 3) / 2) : (NA + 8) * ((J + 2) / 2) + 4 + NA; }
constexpr int ring_isA(int p, int NA) { return p < NA ? 1 : p < NA + 8 ? 0 : (p % (8 + NA) >= 4 && p % (8 + NA) < 4 + NA); }
constexpr int ring_tile(int p, int NA) { return p < NA ? 0 : p < NA + 8 ? (p - NA) / 4 : ring_isA(p, NA) ? p / (8 + NA) : 2 * (p / (8 + NA)) + (p % (8 + NA) >= 4); }
constexpr int ring_sub(int p, int NA) { return p < NA ? p : p < NA + 8 ? (p - NA) % 4 : p % (8 + NA) < 4 ? p % (8 + NA) : p % (8 + NA) < 4 + NA ? p % (8 + NA) - 4 : p % (8 + NA) - 4 - NA; }
constexpr int ring_cum(int J, int NA) {
    if (J > JL || J >= 16) return ring_total(NA);
    int c = NA + 8 + (8 + NA) * (2 * RING_E0 + RING_F * (J + 1) + (J > RING_J ? (RING_F2 - RING_F) * (J - RING_J) : 0)) / 200;
    if (c > ring_total(NA)) c = ring_total(NA);
    if (c < ring_need(J, NA)) c = ring_need(J, NA);
    while (ring_isA(c - 1, NA) ? 2 * (ring_tile(c - 1, NA) - 3) + 1 > J - 1 : ring_tile(c - 1, NA) - NW > J - 1) --c;
    return c;
}
static_assert(JL >= 12 && JL <= 17, "behind the last barrier (the compute waves have read the operands of step JL + 1) every slot is free");
template <int NA, int P0, int P1>
__device__ __forceinline__ void ring_issue(const unsigned (&ev)[9], const void* xp, const void* wp, int kr, unsigned lw) {
    if constexpr (P0 < P1) {
        constexpr int t = ring_tile(P0, NA), sub = ring_sub(P0, NA);
        if constexpr (ring_isA(P0, NA)) {
            const int c = __builtin_amdgcn_readfirstlane(kr + t >= 10 ? kr + t - 10 : kr + t);
            GLS(ev[4 + sub], (const char*)xp + c * 128, lw, L_A + 2048 + (t % 3) * A_SLOT + sub * 4096);
        } else {
            const int c = __builtin_amdgcn_readfirstlane(2 * kr + t >= 20 ? 2 * kr + t - 20 : 2 * kr + t);
            GLS(ev[sub], (const char*)wp + (size_t)c * (32 * NQKV * 2), lw, L_W + (t % NW) * W_SLOT + sub * 4096);
        }
        ring_issue<NA, P0 + 1, P1>(ev, xp, wp, kr, lw);
    }
}
template <int NA, int J>
__device__ __forceinline__ void ring_run17(const unsigned (&ev)[9], const void* xp, const void* wp, int kr, unsigned lw, unsigned& c0, unsigned& r0) {
    if constexpr (J > 0) ring_issue<NA, (J < 0 ? 0 : ring_cum(J - 1, NA)), ring_cum(J, NA)>(ev, xp, wp, kr, lw);
    if constexpr (J < 16) {
        waitvm<ring_cum(J, NA) - ring_need(J, NA)>();
        BARRIER();
        if constexpr (J == 9) { c0 = (unsigned)__builtin_amdgcn_s_memtime(); r0 = (unsigned)__builtin_amdgcn_s_memrealtime(); }
        ring_run17<NA, J + 1>(ev, xp, wp, kr, lw, c0, r0);
    }
}
constexpr int ring_cum2(int J, int NA) {
    int c = ring_cum(JL, NA) + (J - JL - 1) * RING_T;
    if (c < ring_need(J, NA)) c = ring_need(J, NA);
    if (J == 17 || c > ring_total(NA)) c = ring_total(NA);
    return c;
}
template <int NA, int J>
__device__ __forceinline__ void ring_run(const unsigned (&ev)[9], const void* xp, const void* wp, int kr, unsigned lw, unsigned full, int lane) {
    if constexpr (J <= JL) {
        ring_issue<NA, (J < 0 ? 0 : ring_cum(J - 1, NA)), ring_cum(J, NA)>(ev, xp, wp, kr, lw);
        waitvm<ring_cum(J, NA) - ring_need(J, NA)>();
        BARRIER();
        ring_run<NA, J + 1>(ev, xp, wp, kr, lw, full, lane);
    } else if constexpr (J <= 17) {
        ring_issue<NA, (J == JL + 1 ? ring_cum(JL, NA) : ring_cum2(J - 1, NA)), ring_cum2(J, NA)>(ev, xp, wp, kr, lw);
        waitvm<ring_cum2(J, NA) - ring_need(J, NA)>();
        lds_add(full + (J - 13) * 4, lane);
        ring_run<NA, J + 1>(ev, xp, wp, kr, lw, full, lane);
    }
}

#define CONV_ASM \
    "s_mov_b32 m0, %[kvl]\n" \
    "v_mov_b32 v50, 1\n" \
    "global_load_lds_dwordx4 %[xo], %[wsp] offset:0 sc1\n" \
    "global_load_lds_dwordx4 %[xo], %[wsp] offset:1024 sc1\n" \
    "global_load_lds_dwordx4 %[xo], %[wsp] offset:2048 sc1\n" \
    "global_load_lds_dwordx4 %[xo], %[wsp] offset:3072 sc1\n" \
    "s_mov_b64 exec, 1\n" \
    "ds_add_u32 %[ctl], v50 offset:80\n" \
    "s_mov_b64 exec, -1\n" \
    "v_add_u32 v84, %[sx4], %[xp]\n" \
    "v_add_u32 v51, %[sh4], %[hp]\n" \
    "v_add_u32 v160, 0, %[xp]\n" \
    "v_add_u32 v161, 1152, %[xp]\n" \
    "v_add_u32 v85, 1152, v84\n" \
    "v_add_u32 v162, 2304, %[xp]\n" \
    "v_add_u32 v86, 2304, v84\n" \
    "v_add_u32 v163, 3456, %[xp]\n" \
    "v_add_u32 v87, 3456, v84\n" \
    "ds_read2_b32 v[88:89], v87 offset0:144 offset1:145\n" \
    "ds_read2_b32 v[90:91], v87 offset0:146 offset1:147\n" \
    "ds_read2_b32 v[92:93], v87 offset0:0 offset1:1\n" \
    "ds_read2_b32 v[94:95], v87 offset0:2 offset1:3\n" \
    "ds_read2_b32 v[96:97], v86 offset0:144 offset1:145\n" \
    "ds_read2_b32 v[98:99], v86 offset0:146 offset1:147\n" \
    "ds_read2_b32 v[100:101], v86 offset0:0 offset1:1\n" \
    "ds_read2_b32 v[102:103], v86 offset0:2 offset1:3\n" \
    "ds_read2_b32 v[104:105], v85 offset0:144 offset1:145\n" \
    "ds_read2_b32 v[106:107], v85 offset0:146 offset1:147\n" \
    "ds_read2_b32 v[108:109], v85 offset0:0 offset1:1\n" \
    "ds_read2_b32 v[110:111], v85 offset0:2 offset1:3\n" \
    "ds_read2_b32 v[112:113], v84 offset0:144 offset1:145\n" \
    "ds_read2_b32 v[114:115], v84 offset0:146 offset1:147\n" \
    "ds_read2_b32 v[116:117], v84 offset0:0 offset1:1\n" \
    "ds_read2_b32 v[118:119], v84 offset0:2 offset1:3\n" \
    "s_mov_b32 %[cnt], 0\n" \
    "Lsp_%=:\n" \
    "ds_read_b32 v50, %[ctl] offset:144\n" \
    "s_add_u32 %[cnt], %[cnt], 1\n" \
    "s_waitcnt lgkmcnt(0)\n" \
    "v_readfirstlane_b32 %[tmp], v50\n" \
    "s_cmp_ge_u32 %[cnt], %[cap]\n" \
    "s_cbranch_scc1 Lgo_%=\n" \
    "s_cmp_lt_u32 %[tmp], 4\n" \
    "s_cbranch_scc1 Lsp_%=\n" \
    "Lgo_%=:\n" \
    "ds_read_b128 v[148:151], v51 offset:3584\n" \
    "ds_read_b128 v[144:147], v51 offset:3072\n" \
    "ds_read_b128 v[140:143], v51 offset:2560\n" \
    "ds_read_b128 v[136:139], v51 offset:2048\n" \
    "ds_read_b128 v[132:135], v51 offset:1536\n" \
    "ds_read_b128 v[128:131], v51 offset:1024\n" \
    "ds_read_b128 v[124:127], v51 offset:512\n" \
    "ds_read_b128 v[120:123], v51 offset:0\n" \
    "s_waitcnt lgkmcnt(7)\n" \
    "v_mfma_f32_16x16x32_bf16 v[80:83], v[88:91], v[148:151], 0\n" \
    "ds_read_b128 v[148:151], v51 offset:3648\n" \
    "s_waitcnt lgkmcnt(7)\n" \
    "v_mfma_f32_16x16x32_bf16 v[76:79], v[88:91], v[144:147], 0\n" \
    "ds_read_b128 v[156:159], v51 offset:576\n" \
    "s_waitcnt lgkmcnt(7)\n" \
    "v_mfma_f32_16x16x32_bf16 v[72:75], v[88:91], v[140:143], 0\n" \
    "ds_read_b128 v[152:155], v51 offset:64\n" \
    "s_waitcnt lgkmcnt(7)\n" \
    "v_mfma_f32_16x16x32_bf16 v[68:71], v[88:91], v[136:139], 0\n" \
    "s_waitcnt lgkmcnt(6)\n" \
    "v_mfma_f32_16x16x32_bf16 v[64:67], v[88:91], v[132:135], 0\n" \
    "s_waitcnt lgkmcnt(5)\n" \
    "v_mfma_f32_16x16x32_bf16 v[60:63], v[88:91], v[128:131], 0\n" \
    "s_waitcnt lgkmcnt(4)\n" \
    "v_mfma_f32_16x16x32_bf16 v[56:59], v[88:91], v[124:127], 0\n" \
    "s_waitcnt lgkmcnt(3)\n" \
    "v_mfma_f32_16x16x32_bf16 v[52:55], v[88:91], v[120:123], 0\n" \
    "ds_read2_b32 v[88:89], v87 offset0:160 offset1:161\n" \
    "v_mfma_f32_16x16x32_bf16 v[80:83], v[92:95], v[144:147], v[80:83]\n" \
    "ds_read2_b32 v[90:91], v87 offset0:162 offset1:163\n" \
    "v_mfma_f32_16x16x32_bf16 v[76:79], v[92:95], v[140:143], v[76:79]\n" \
    "ds_read_b128 v[144:147], v51 offset:3136\n" \
    "v_mfma_f32_16x16x32_bf16 v[72:75], v[92:95], v[136:139], v[72:75]\n" \
    "v_mfma_f32_16x16x32_bf16 v[68:71], v[92:95], v[132:135], v[68:71]\n" \
    "v_mfma_f32_16x16x32_bf16 v[64:67], v[92:95], v[128:131], v[64:67]\n" \
    "v_mfma_f32_16x16x32_bf16 v[60:63], v[92:95], v[124:127], v[60:63]\n" \
    "v_mfma_f32_16x16x32_bf16 v[56:59], v[92:95], v[120:123], v[56:59]\n" \
    "ds_read2_b32 v[92:93], v87 offset0:16 offset1:17\n" \
    "v_mfma_f32_16x16x32_bf16 v[80:83], v[96:99], v[140:143], v[80:83]\n" \
    "ds_read_b128 v[140:143], v51 offset:2624\n" \
    "v_mfma_f32_16x16x32_bf16 v[76:79], v[96:99], v[136:139], v[76:79]\n" \
    "ds_read2_b32 v[94:95], v87 offset0:18 offset1:19\n" \
    "v_mfma_f32_16x16x32_bf16 v[72:75], v[96:99], v[132:135], v[72:75]\n" \
    "v_mfma_f32_16x16x32_bf16 v[68:71], v[96:99], v[128:131], v[68:71]\n" \
    "v_mfma_f32_16x16x32_bf16 v[64:67], v[96:99], v[124:127], v[64:67]\n" \
    "v_mfma_f32_16x16x32_bf16 v[60:63], v[96:99], v[120:123], v[60:63]\n" \
    "ds_read2_b32 v[96:97], v86 offset0:160 offset1:161\n" \
    "v_mfma_f32_16x16x32_bf16 v[80:83], v[100:103], v[136:139], v[80:83]\n" \
    "ds_read_b128 v[136:139], v51 offset:2112\n" \
    "v_mfma_f32_16x16x32_bf16 v[76:79], v[100:103], v[132:135], v[76:79]\n" \
    "ds_read2_b32 v[98:99], v86 offset0:162 offset1:163\n" \
    "v_mfma_f32_16x16x32_bf16 v[72:75], v[100:103], v[128:131], v[72:75]\n" \
    "v_mfma_f32_16x16x32_bf16 v[68:71], v[100:103], v[124:127], v[68:71]\n" \
    "v_mfma_f32_16x16x32_bf16 v[64:67], v[100:103], v[120:123], v[64:67]\n" \
    "ds_read2_b32 v[100:101], v86 offset0:16 offset1:17\n" \
    "v_mfma_f32_16x16x32_bf16 v[80:83], v[104:107], v[132:135], v[80:83]\n" \
    "ds_read_b128 v[132:135], v51 offset:1600\n" \
    "v_mfma_f32_16x16x32_bf16 v[76:79], v[104:107], v[128:131], v[76:79]\n" \
    "ds_read2_b32 v[102:103], v86 offset0:18 offset1:19\n" \
    "v_mfma_f32_16x16x32_bf16 v[72:75], v[104:107], v[124:127], v[72:75]\n" \
    "v_mfma_f32_16x16x32_bf16 v[68:71], v[104:107], v[120:123], v[68:71]\n" \
    "ds_read2_b32 v[104:105], v85 offset0:160 offset1:161\n" \
    "v_mfma_f32_16x16x32_bf16 v[80:83], v[108:111], v[128:131], v[80:83]\n" \
    "ds_read_b128 v[128:131], v51 offset:1088\n" \
    "v_mfma_f32_16x16x32_bf16 v[76:79], v[108:111], v[124:127], v[76:79]\n" \
    "ds_read2_b32 v[106:107], v85 offset0:162 offset1:163\n" \
    "v_mfma_f32_16x16x32_bf16 v[72:75], v[108:111], v[120:123], v[72:75]\n" \
    "ds_read2_b32 v[108:109], v85 offset0:16 offset1:17\n" \
    "v_mfma_f32_16x16x32_bf16 v[80:83], v[112:115], v[124:127], v[80:83]\n" \
    "ds_read2_b32 v[110:111], v85 offset0:18 offset1:19\n" \
    "v_mfma_f32_16x16x32_bf16 v[76:79], v[112:115], v[120:123], v[76:79]\n" \
    "ds_read2_b32 v[112:113], v84 offset0:160 offset1:161\n" \
    "v_mfma_f32_16x16x32_bf16 v[80:83], v[116:119], v[120:123], v[80:83]\n" \
    "ds_read2_b32 v[114:115], v84 offset0:162 offset1:163\n" \
    "s_waitcnt lgkmcnt(15)\n" \
    "v_mfma_f32_16x16x32_bf16 v[80:83], v[88:91], v[148:151], v[80:83]\n" \
    "ds_read2_b32 v[116:117], v84 offset0:16 offset1:17\n" \
    "v_mfma_f32_16x16x32_bf16 v[76:79], v[88:91], v[144:147], v[76:79]\n" \
    "ds_read2_b32 v[118:119], v84 offset0:18 offset1:19\n" \
    "s_waitcnt lgkmcnt(15)\n" \
    "v_mfma_f32_16x16x32_bf16 v[72:75], v[88:91], v[140:143], v[72:75]\n" \
    "s_waitcnt vmcnt(0)\n" \
    "ds_read_b128 v[148:151], %[hp] offset:3584\n" \
    "s_waitcnt lgkmcnt(14)\n" \
    "v_mfma_f32_16x16x32_bf16 v[68:71], v[88:91], v[136:139], v[68:71]\n" \
    "ds_read_b128 v[124:127], %[hp] offset:512\n" \
    "s_waitcnt lgkmcnt(12)\n" \
    "v_mfma_f32_16x16x32_bf16 v[64:67], v[88:91], v[132:135], v[64:67]\n" \
    "ds_read_b128 v[120:123], %[hp] offset:0\n" \
    "s_waitcnt lgkmcnt(10)\n" \
    "v_mfma_f32_16x16x32_bf16 v[60:63], v[88:91], v[128:131], v[60:63]\n" \
    "v_mfma_f32_16x16x32_bf16 v[56:59], v[88:91], v[156:159], v[56:59]\n" \
    "v_mfma_f32_16x16x32_bf16 v[52:55], v[88:91], v[152:155], v[52:55]\n" \
    "ds_read2_b32 v[88:89], v163 offset0:144 offset1:145\n" \
    "v_mfma_f32_16x16x32_bf16 v[80:83], v[92:95], v[144:147], v[80:83]\n" \
    "ds_read2_b32 v[90:91], v163 offset0:146 offset1:147\n" \
    "v_mfma_f32_16x16x32_bf16 v[76:79], v[92:95], v[140:143], v[76:79]\n" \
    "ds_read_b128 v[144:147], %[hp] offset:3072\n" \
    "v_mfma_f32_16x16x32_bf16 v[72:75], v[92:95], v[136:139], v[72:75]\n" \
    "v_mfma_f32_16x16x32_bf16 v[68:71], v[92:95], v[132:135], v[68:71]\n" \
    "ds_write_b128 %[scr], v[52:55] offset:0\n" \
    "v_mfma_f32_16x16x32_bf16 v[64:67], v[92:95], v[128:131], v[64:67]\n" \
    "ds_read_b64 v[86:87], %[kvr] offset:0\n" \
    "v_mfma_f32_16x16x32_bf16 v[60:63], v[92:95], v[156:159], v[60:63]\n" \
    "v_mfma_f32_16x16x32_bf16 v[56:59], v[92:95], v[152:155], v[56:59]\n" \
    "ds_read2_b32 v[92:93], v163 offset0:0 offset1:1\n" \
    "v_mfma_f32_16x16x32_bf16 v[80:83], v[96:99], v[140:143], v[80:83]\n" \
    "ds_read_b128 v[140:143], %[hp] offset:2560\n" \
    "v_mfma_f32_16x16x32_bf16 v[76:79], v[96:99], v[136:139], v[76:79]\n" \
    "ds_read2_b32 v[94:95], v163 offset0:2 offset1:3\n" \
    "v_mfma_f32_16x16x32_bf16 v[72:75], v[96:99], v[132:135], v[72:75]\n" \
    "v_mfma_f32_16x16x32_bf16 v[68:71], v[96:99], v[128:131], v[68:71]\n" \
    "ds_write_b128 %[scr], v[56:59] offset:1024\n" \
    "v_mfma_f32_16x16x32_bf16 v[64:67], v[96:99], v[156:159], v[64:67]\n" \
    "v_mfma_f32_16x16x32_bf16 v[60:63], v[96:99], v[152:155], v[60:63]\n" \
    "ds_read2_b32 v[96:97], v162 offset0:144 offset1:145\n" \
    "v_mfma_f32_16x16x32_bf16 v[80:83], v[100:103], v[136:139], v[80:83]\n" \
    "ds_read_b128 v[136:139], %[hp] offset:2048\n" \
    "v_mfma_f32_16x16x32_bf16 v[76:79], v[100:103], v[132:135], v[76:79]\n" \
    "ds_read2_b32 v[98:99], v162 offset0:146 offset1:147\n" \
    "v_mfma_f32_16x16x32_bf16 v[72:75], v[100:103], v[128:131], v[72:75]\n" \
    "v_mfma_f32_16x16x32_bf16 v[68:71], v[100:103], v[156:159], v[68:71]\n" \
    "s_waitcnt lgkmcnt(7)\n" \
    "v_lshlrev_b32 v84, 16, v86\n" \
    "v_mfma_f32_16x16x32_bf16 v[64:67], v[100:103], v[152:155], v[64:67]\n" \
    "ds_read2_b32 v[100:101], v162 offset0:0 offset1:1\n" \
    "v_mfma_f32_16x16x32_bf16 v[80:83], v[104:107], v[132:135], v[80:83]\n" \
    "ds_read_b128 v[132:135], %[hp] offset:1536\n" \
    "v_and_b32 v85, 0xffff0000, v86\n" \
    "v_mfma_f32_16x16x32_bf16 v[76:79], v[104:107], v[128:131], v[76:79]\n" \
    "ds_read2_b32 v[102:103], v162 offset0:2 offset1:3\n" \
    "v_mfma_f32_16x16x32_bf16 v[72:75], v[104:107], v[156:159], v[72:75]\n" \
    "v_lshlrev_b32 v86, 16, v87\n" \
    "v_mfma_f32_16x16x32_bf16 v[68:71], v[104:107], v[152:155], v[68:71]\n" \
    "ds_read2_b32 v[104:105], v161 offset0:144 offset1:145\n" \
    "v_mfma_f32_16x16x32_bf16 v[80:83], v[108:111], v[128:131], v[80:83]\n" \
    "ds_read_b128 v[128:131], %[hp] offset:1024\n" \
    "v_and_b32 v87, 0xffff0000, v87\n" \
    "v_mfma_f32_16x16x32_bf16 v[76:79], v[108:111], v[156:159], v[76:79]\n" \
    "ds_read2_b32 v[106:107], v161 offset0:146 offset1:147\n" \
    "v_mfma_f32_16x16x32_bf16 v[72:75], v[108:111], v[152:155], v[72:75]\n" \
    "ds_read2_b32 v[108:109], v161 offset0:0 offset1:1\n" \
    "v_mfma_f32_16x16x32_bf16 v[80:83], v[112:115], v[156:159], v[80:83]\n" \
    "ds_read2_b32 v[110:111], v161 offset0:2 offset1:3\n" \
    "v_mfma_f32_16x16x32_bf16 v[76:79], v[112:115], v[152:155], v[76:79]\n" \
    "ds_read2_b32 v[112:113], v160 offset0:144 offset1:145\n" \
    "v_mfma_f32_16x16x32_bf16 v[80:83], v[116:119], v[152:155], v[80:83]\n" \
    "ds_read2_b32 v[114:115], v160 offset0:146 offset1:147\n" \
    "v_mfma_f32_16x16x32_bf16 v[44:47], v[88:91], v[148:151], 0\n" \
    "ds_read2_b32 v[116:117], v160 offset0:0 offset1:1\n" \
    "v_mfma_f32_16x16x32_bf16 v[40:43], v[88:91], v[144:147], 0\n" \
    "ds_read2_b32 v[118:119], v160 offset0:2 offset1:3\n" \
    "s_waitcnt lgkmcnt(15)\n" \
    "v_mfma_f32_16x16x32_bf16 v[36:39], v[88:91], v[140:143], 0\n" \
    "ds_read_b128 v[148:151], %[hp] offset:3648\n" \
    "s_waitcnt lgkmcnt(14)\n" \
    "v_mfma_f32_16x16x32_bf16 v[32:35], v[88:91], v[136:139], 0\n" \
    "ds_read_b128 v[156:159], %[hp] offset:576\n" \
    "s_waitcnt lgkmcnt(12)\n" \
    "v_mfma_f32_16x16x32_bf16 v[28:31], v[88:91], v[132:135], 0\n" \
    "ds_read_b128 v[152:155], %[hp] offset:64\n" \
    "s_waitcnt lgkmcnt(10)\n" \
    "v_mfma_f32_16x16x32_bf16 v[24:27], v[88:91], v[128:131], 0\n" \
    "ds_write_b128 %[scr], v[60:63] offset:2048\n" \
    "v_mfma_f32_16x16x32_bf16 v[20:23], v[88:91], v[124:127], 0\n" \
    "v_mfma_f32_16x16x32_bf16 v[16:19], v[88:91], v[120:123], 0\n" \
    "ds_read2_b32 v[88:89], v163 offset0:160 offset1:161\n" \
    "v_mfma_f32_16x16x32_bf16 v[44:47], v[92:95], v[144:147], v[44:47]\n" \
    "ds_read2_b32 v[90:91], v163 offset0:162 offset1:163\n" \
    "v_mfma_f32_16x16x32_bf16 v[40:43], v[92:95], v[140:143], v[40:43]\n" \
    "ds_read_b128 v[144:147], %[hp] offset:3136\n" \
    "v_mfma_f32_16x16x32_bf16 v[36:39], v[92:95], v[136:139], v[36:39]\n" \
    "v_fmac_f32 v16, %[dd], v84\n" \
    "v_fmac_f32 v17, %[dd], v85\n" \
    "v_mfma_f32_16x16x32_bf16 v[32:35], v[92:95], v[132:135], v[32:35]\n" \
    "v_fmac_f32 v18, %[dd], v86\n" \
    "v_fmac_f32 v19, %[dd], v87\n" \
    "v_mfma_f32_16x16x32_bf16 v[28:31], v[92:95], v[128:131], v[28:31]\n" \
    "ds_read_b64 v[86:87], %[kvr] offset:512\n" \
    "v_mfma_f32_16x16x32_bf16 v[24:27], v[92:95], v[124:127], v[24:27]\n" \
    "ds_write_b128 %[scr], v[64:67] offset:3072\n" \
    "v_mfma_f32_16x16x32_bf16 v[20:23], v[92:95], v[120:123], v[20:23]\n" \
    "ds_read2_b32 v[92:93], v163 offset0:16 offset1:17\n" \
    "v_mfma_f32_16x16x32_bf16 v[44:47], v[96:99], v[140:143], v[44:47]\n" \
    "ds_read_b128 v[140:143], %[hp] offset:2624\n" \
    "v_mfma_f32_16x16x32_bf16 v[40:43], v[96:99], v[136:139], v[40:43]\n" \
    "ds_read2_b32 v[94:95], v163 offset0:18 offset1:19\n" \
    "v_mfma_f32_16x16x32_bf16 v[36:39], v[96:99], v[132:135], v[36:39]\n" \
    "ds_write_b128 %[scr], v[68:71] offset:4096\n" \
    "v_mfma_f32_16x16x32_bf16 v[32:35], v[96:99], v[128:131], v[32:35]\n" \
    "v_mfma_f32_16x16x32_bf16 v[28:31], v[96:99], v[124:127], v[28:31]\n" \
    "v_mfma_f32_16x16x32_bf16 v[24:27], v[96:99], v[120:123], v[24:27]\n" \
    "ds_read2_b32 v[96:97], v162 offset0:160 offset1:161\n" \
    "v_mfma_f32_16x16x32_bf16 v[44:47], v[100:103], v[136:139], v[44:47]\n" \
    "ds_read_b128 v[136:139], %[hp] offset:2112\n" \
    "v_mfma_f32_16x16x32_bf16 v[40:43], v[100:103], v[132:135], v[40:43]\n" \
    "ds_read2_b32 v[98:99], v162 offset0:162 offset1:163\n" \
    "v_mfma_f32_16x16x32_bf16 v[36:39], v[100:103], v[128:131], v[36:39]\n" \
    "ds_write_b128 %[scr], v[72:75] offset:5120\n" \
    "v_mfma_f32_16x16x32_bf16 v[32:35], v[100:103], v[124:127], v[32:35]\n" \
    "s_waitcnt lgkmcnt(9)\n" \
    "v_lshlrev_b32 v84, 16, v86\n" \
    "v_mfma_f32_16x16x32_bf16 v[28:31], v[100:103], v[120:123], v[28:31]\n" \
    "ds_read2_b32 v[100:101], v162 offset0:16 offset1:17\n" \
    "v_mfma_f32_16x16x32_bf16 v[44:47], v[104:107], v[132:135], v[44:47]\n" \
    "ds_read_b128 v[132:135], %[hp] offset:1600\n" \
    "v_and_b32 v85, 0xffff0000, v86\n" \
    "v_mfma_f32_16x16x32_bf16 v[40:43], v[104:107], v[128:131], v[40:43]\n" \
    "ds_read2_b32 v[102:103], v162 offset0:18 offset1:19\n" \
    "v_mfma_f32_16x16x32_bf16 v[36:39], v[104:107], v[124:127], v[36:39]\n" \
    "v_lshlrev_b32 v86, 16, v87\n" \
    "v_mfma_f32_16x16x32_bf16 v[32:35], v[104:107], v[120:123], v[32:35]\n" \
    "ds_read2_b32 v[104:105], v161 offset0:160 offset1:161\n" \
    "v_mfma_f32_16x16x32_bf16 v[44:47], v[108:111], v[128:131], v[44:47]\n" \
    "ds_read_b128 v[128:131], %[hp] offset:1088\n" \
    "v_and_b32 v87, 0xffff0000, v87\n" \
    "v_mfma_f32_16x16x32_bf16 v[40:43], v[108:111], v[124:127], v[40:43]\n" \
    "ds_read2_b32 v[106:107], v161 offset0:162 offset1:163\n" \
    "v_mfma_f32_16x16x32_bf16 v[36:39], v[108:111], v[120:123], v[36:39]\n" \
    "ds_read2_b32 v[108:109], v161 offset0:16 offset1:17\n" \
    "v_mfma_f32_16x16x32_bf16 v[44:47], v[112:115], v[124:127], v[44:47]\n" \
    "ds_read2_b32 v[110:111], v161 offset0:18 offset1:19\n" \
    "v_mfma_f32_16x16x32_bf16 v[40:43], v[112:115], v[120:123], v[40:43]\n" \
    "ds_read2_b32 v[112:113], v160 offset0:160 offset1:161\n" \
    "v_mfma_f32_16x16x32_bf16 v[44:47], v[116:119], v[120:123], v[44:47]\n" \
    "ds_read2_b32 v[114:115], v160 offset0:162 offset1:163\n" \
    "v_mfma_f32_16x16x32_bf16 v[44:47], v[88:91], v[148:151], v[44:47]\n" \
    "ds_read2_b32 v[116:117], v160 offset0:16 offset1:17\n" \
    "v_mfma_f32_16x16x32_bf16 v[40:43], v[88:91], v[144:147], v[40:43]\n" \
    "ds_read2_b32 v[118:119], v160 offset0:18 offset1:19\n" \
    "s_waitcnt lgkmcnt(15)\n" \
    "v_mfma_f32_16x16x32_bf16 v[36:39], v[88:91], v[140:143], v[36:39]\n" \
    "ds_read_b128 v[148:151], %[hp] offset:3712\n" \
    "s_waitcnt lgkmcnt(15)\n" \
    "v_mfma_f32_16x16x32_bf16 v[32:35], v[88:91], v[136:139], v[32:35]\n" \
    "ds_read_b128 v[124:127], %[hp] offset:640\n" \
    "s_waitcnt lgkmcnt(12)\n" \
    "v_mfma_f32_16x16x32_bf16 v[28:31], v[88:91], v[132:135], v[28:31]\n" \
    "ds_read_b128 v[120:123], %[hp] offset:128\n" \
    "s_waitcnt lgkmcnt(10)\n" \
    "v_mfma_f32_16x16x32_bf16 v[24:27], v[88:91], v[128:131], v[24:27]\n" \
    "ds_write_b128 %[scr], v[76:79] offset:6144\n" \
    "v_mfma_f32_16x16x32_bf16 v[20:23], v[88:91], v[156:159], v[20:23]\n" \
    "v_mfma_f32_16x16x32_bf16 v[16:19], v[88:91], v[152:155], v[16:19]\n" \
    "ds_read2_b32 v[88:89], v163 offset0:176 offset1:177\n" \
    "v_mfma_f32_16x16x32_bf16 v[44:47], v[92:95], v[144:147], v[44:47]\n" \
    "ds_read2_b32 v[90:91], v163 offset0:178 offset1:179\n" \
    "v_mfma_f32_16x16x32_bf16 v[40:43], v[92:95], v[140:143], v[40:43]\n" \
    "ds_read_b128 v[144:147], %[hp] offset:3200\n" \
    "v_fmac_f32 v20, %[dd], v84\n" \
    "v_fmac_f32 v21, %[dd], v85\n" \
    "v_mfma_f32_16x16x32_bf16 v[36:39], v[92:95], v[136:139], v[36:39]\n" \
    "v_fmac_f32 v22, %[dd], v86\n" \
    "v_fmac_f32 v23, %[dd], v87\n" \
    "v_mfma_f32_16x16x32_bf16 v[32:35], v[92:95], v[132:135], v[32:35]\n" \
    "ds_write_b128 %[scr], v[80:83] offset:7168\n" \
    "v_mov_b32 v50, 1\n" \
    "s_mov_b64 exec, 1\n" \
    "ds_add_u32 %[ctl], v50 offset:84\n" \
    "s_mov_b64 exec, -1\n" \
    "v_mfma_f32_16x16x32_bf16 v[28:31], v[92:95], v[128:131], v[28:31]\n" \
    "ds_read_b64 v[86:87], %[kvr] offset:1024\n" \
    "v_mfma_f32_16x16x32_bf16 v[24:27], v[92:95], v[156:159], v[24:27]\n" \
    "v_mfma_f32_16x16x32_bf16 v[20:23], v[92:95], v[152:155], v[20:23]\n" \
    "ds_read2_b32 v[92:93], v163 offset0:32 offset1:33\n" \
    "v_mfma_f32_16x16x32_bf16 v[44:47], v[96:99], v[140:143], v[44:47]\n" \
    "ds_read_b128 v[140:143], %[hp] offset:2688\n" \
    "v_mfma_f32_16x16x32_bf16 v[40:43], v[96:99], v[136:139], v[40:43]\n" \
    "ds_read2_b32 v[94:95], v163 offset0:34 offset1:35\n" \
    "v_mfma_f32_16x16x32_bf16 v[36:39], v[96:99], v[132:135], v[36:39]\n" \
    "v_mfma_f32_16x16x32_bf16 v[32:35], v[96:99], v[128:131], v[32:35]\n" \
    "v_mfma_f32_16x16x32_bf16 v[28:31], v[96:99], v[156:159], v[28:31]\n" \
    "v_mfma_f32_16x16x32_bf16 v[24:27], v[96:99], v[152:155], v[24:27]\n" \
    "ds_read2_b32 v[96:97], v162 offset0:176 offset1:177\n" \
    "v_mfma_f32_16x16x32_bf16 v[44:47], v[100:103], v[136:139], v[44:47]\n" \
    "ds_read_b128 v[136:139], %[hp] offset:2176\n" \
    "v_mfma_f32_16x16x32_bf16 v[40:43], v[100:103], v[132:135], v[40:43]\n" \
    "ds_read2_b32 v[98:99], v162 offset0:178 offset1:179\n" \
    "v_mfma_f32_16x16x32_bf16 v[36:39], v[100:103], v[128:131], v[36:39]\n" \
    "v_mfma_f32_16x16x32_bf16 v[32:35], v[100:103], v[156:159], v[32:35]\n" \
    "s_waitcnt lgkmcnt(6)\n" \
    "v_lshlrev_b32 v84, 16, v86\n" \
    "v_mfma_f32_16x16x32_bf16 v[28:31], v[100:103], v[152:155], v[28:31]\n" \
    "ds_read2_b32 v[100:101], v162 offset0:32 offset1:33\n" \
    "v_mfma_f32_16x16x32_bf16 v[44:47], v[104:107], v[132:135], v[44:47]\n" \
    "ds_read_b128 v[132:135], %[hp] offset:1664\n" \
    "v_and_b32 v85, 0xffff0000, v86\n" \
    "v_mfma_f32_16x16x32_bf16 v[40:43], v[104:107], v[128:131], v[40:43]\n" \
    "ds_read2_b32 v[102:103], v162 offset0:34 offset1:35\n" \
    "v_mfma_f32_16x16x32_bf16 v[36:39], v[104:107], v[156:159], v[36:39]\n" \
    "v_lshlrev_b32 v86, 16, v87\n" \
    "v_mfma_f32_16x16x32_bf16 v[32:35], v[104:107], v[152:155], v[32:35]\n" \
    "ds_read2_b32 v[104:105], v161 offset0:176 offset1:177\n" \
    "v_mfma_f32_16x16x32_bf16 v[44:47], v[108:111], v[128:131], v[44:47]\n" \
    "ds_read_b128 v[128:131], %[hp] offset:1152\n" \
    "v_and_b32 v87, 0xffff0000, v87\n" \
    "v_mfma_f32_16x16x32_bf16 v[40:43], v[108:111], v[156:159], v[40:43]\n" \
    "ds_read2_b32 v[106:107], v161 offset0:178 offset1:179\n" \
    "v_mfma_f32_16x16x32_bf16 v[36:39], v[108:111], v[152:155], v[36:39]\n" \
    "ds_read2_b32 v[108:109], v161 offset0:32 offset1:33\n" \
    "v_mfma_f32_16x16x32_bf16 v[44:47], v[112:115], v[156:159], v[44:47]\n" \
    "ds_read2_b32 v[110:111], v161 offset0:34 offset1:35\n" \
    "v_mfma_f32_16x16x32_bf16 v[40:43], v[112:115], v[152:155], v[40:43]\n" \
    "ds_read2_b32 v[112:113], v160 offset0:176 offset1:177\n" \
    "v_mfma_f32_16x16x32_bf16 v[44:47], v[116:119], v[152:155], v[44:47]\n" \
    "ds_read2_b32 v[114:115], v160 offset0:178 offset1:179\n" \
    "v_mfma_f32_16x16x32_bf16 v[44:47], v[88:91], v[148:151], v[44:47]\n" \
    "ds_read2_b32 v[116:117], v160 offset0:32 offset1:33\n" \
    "v_mfma_f32_16x16x32_bf16 v[40:43], v[88:91], v[144:147], v[40:43]\n" \
    "ds_read2_b32 v[118:119], v160 offset0:34 offset1:35\n" \
    "s_waitcnt lgkmcnt(15)\n" \
    "v_mfma_f32_16x16x32_bf16 v[36:39], v[88:91], v[140:143], v[36:39]\n" \
    "ds_read_b128 v[148:151], %[hp] offset:3776\n" \
    "s_waitcnt lgkmcnt(14)\n" \
    "v_mfma_f32_16x16x32_bf16 v[32:35], v[88:91], v[136:139], v[32:35]\n" \
    "ds_read_b128 v[156:159], %[hp] offset:704\n" \
    "s_waitcnt lgkmcnt(12)\n" \
    "v_mfma_f32_16x16x32_bf16 v[28:31], v[88:91], v[132:135], v[28:31]\n" \
    "ds_read_b128 v[152:155], %[hp] offset:192\n" \
    "s_waitcnt lgkmcnt(10)\n" \
    "v_mfma_f32_16x16x32_bf16 v[24:27], v[88:91], v[128:131], v[24:27]\n" \
    "v_mfma_f32_16x16x32_bf16 v[20:23], v[88:91], v[124:127], v[20:23]\n" \
    "v_mfma_f32_16x16x32_bf16 v[16:19], v[88:91], v[120:123], v[16:19]\n" \
    "ds_read2_b32 v[88:89], v163 offset0:192 offset1:193\n" \
    "v_mfma_f32_16x16x32_bf16 v[44:47], v[92:95], v[144:147], v[44:47]\n" \
    "ds_read2_b32 v[90:91], v163 offset0:194 offset1:195\n" \
    "v_fmac_f32 v24, %[dd], v84\n" \
    "v_fmac_f32 v25, %[dd], v85\n" \
    "v_mfma_f32_16x16x32_bf16 v[40:43], v[92:95], v[140:143], v[40:43]\n" \
    "ds_read_b128 v[144:147], %[hp] offset:3264\n" \
    "v_fmac_f32 v26, %[dd], v86\n" \
    "v_fmac_f32 v27, %[dd], v87\n" \
    "v_mfma_f32_16x16x32_bf16 v[36:39], v[92:95], v[136:139], v[36:39]\n" \
    "v_mfma_f32_16x16x32_bf16 v[32:35], v[92:95], v[132:135], v[32:35]\n" \
    "v_mfma_f32_16x16x32_bf16 v[28:31], v[92:95], v[128:131], v[28:31]\n" \
    "ds_read_b64 v[86:87], %[kvr] offset:1536\n" \
    "v_mfma_f32_16x16x32_bf16 v[24:27], v[92:95], v[124:127], v[24:27]\n" \
    "v_mfma_f32_16x16x32_bf16 v[20:23], v[92:95], v[120:123], v[20:23]\n" \
    "ds_read2_b32 v[92:93], v163 offset0:48 offset1:49\n" \
    "v_mfma_f32_16x16x32_bf16 v[44:47], v[96:99], v[140:143], v[44:47]\n" \
    "ds_read_b128 v[140:143], %[hp] offset:2752\n" \
    "v_mfma_f32_16x16x32_bf16 v[40:43], v[96:99], v[136:139], v[40:43]\n" \
    "ds_read2_b32 v[94:95], v163 offset0:50 offset1:51\n" \
    "v_mfma_f32_16x16x32_bf16 v[36:39], v[96:99], v[132:135], v[36:39]\n" \
    "v_mfma_f32_16x16x32_bf16 v[32:35], v[96:99], v[128:131], v[32:35]\n" \
    "v_mfma_f32_16x16x32_bf16 v[28:31], v[96:99], v[124:127], v[28:31]\n" \
    "v_mfma_f32_16x16x32_bf16 v[24:27], v[96:99], v[120:123], v[24:27]\n" \
    "ds_read2_b32 v[96:97], v162 offset0:192 offset1:193\n" \
    "v_mfma_f32_16x16x32_bf16 v[44:47], v[100:103], v[136:139], v[44:47]\n" \
    "ds_read_b128 v[136:139], %[hp] offset:2240\n" \
    "v_mfma_f32_16x16x32_bf16 v[40:43], v[100:103], v[132:135], v[40:43]\n" \
    "ds_read2_b32 v[98:99], v162 offset0:194 offset1:195\n" \
    "v_mfma_f32_16x16x32_bf16 v[36:39], v[100:103], v[128:131], v[36:39]\n" \
    "v_mfma_f32_16x16x32_bf16 v[32:35], v[100:103], v[124:127], v[32:35]\n" \
    "s_waitcnt lgkmcnt(6)\n" \
    "v_lshlrev_b32 v84, 16, v86\n" \
    "v_mfma_f32_16x16x32_bf16 v[28:31], v[100:103], v[120:123], v[28:31]\n" \
    "ds_read2_b32 v[100:101], v162 offset0:48 offset1:49\n" \
    "v_mfma_f32_16x16x32_bf16 v[44:47], v[104:107], v[132:135], v[44:47]\n" \
    "ds_read_b128 v[132:135], %[hp] offset:1728\n" \
    "v_and_b32 v85, 0xffff0000, v86\n" \
    "v_mfma_f32_16x16x32_bf16 v[40:43], v[104:107], v[128:131], v[40:43]\n" \
    "ds_read2_b32 v[102:103], v162 offset0:50 offset1:51\n" \
    "v_mfma_f32_16x16x32_bf16 v[36:39], v[104:107], v[124:127], v[36:39]\n" \
    "v_lshlrev_b32 v86, 16, v87\n" \
    "v_mfma_f32_16x16x32_bf16 v[32:35], v[104:107], v[120:123], v[32:35]\n" \
    "ds_read2_b32 v[104:105], v161 offset0:192 offset1:193\n" \
    "v_mfma_f32_16x16x32_bf16 v[44:47], v[108:111], v[128:131], v[44:47]\n" \
    "ds_read_b128 v[128:131], %[hp] offset:1216\n" \
    "v_and_b32 v87, 0xffff0000, v87\n" \
    "v_mfma_f32_16x16x32_bf16 v[40:43], v[108:111], v[124:127], v[40:43]\n" \
    "ds_read2_b32 v[106:107], v161 offset0:194 offset1:195\n" \
    "v_mfma_f32_16x16x32_bf16 v[36:39], v[108:111], v[120:123], v[36:39]\n" \
    "ds_read2_b32 v[108:109], v161 offset0:48 offset1:49\n" \
    "v_mfma_f32_16x16x32_bf16 v[44:47], v[112:115], v[124:127], v[44:47]\n" \
    "ds_read2_b32 v[110:111], v161 offset0:50 offset1:51\n" \
    "v_mfma_f32_16x16x32_bf16 v[40:43], v[112:115], v[120:123], v[40:43]\n" \
    "ds_read2_b32 v[112:113], v160 offset0:192 offset1:193\n" \
    "v_mfma_f32_16x16x32_bf16 v[44:47], v[116:119], v[120:123], v[44:47]\n" \
    "ds_read2_b32 v[114:115], v160 offset0:194 offset1:195\n" \
    "v_mfma_f32_16x16x32_bf16 v[44:47], v[88:91], v[148:151], v[44:47]\n" \
    "ds_read2_b32 v[116:117], v160 offset0:48 offset1:49\n" \
    "v_mfma_f32_16x16x32_bf16 v[40:43], v[88:91], v[144:147], v[40:43]\n" \
    "ds_read2_b32 v[118:119], v160 offset0:50 offset1:51\n" \
    "s_waitcnt lgkmcnt(15)\n" \
    "v_mfma_f32_16x16x32_bf16 v[36:39], v[88:91], v[140:143], v[36:39]\n" \
    "ds_read_b128 v[148:151], %[hp] offset:3840\n" \
    "s_waitcnt lgkmcnt(14)\n" \
    "v_mfma_f32_16x16x32_bf16 v[32:35], v[88:91], v[136:139], v[32:35]\n" \
    "ds_read_b128 v[124:127], %[hp] offset:768\n" \
    "s_waitcnt lgkmcnt(12)\n" \
    "v_mfma_f32_16x16x32_bf16 v[28:31], v[88:91], v[132:135], v[28:31]\n" \
    "ds_read_b128 v[120:123], %[hp] offset:256\n" \
    "s_waitcnt lgkmcnt(10)\n" \
    "v_mfma_f32_16x16x32_bf16 v[24:27], v[88:91], v[128:131], v[24:27]\n" \
    "v_mfma_f32_16x16x32_bf16 v[20:23], v[88:91], v[156:159], v[20:23]\n" \
    "v_mfma_f32_16x16x32_bf16 v[16:19], v[88:91], v[152:155], v[16:19]\n" \
    "ds_read2_b32 v[88:89], v163 offset0:216 offset1:217\n" \
    "v_fmac_f32 v28, %[dd], v84\n" \
    "v_fmac_f32 v29, %[dd], v85\n" \
    "v_mfma_f32_16x16x32_bf16 v[44:47], v[92:95], v[144:147], v[44:47]\n" \
    "ds_read2_b32 v[90:91], v163 offset0:218 offset1:219\n" \
    "v_fmac_f32 v30, %[dd], v86\n" \
    "v_fmac_f32 v31, %[dd], v87\n" \
    "v_mfma_f32_16x16x32_bf16 v[40:43], v[92:95], v[140:143], v[40:43]\n" \
    "ds_read_b128 v[144:147], %[hp] offset:3328\n" \
    "v_mfma_f32_16x16x32_bf16 v[36:39], v[92:95], v[136:139], v[36:39]\n" \
    "v_mfma_f32_16x16x32_bf16 v[32:35], v[92:95], v[132:135], v[32:35]\n" \
    "v_mfma_f32_16x16x32_bf16 v[28:31], v[92:95], v[128:131], v[28:31]\n" \
    "ds_read_b64 v[86:87], %[kvr] offset:2048\n" \
    "v_mfma_f32_16x16x32_bf16 v[24:27], v[92:95], v[156:159], v[24:27]\n" \
    "v_mfma_f32_16x16x32_bf16 v[20:23], v[92:95], v[152:155], v[20:23]\n" \
    "ds_read2_b32 v[92:93], v163 offset0:72 offset1:73\n" \
    "v_mfma_f32_16x16x32_bf16 v[44:47], v[96:99], v[140:143], v[44:47]\n" \
    "ds_read_b128 v[140:143], %[hp] offset:2816\n" \
    "v_mfma_f32_16x16x32_bf16 v[40:43], v[96:99], v[136:139], v[40:43]\n" \
    "ds_read2_b32 v[94:95], v163 offset0:74 offset1:75\n" \
    "v_mfma_f32_16x16x32_bf16 v[36:39], v[96:99], v[132:135], v[36:39]\n" \
    "v_mfma_f32_16x16x32_bf16 v[32:35], v[96:99], v[128:131], v[32:35]\n" \
    "v_mfma_f32_16x16x32_bf16 v[28:31], v[96:99], v[156:159], v[28:31]\n" \
    "v_mfma_f32_16x16x32_bf16 v[24:27], v[96:99], v[152:155], v[24:27]\n" \
    "ds_read2_b32 v[96:97], v162 offset0:216 offset1:217\n" \
    "v_mfma_f32_16x16x32_bf16 v[44:47], v[100:103], v[136:139], v[44:47]\n" \
    "ds_read_b128 v[136:139], %[hp] offset:2304\n" \
    "v_mfma_f32_16x16x32_bf16 v[40:43], v[100:103], v[132:135], v[40:43]\n" \
    "ds_read2_b32 v[98:99], v162 offset0:218 offset1:219\n" \
    "v_mfma_f32_16x16x32_bf16 v[36:39], v[100:103], v[128:131], v[36:39]\n" \
    "v_mfma_f32_16x16x32_bf16 v[32:35], v[100:103], v[156:159], v[32:35]\n" \
    "s_waitcnt lgkmcnt(6)\n" \
    "v_lshlrev_b32 v84, 16, v86\n" \
    "v_mfma_f32_16x16x32_bf16 v[28:31], v[100:103], v[152:155], v[28:31]\n" \
    "ds_read2_b32 v[100:101], v162 offset0:72 offset1:73\n" \
    "v_mfma_f32_16x16x32_bf16 v[44:47], v[104:107], v[132:135], v[44:47]\n" \
    "ds_read_b128 v[132:135], %[hp] offset:1792\n" \
    "v_and_b32 v85, 0xffff0000, v86\n" \
    "v_mfma_f32_16x16x32_bf16 v[40:43], v[104:107], v[128:131], v[40:43]\n" \
    "ds_read2_b32 v[102:103], v162 offset0:74 offset1:75\n" \
    "v_mfma_f32_16x16x32_bf16 v[36:39], v[104:107], v[156:159], v[36:39]\n" \
    "v_lshlrev_b32 v86, 16, v87\n" \
    "v_mfma_f32_16x16x32_bf16 v[32:35], v[104:107], v[152:155], v[32:35]\n" \
    "ds_read2_b32 v[104:105], v161 offset0:216 offset1:217\n" \
    "v_mfma_f32_16x16x32_bf16 v[44:47], v[108:111], v[128:131], v[44:47]\n" \
    "ds_read_b128 v[128:131], %[hp] offset:1280\n" \
    "v_and_b32 v87, 0xffff0000, v87\n" \
    "v_mfma_f32_16x16x32_bf16 v[40:43], v[108:111], v[156:159], v[40:43]\n" \
    "ds_read2_b32 v[106:107], v161 offset0:218 offset1:219\n" \
    "v_mfma_f32_16x16x32_bf16 v[36:39], v[108:111], v[152:155], v[36:39]\n" \
    "ds_read2_b32 v[108:109], v161 offset0:72 offset1:73\n" \
    "v_mfma_f32_16x16x32_bf16 v[44:47], v[112:115], v[156:159], v[44:47]\n" \
    "ds_read2_b32 v[110:111], v161 offset0:74 offset1:75\n" \
    "v_mfma_f32_16x16x32_bf16 v[40:43], v[112:115], v[152:155], v[40:43]\n" \
    "ds_read2_b32 v[112:113], v160 offset0:216 offset1:217\n" \
    "v_mfma_f32_16x16x32_bf16 v[44:47], v[116:119], v[152:155], v[44:47]\n" \
    "ds_read2_b32 v[114:115], v160 offset0:218 offset1:219\n" \
    "v_add_u32 v160, 288, v160\n" \
    "v_add_u32 v161, 288, v161\n" \
    "v_add_u32 v162, 288, v162\n" \
    "v_add_u32 v163, 288, v163\n" \
    "v_mfma_f32_16x16x32_bf16 v[44:47], v[88:91], v[148:151], v[44:47]\n" \
    "ds_read2_b32 v[116:117], v160 offset0:0 offset1:1\n" \
    "v_mfma_f32_16x16x32_bf16 v[40:43], v[88:91], v[144:147], v[40:43]\n" \
    "ds_read2_b32 v[118:119], v160 offset0:2 offset1:3\n" \
    "s_waitcnt lgkmcnt(15)\n" \
    "v_mfma_f32_16x16x32_bf16 v[36:39], v[88:91], v[140:143], v[36:39]\n" \
    "ds_read_b128 v[148:151], %[hp] offset:3904\n" \
    "s_waitcnt lgkmcnt(14)\n" \
    "v_mfma_f32_16x16x32_bf16 v[32:35], v[88:91], v[136:139], v[32:35]\n" \
    "ds_read_b128 v[156:159], %[hp] offset:832\n" \
    "s_waitcnt lgkmcnt(12)\n" \
    "v_mfma_f32_16x16x32_bf16 v[28:31], v[88:91], v[132:135], v[28:31]\n" \
    "ds_read_b128 v[152:155], %[hp] offset:320\n" \
    "s_waitcnt lgkmcnt(10)\n" \
    "v_mfma_f32_16x16x32_bf16 v[24:27], v[88:91], v[128:131], v[24:27]\n" \
    "v_mfma_f32_16x16x32_bf16 v[20:23], v[88:91], v[124:127], v[20:23]\n" \
    "v_fmac_f32 v32, %[dd], v84\n" \
    "v_fmac_f32 v33, %[dd], v85\n" \
    "v_mfma_f32_16x16x32_bf16 v[16:19], v[88:91], v[120:123], v[16:19]\n" \
    "ds_read2_b32 v[88:89], v163 offset0:160 offset1:161\n" \
    "v_fmac_f32 v34, %[dd], v86\n" \
    "v_fmac_f32 v35, %[dd], v87\n" \
    "v_mfma_f32_16x16x32_bf16 v[44:47], v[92:95], v[144:147], v[44:47]\n" \
    "ds_read2_b32 v[90:91], v163 offset0:162 offset1:163\n" \
    "v_mfma_f32_16x16x32_bf16 v[40:43], v[92:95], v[140:143], v[40:43]\n" \
    "ds_read_b128 v[144:147], %[hp] offset:3392\n" \
    "v_mfma_f32_16x16x32_bf16 v[36:39], v[92:95], v[136:139], v[36:39]\n" \
    "v_mfma_f32_16x16x32_bf16 v[32:35], v[92:95], v[132:135], v[32:35]\n" \
    "v_mfma_f32_16x16x32_bf16 v[28:31], v[92:95], v[128:131], v[28:31]\n" \
    "ds_read_b64 v[86:87], %[kvr] offset:2560\n" \
    "v_mfma_f32_16x16x32_bf16 v[24:27], v[92:95], v[124:127], v[24:27]\n" \
    "v_mfma_f32_16x16x32_bf16 v[20:23], v[92:95], v[120:123], v[20:23]\n" \
    "ds_read2_b32 v[92:93], v163 offset0:16 offset1:17\n" \
    "v_mfma_f32_16x16x32_bf16 v[44:47], v[96:99], v[140:143], v[44:47]\n" \
    "ds_read_b128 v[140:143], %[hp] offset:2880\n" \
    "v_mfma_f32_16x16x32_bf16 v[40:43], v[96:99], v[136:139], v[40:43]\n" \
    "ds_read2_b32 v[94:95], v163 offset0:18 offset1:19\n" \
    "v_mfma_f32_16x16x32_bf16 v[36:39], v[96:99], v[132:135], v[36:39]\n" \
    "v_mfma_f32_16x16x32_bf16 v[32:35], v[96:99], v[128:131], v[32:35]\n" \
    "v_mfma_f32_16x16x32_bf16 v[28:31], v[96:99], v[124:127], v[28:31]\n" \
    "v_mfma_f32_16x16x32_bf16 v[24:27], v[96:99], v[120:123], v[24:27]\n" \
    "ds_read2_b32 v[96:97], v162 offset0:160 offset1:161\n" \
    "v_mfma_f32_16x16x32_bf16 v[44:47], v[100:103], v[136:139], v[44:47]\n" \
    "ds_read_b128 v[136:139], %[hp] offset:2368\n" \
    "v_mfma_f32_16x16x32_bf16 v[40:43], v[100:103], v[132:135], v[40:43]\n" \
    "ds_read2_b32 v[98:99], v162 offset0:162 offset1:163\n" \
    "v_mfma_f32_16x16x32_bf16 v[36:39], v[100:103], v[128:131], v[36:39]\n" \
    "v_mfma_f32_16x16x32_bf16 v[32:35], v[100:103], v[124:127], v[32:35]\n" \
    "s_waitcnt lgkmcnt(6)\n" \
    "v_lshlrev_b32 v84, 16, v86\n" \
    "v_mfma_f32_16x16x32_bf16 v[28:31], v[100:103], v[120:123], v[28:31]\n" \
    "ds_read2_b32 v[100:101], v162 offset0:16 offset1:17\n" \
    "v_mfma_f32_16x16x32_bf16 v[44:47], v[104:107], v[132:135], v[44:47]\n" \
    "ds_read_b128 v[132:135], %[hp] offset:1856\n" \
    "v_and_b32 v85, 0xffff0000, v86\n" \
    "v_mfma_f32_16x16x32_bf16 v[40:43], v[104:107], v[128:131], v[40:43]\n" \
    "ds_read2_b32 v[102:103], v162 offset0:18 offset1:19\n" \
    "v_mfma_f32_16x16x32_bf16 v[36:39], v[104:107], v[124:127], v[36:39]\n" \
    "v_lshlrev_b32 v86, 16, v87\n" \
    "v_mfma_f32_16x16x32_bf16 v[32:35], v[104:107], v[120:123], v[32:35]\n" \
    "ds_read2_b32 v[104:105], v161 offset0:160 offset1:161\n" \
    "v_mfma_f32_16x16x32_bf16 v[44:47], v[108:111], v[128:131], v[44:47]\n" \
    "ds_read_b128 v[128:131], %[hp] offset:1344\n" \
    "v_and_b32 v87, 0xffff0000, v87\n" \
    "v_mfma_f32_16x16x32_bf16 v[40:43], v[108:111], v[124:127], v[40:43]\n" \
    "ds_read2_b32 v[106:107], v161 offset0:162 offset1:163\n" \
    "v_mfma_f32_16x16x32_bf16 v[36:39], v[108:111], v[120:123], v[36:39]\n" \
    "ds_read2_b32 v[108:109], v161 offset0:16 offset1:17\n" \
    "v_mfma_f32_16x16x32_bf16 v[44:47], v[112:115], v[124:127], v[44:47]\n" \
    "ds_read2_b32 v[110:111], v161 offset0:18 offset1:19\n" \
    "v_mfma_f32_16x16x32_bf16 v[40:43], v[112:115], v[120:123], v[40:43]\n" \
    "ds_read2_b32 v[112:113], v160 offset0:160 offset1:161\n" \
    "v_mfma_f32_16x16x32_bf16 v[44:47], v[116:119], v[120:123], v[44:47]\n" \
    "ds_read2_b32 v[114:115], v160 offset0:162 offset1:163\n" \
    "v_mfma_f32_16x16x32_bf16 v[44:47], v[88:91], v[148:151], v[44:47]\n" \
    "ds_read2_b32 v[116:117], v160 offset0:16 offset1:17\n" \
    "v_mfma_f32_16x16x32_bf16 v[40:43], v[88:91], v[144:147], v[40:43]\n" \
    "ds_read2_b32 v[118:119], v160 offset0:18 offset1:19\n" \
    "s_waitcnt lgkmcnt(15)\n" \
    "v_mfma_f32_16x16x32_bf16 v[36:39], v[88:91], v[140:143], v[36:39]\n" \
    "ds_read_b128 v[148:151], %[hp] offset:3968\n" \
    "s_waitcnt lgkmcnt(14)\n" \
    "v_mfma_f32_16x16x32_bf16 v[32:35], v[88:91], v[136:139], v[32:35]\n" \
    "ds_read_b128 v[124:127], %[hp] offset:896\n" \
    "s_waitcnt lgkmcnt(12)\n" \
    "v_mfma_f32_16x16x32_bf16 v[28:31], v[88:91], v[132:135], v[28:31]\n" \
    "ds_read_b128 v[120:123], %[hp] offset:384\n" \
    "s_waitcnt lgkmcnt(10)\n" \
    "v_mfma_f32_16x16x32_bf16 v[24:27], v[88:91], v[128:131], v[24:27]\n" \
    "v_fmac_f32 v36, %[dd], v84\n" \
    "v_fmac_f32 v37, %[dd], v85\n" \
    "v_mfma_f32_16x16x32_bf16 v[20:23], v[88:91], v[156:159], v[20:23]\n" \
    "v_fmac_f32 v38, %[dd], v86\n" \
    "v_fmac_f32 v39, %[dd], v87\n" \
    "v_mfma_f32_16x16x32_bf16 v[16:19], v[88:91], v[152:155], v[16:19]\n" \
    "ds_read2_b32 v[88:89], v163 offset0:176 offset1:177\n" \
    "v_mfma_f32_16x16x32_bf16 v[44:47], v[92:95], v[144:147], v[44:47]\n" \
    "ds_read2_b32 v[90:91], v163 offset0:178 offset1:179\n" \
    "v_mfma_f32_16x16x32_bf16 v[40:43], v[92:95], v[140:143], v[40:43]\n" \
    "ds_read_b128 v[144:147], %[hp] offset:3456\n" \
    "v_mfma_f32_16x16x32_bf16 v[36:39], v[92:95], v[136:139], v[36:39]\n" \
    "v_mfma_f32_16x16x32_bf16 v[32:35], v[92:95], v[132:135], v[32:35]\n" \
    "v_mfma_f32_16x16x32_bf16 v[28:31], v[92:95], v[128:131], v[28:31]\n" \
    "ds_read_b64 v[86:87], %[kvr] offset:3072\n" \
    "v_mfma_f32_16x16x32_bf16 v[24:27], v[92:95], v[156:159], v[24:27]\n" \
    "v_mfma_f32_16x16x32_bf16 v[20:23], v[92:95], v[152:155], v[20:23]\n" \
    "ds_read2_b32 v[92:93], v163 offset0:32 offset1:33\n" \
    "v_mfma_f32_16x16x32_bf16 v[44:47], v[96:99], v[140:143], v[44:47]\n" \
    "ds_read_b128 v[140:143], %[hp] offset:2944\n" \
    "v_mfma_f32_16x16x32_bf16 v[40:43], v[96:99], v[136:139], v[40:43]\n" \
    "ds_read2_b32 v[94:95], v163 offset0:34 offset1:35\n" \
    "v_mfma_f32_16x16x32_bf16 v[36:39], v[96:99], v[132:135], v[36:39]\n" \
    "v_mfma_f32_16x16x32_bf16 v[32:35], v[96:99], v[128:131], v[32:35]\n" \
    "v_mfma_f32_16x16x32_bf16 v[28:31], v[96:99], v[156:159], v[28:31]\n" \
    "v_mfma_f32_16x16x32_bf16 v[24:27], v[96:99], v[152:155], v[24:27]\n" \
    "ds_read2_b32 v[96:97], v162 offset0:176 offset1:177\n" \
    "v_mfma_f32_16x16x32_bf16 v[44:47], v[100:103], v[136:139], v[44:47]\n" \
    "ds_read_b128 v[136:139], %[hp] offset:2432\n" \
    "v_mfma_f32_16x16x32_bf16 v[40:43], v[100:103], v[132:135], v[40:43]\n" \
    "ds_read2_b32 v[98:99], v162 offset0:178 offset1:179\n" \
    "v_mfma_f32_16x16x32_bf16 v[36:39], v[100:103], v[128:131], v[36:39]\n" \
    "v_mfma_f32_16x16x32_bf16 v[32:35], v[100:103], v[156:159], v[32:35]\n" \
    "s_waitcnt lgkmcnt(6)\n" \
    "v_lshlrev_b32 v84, 16, v86\n" \
    "v_mfma_f32_16x16x32_bf16 v[28:31], v[100:103], v[152:155], v[28:31]\n" \
    "ds_read2_b32 v[100:101], v162 offset0:32 offset1:33\n" \
    "v_mfma_f32_16x16x32_bf16 v[44:47], v[104:107], v[132:135], v[44:47]\n" \
    "ds_read_b128 v[132:135], %[hp] offset:1920\n" \
    "v_and_b32 v85, 0xffff0000, v86\n" \
    "v_mfma_f32_16x16x32_bf16 v[40:43], v[104:107], v[128:131], v[40:43]\n" \
    "ds_read2_b32 v[102:103], v162 offset0:34 offset1:35\n" \
    "v_mfma_f32_16x16x32_bf16 v[36:39], v[104:107], v[156:159], v[36:39]\n" \
    "v_lshlrev_b32 v86, 16, v87\n" \
    "v_mfma_f32_16x16x32_bf16 v[32:35], v[104:107], v[152:155], v[32:35]\n" \
    "ds_read2_b32 v[104:105], v161 offset0:176 offset1:177\n" \
    "v_mfma_f32_16x16x32_bf16 v[44:47], v[108:111], v[128:131], v[44:47]\n" \
    "ds_read_b128 v[128:131], %[hp] offset:1408\n" \
    "v_and_b32 v87, 0xffff0000, v87\n" \
    "v_mfma_f32_16x16x32_bf16 v[40:43], v[108:111], v[156:159], v[40:43]\n" \
    "ds_read2_b32 v[106:107], v161 offset0:178 offset1:179\n" \
    "v_mfma_f32_16x16x32_bf16 v[36:39], v[108:111], v[152:155], v[36:39]\n" \
    "ds_read2_b32 v[108:109], v161 offset0:32 offset1:33\n" \
    "v_mfma_f32_16x16x32_bf16 v[44:47], v[112:115], v[156:159], v[44:47]\n" \
    "ds_read2_b32 v[110:111], v161 offset0:34 offset1:35\n" \
    "v_mfma_f32_16x16x32_bf16 v[40:43], v[112:115], v[152:155], v[40:43]\n" \
    "ds_read2_b32 v[112:113], v160 offset0:176 offset1:177\n" \
    "v_mfma_f32_16x16x32_bf16 v[44:47], v[116:119], v[152:155], v[44:47]\n" \
    "ds_read2_b32 v[114:115], v160 offset0:178 offset1:179\n" \
    "v_mfma_f32_16x16x32_bf16 v[44:47], v[88:91], v[148:151], v[44:47]\n" \
    "ds_read2_b32 v[116:117], v160 offset0:32 offset1:33\n" \
    "v_mfma_f32_16x16x32_bf16 v[40:43], v[88:91], v[144:147], v[40:43]\n" \
    "ds_read2_b32 v[118:119], v160 offset0:34 offset1:35\n" \
    "s_waitcnt lgkmcnt(15)\n" \
    "v_mfma_f32_16x16x32_bf16 v[36:39], v[88:91], v[140:143], v[36:39]\n" \
    "ds_read_b128 v[152:155], %[hp] offset:448\n" \
    "s_waitcnt lgkmcnt(14)\n" \
    "v_mfma_f32_16x16x32_bf16 v[32:35], v[88:91], v[136:139], v[32:35]\n" \
    "ds_read_b128 v[156:159], %[hp] offset:960\n" \
    "s_waitcnt lgkmcnt(12)\n" \
    "v_mfma_f32_16x16x32_bf16 v[28:31], v[88:91], v[132:135], v[28:31]\n" \
    "ds_read_b128 v[148:151], %[hp] offset:4032\n" \
    "v_fmac_f32 v40, %[dd], v84\n" \
    "v_fmac_f32 v41, %[dd], v85\n" \
    "s_waitcnt lgkmcnt(10)\n" \
    "v_mfma_f32_16x16x32_bf16 v[24:27], v[88:91], v[128:131], v[24:27]\n" \
    "v_fmac_f32 v42, %[dd], v86\n" \
    "v_fmac_f32 v43, %[dd], v87\n" \
    "v_mfma_f32_16x16x32_bf16 v[20:23], v[88:91], v[124:127], v[20:23]\n" \
    "v_mfma_f32_16x16x32_bf16 v[16:19], v[88:91], v[120:123], v[16:19]\n" \
    "ds_read2_b32 v[88:89], v163 offset0:192 offset1:193\n" \
    "v_mfma_f32_16x16x32_bf16 v[44:47], v[92:95], v[144:147], v[44:47]\n" \
    "ds_read2_b32 v[90:91], v163 offset0:194 offset1:195\n" \
    "v_mfma_f32_16x16x32_bf16 v[40:43], v[92:95], v[140:143], v[40:43]\n" \
    "ds_read_b128 v[144:147], %[hp] offset:3520\n" \
    "v_mfma_f32_16x16x32_bf16 v[36:39], v[92:95], v[136:139], v[36:39]\n" \
    "v_mfma_f32_16x16x32_bf16 v[32:35], v[92:95], v[132:135], v[32:35]\n" \
    "v_mfma_f32_16x16x32_bf16 v[28:31], v[92:95], v[128:131], v[28:31]\n" \
    "ds_read_b64 v[86:87], %[kvr] offset:3584\n" \
    "v_mfma_f32_16x16x32_bf16 v[24:27], v[92:95], v[124:127], v[24:27]\n" \
    "v_mfma_f32_16x16x32_bf16 v[20:23], v[92:95], v[120:123], v[20:23]\n" \
    "ds_read2_b32 v[92:93], v163 offset0:48 offset1:49\n" \
    "v_mfma_f32_16x16x32_bf16 v[44:47], v[96:99], v[140:143], v[44:47]\n" \
    "ds_read2_b32 v[94:95], v163 offset0:50 offset1:51\n" \
    "v_mfma_f32_16x16x32_bf16 v[40:43], v[96:99], v[136:139], v[40:43]\n" \
    "ds_read_b128 v[140:143], %[hp] offset:3008\n" \
    "v_mfma_f32_16x16x32_bf16 v[36:39], v[96:99], v[132:135], v[36:39]\n" \
    "v_mfma_f32_16x16x32_bf16 v[32:35], v[96:99], v[128:131], v[32:35]\n" \
    "v_mfma_f32_16x16x32_bf16 v[28:31], v[96:99], v[124:127], v[28:31]\n" \
    "v_mfma_f32_16x16x32_bf16 v[24:27], v[96:99], v[120:123], v[24:27]\n" \
    "ds_read2_b32 v[96:97], v162 offset0:192 offset1:193\n" \
    "v_mfma_f32_16x16x32_bf16 v[44:47], v[100:103], v[136:139], v[44:47]\n" \
    "ds_read2_b32 v[98:99], v162 offset0:194 offset1:195\n" \
    "v_mfma_f32_16x16x32_bf16 v[40:43], v[100:103], v[132:135], v[40:43]\n" \
    "ds_read_b128 v[136:139], %[hp] offset:2496\n" \
    "v_mfma_f32_16x16x32_bf16 v[36:39], v[100:103], v[128:131], v[36:39]\n" \
    "v_mfma_f32_16x16x32_bf16 v[32:35], v[100:103], v[124:127], v[32:35]\n" \
    "s_waitcnt lgkmcnt(6)\n" \
    "v_lshlrev_b32 v84, 16, v86\n" \
    "v_mfma_f32_16x16x32_bf16 v[28:31], v[100:103], v[120:123], v[28:31]\n" \
    "ds_read2_b32 v[100:101], v162 offset0:48 offset1:49\n" \
    "v_mfma_f32_16x16x32_bf16 v[44:47], v[104:107], v[132:135], v[44:47]\n" \
    "ds_read_b128 v[132:135], %[hp] offset:1984\n" \
    "v_and_b32 v85, 0xffff0000, v86\n" \
    "v_mfma_f32_16x16x32_bf16 v[40:43], v[104:107], v[128:131], v[40:43]\n" \
    "ds_read2_b32 v[102:103], v162 offset0:50 offset1:51\n" \
    "v_mfma_f32_16x16x32_bf16 v[36:39], v[104:107], v[124:127], v[36:39]\n" \
    "v_lshlrev_b32 v86, 16, v87\n" \
    "v_mfma_f32_16x16x32_bf16 v[32:35], v[104:107], v[120:123], v[32:35]\n" \
    "ds_read2_b32 v[104:105], v161 offset0:192 offset1:193\n" \
    "v_mfma_f32_16x16x32_bf16 v[44:47], v[108:111], v[128:131], v[44:47]\n" \
    "ds_read_b128 v[128:131], %[hp] offset:1472\n" \
    "v_and_b32 v87, 0xffff0000, v87\n" \
    "v_mfma_f32_16x16x32_bf16 v[40:43], v[108:111], v[124:127], v[40:43]\n" \
    "ds_read2_b32 v[106:107], v161 offset0:194 offset1:195\n" \
    "v_mfma_f32_16x16x32_bf16 v[36:39], v[108:111], v[120:123], v[36:39]\n" \
    "ds_read2_b32 v[108:109], v161 offset0:48 offset1:49\n" \
    "v_mfma_f32_16x16x32_bf16 v[44:47], v[112:115], v[124:127], v[44:47]\n" \
    "ds_read2_b32 v[110:111], v161 offset0:50 offset1:51\n" \
    "v_mfma_f32_16x16x32_bf16 v[40:43], v[112:115], v[120:123], v[40:43]\n" \
    "ds_read2_b32 v[112:113], v160 offset0:192 offset1:193\n" \
    "v_mfma_f32_16x16x32_bf16 v[44:47], v[116:119], v[120:123], v[44:47]\n" \
    "ds_read2_b32 v[114:115], v160 offset0:194 offset1:195\n" \
    "v_mfma_f32_16x16x32_bf16 v[16:19], v[88:91], v[152:155], v[16:19]\n" \
    "ds_read2_b32 v[116:117], v160 offset0:48 offset1:49\n" \
    "v_mfma_f32_16x16x32_bf16 v[20:23], v[88:91], v[156:159], v[20:23]\n" \
    "ds_read2_b32 v[118:119], v160 offset0:50 offset1:51\n" \
    "s_waitcnt lgkmcnt(15)\n" \
    "v_mfma_f32_16x16x32_bf16 v[20:23], v[92:95], v[152:155], v[20:23]\n" \
    "ds_read_b32 v50, %[ctl] offset:88\n" \
    "s_waitcnt lgkmcnt(8)\n" \
    "v_mfma_f32_16x16x32_bf16 v[24:27], v[88:91], v[128:131], v[24:27]\n" \
    "v_fmac_f32 v44, %[dd], v84\n" \
    "v_fmac_f32 v45, %[dd], v85\n" \
    "v_mfma_f32_16x16x32_bf16 v[24:27], v[92:95], v[156:159], v[24:27]\n" \
    "v_fmac_f32 v46, %[dd], v86\n" \
    "v_fmac_f32 v47, %[dd], v87\n" \
    "v_mfma_f32_16x16x32_bf16 v[24:27], v[96:99], v[152:155], v[24:27]\n" \
    "v_mfma_f32_16x16x32_bf16 v[28:31], v[88:91], v[132:135], v[28:31]\n" \
    "v_cvt_pk_bf16_f32 v52, v16, v17\n" \
    "v_cvt_pk_bf16_f32 v54, v20, v21\n" \
    "v_mfma_f32_16x16x32_bf16 v[28:31], v[92:95], v[128:131], v[28:31]\n" \
    "v_cvt_pk_bf16_f32 v53, v18, v19\n" \
    "v_cvt_pk_bf16_f32 v55, v22, v23\n" \
    "v_mfma_f32_16x16x32_bf16 v[28:31], v[96:99], v[156:159], v[28:31]\n" \
    "v_min_u32 v52, -2, v52\n" \
    "v_min_u32 v54, -2, v54\n" \
    "v_mfma_f32_16x16x32_bf16 v[28:31], v[100:103], v[152:155], v[28:31]\n" \
    "v_min_u32 v53, -2, v53\n" \
    "v_min_u32 v55, -2, v55\n" \
    "v_mfma_f32_16x16x32_bf16 v[32:35], v[88:91], v[136:139], v[32:35]\n" \
    "v_permlane16_swap_b32 v52, v54\n" \
    "v_add_u32 v68, 0, %[yo]\n" \
    "v_mfma_f32_16x16x32_bf16 v[32:35], v[92:95], v[132:135], v[32:35]\n" \
    "v_permlane16_swap_b32 v53, v55\n" \
    "v_mfma_f32_16x16x32_bf16 v[32:35], v[96:99], v[128:131], v[32:35]\n" \
    "s_waitcnt lgkmcnt(0)\n" \
    "v_readfirstlane_b32 %[tmp], v50\n" \
    "s_mov_b32 %[cnt], 0\n" \
    "s_cmp_lg_u32 %[tmp], 0\n" \
    "s_cbranch_scc1 Lok_%=\n" \
    "Ls2_%=:\n" \
    "ds_read_b32 v50, %[ctl] offset:88\n" \
    "s_add_u32 %[cnt], %[cnt], 1\n" \
    "s_waitcnt lgkmcnt(0)\n" \
    "v_readfirstlane_b32 %[tmp], v50\n" \
    "s_cmp_ge_u32 %[cnt], %[cap]\n" \
    "s_cbranch_scc1 Lok_%=\n" \
    "s_cmp_eq_u32 %[tmp], 0\n" \
    "s_cbranch_scc1 Ls2_%=\n" \
    "Lok_%=:\n" \
    "global_store_dwordx4 v68, v[52:55], %[wsp] sc1\n" \
    "v_mfma_f32_16x16x32_bf16 v[32:35], v[100:103], v[156:159], v[32:35]\n" \
    "v_cvt_pk_bf16_f32 v56, v24, v25\n" \
    "v_cvt_pk_bf16_f32 v58, v28, v29\n" \
    "v_mfma_f32_16x16x32_bf16 v[32:35], v[104:107], v[152:155], v[32:35]\n" \
    "v_cvt_pk_bf16_f32 v57, v26, v27\n" \
    "v_cvt_pk_bf16_f32 v59, v30, v31\n" \
    "v_mfma_f32_16x16x32_bf16 v[36:39], v[88:91], v[140:143], v[36:39]\n" \
    "v_min_u32 v56, -2, v56\n" \
    "v_min_u32 v58, -2, v58\n" \
    "v_mfma_f32_16x16x32_bf16 v[36:39], v[92:95], v[136:139], v[36:39]\n" \
    "v_min_u32 v57, -2, v57\n" \
    "v_min_u32 v59, -2, v59\n" \
    "v_mfma_f32_16x16x32_bf16 v[36:39], v[96:99], v[132:135], v[36:39]\n" \
    "v_permlane16_swap_b32 v56, v58\n" \
    "v_add_u32 v69, 655360, %[yo]\n" \
    "v_mfma_f32_16x16x32_bf16 v[36:39], v[100:103], v[128:131], v[36:39]\n" \
    "v_permlane16_swap_b32 v57, v59\n" \
    "v_mfma_f32_16x16x32_bf16 v[36:39], v[104:107], v[156:159], v[36:39]\n" \
    "global_store_dwordx4 v69, v[56:59], %[wsp] sc1\n" \
    "v_mfma_f32_16x16x32_bf16 v[36:39], v[108:111], v[152:155], v[36:39]\n" \
    "v_mfma_f32_16x16x32_bf16 v[40:43], v[88:91], v[144:147], v[40:43]\n" \
    "v_mfma_f32_16x16x32_bf16 v[40:43], v[92:95], v[140:143], v[40:43]\n" \
    "v_mfma_f32_16x16x32_bf16 v[40:43], v[96:99], v[136:139], v[40:43]\n" \
    "v_mfma_f32_16x16x32_bf16 v[40:43], v[100:103], v[132:135], v[40:43]\n" \
    "v_cvt_pk_bf16_f32 v60, v32, v33\n" \
    "v_cvt_pk_bf16_f32 v62, v36, v37\n" \
    "v_mfma_f32_16x16x32_bf16 v[40:43], v[104:107], v[128:131], v[40:43]\n" \
    "v_cvt_pk_bf16_f32 v61, v34, v35\n" \
    "v_cvt_pk_bf16_f32 v63, v38, v39\n" \
    "v_mfma_f32_16x16x32_bf16 v[40:43], v[108:111], v[156:159], v[40:43]\n" \
    "v_min_u32 v60, -2, v60\n" \
    "v_min_u32 v62, -2, v62\n" \
    "v_mfma_f32_16x16x32_bf16 v[40:43], v[112:115], v[152:155], v[40:43]\n" \
    "v_min_u32 v61, -2, v61\n" \
    "v_min_u32 v63, -2, v63\n" \
    "v_mfma_f32_16x16x32_bf16 v[44:47], v[88:91], v[148:151], v[44:47]\n" \
    "v_permlane16_swap_b32 v60, v62\n" \
    "v_add_u32 v70, 1310720, %[yo]\n" \
    "v_mfma_f32_16x16x32_bf16 v[44:47], v[92:95], v[144:147], v[44:47]\n" \
    "v_permlane16_swap_b32 v61, v63\n" \
    "v_mfma_f32_16x16x32_bf16 v[44:47], v[96:99], v[140:143], v[44:47]\n" \
    "global_store_dwordx4 v70, v[60:63], %[wsp] sc1\n" \
    "v_mfma_f32_16x16x32_bf16 v[44:47], v[100:103], v[136:139], v[44:47]\n" \
    "v_mfma_f32_16x16x32_bf16 v[44:47], v[104:107], v[132:135], v[44:47]\n" \
    "v_mfma_f32_16x16x32_bf16 v[44:47], v[108:111], v[128:131], v[44:47]\n" \
    "v_mfma_f32_16x16x32_bf16 v[44:47], v[112:115], v[156:159], v[44:47]\n" \
    "v_mfma_f32_16x16x32_bf16 v[44:47], v[116:119], v[152:155], v[44:47]\n" \
    "s_nop 7\n" \
    "v_cvt_pk_bf16_f32 v64, v40, v41\n" \
    "v_cvt_pk_bf16_f32 v66, v44, v45\n" \
    "v_cvt_pk_bf16_f32 v65, v42, v43\n" \
    "v_cvt_pk_bf16_f32 v67, v46, v47\n" \
    "v_min_u32 v64, -2, v64\n" \
    "v_min_u32 v66, -2, v66\n" \
    "v_min_u32 v65, -2, v65\n" \
    "v_min_u32 v67, -2, v67\n" \
    "v_permlane16_swap_b32 v64, v66\n" \
    "v_add_u32 v71, 1966080, %[yo]\n" \
    "v_permlane16_swap_b32 v65, v67\n" \
    "global_store_dwordx4 v71, v[64:67], %[wsp] sc1\n" \
    "s_nop 1\n"
#define CONV_CLOB "v16", "v17", "v18", "v19", "v20", "v21", "v22", "v23", "v24", "v25", "v26", "v27", "v28", "v29", "v30", "v31", "v32", "v33", "v34", "v35", "v36", "v37", "v38", "v39", "v40", "v41", "v42", "v43", "v44", "v45", "v46", "v47", "v50", "v51", "v52", "v53", "v54", "v55", "v56", "v57", "v58", "v59", "v60", "v61", "v62", "v63", "v64", "v65", "v66", "v67", "v68", "v69", "v70", "v71", "v72", "v73", "v74", "v75", "v76", "v77", "v78", "v79", "v80", "v81", "v82", "v83", "v84", "v85", "v86", "v87", "v88", "v89", "v90", "v91", "v92", "v93", "v94", "v95", "v96", "v97", "v98", "v99", "v100", "v101", "v102", "v103", "v104", "v105", "v106", "v107", "v108", "v109", "v110", "v111", "v112", "v113", "v114", "v115", "v116", "v117", "v118", "v119", "v120", "v121", "v122", "v123", "v124", "v125", "v126", "v127", "v128", "v129", "v130", "v131", "v132", "v133", "v134", "v135", "v136", "v137", "v138", "v139", "v140", "v141", "v142", "v143", "v144", "v145", "v146", "v147", "v148", "v149", "v150", "v151", "v152", "v153", "v154", "v155", "v156", "v157", "v158", "v159", "v160", "v161", "v162", "v163", "scc", "memory"

static_assert(C_HR - L_CTRL == 144 && C_RED - L_CTRL == 84 && C_XD - L_CTRL == 80 && C_OWN - L_CTRL == 88, "offsets in the conv stream");
typedef __attribute__((ext_vector_type(16))) unsigned u32x16;
typedef __attribute__((address_space(3))) const unsigned lds_cu32;
typedef __attribute__((address_space(3))) u32x2 lds_u32x2;
typedef __attribute__((address_space(3))) u32x4 lds_u32x4;
typedef __attribute__((address_space(3))) f32x4 lds_f32x4;
#define GPTR(T, lo, hi) ((T*)(__attribute__((address_space(1))) T*)(((u64)(hi) << 32) | (u64)(lo)))
#if defined(__HIP_DEVICE_COMPILE__)
#define NOPK __attribute__((target("no-packed-fp32-ops")))
#else
#define NOPK
#endif
__global__ NOPK __launch_bounds__(768, 3) void fused_kernel(const u16* x_, const u16* wqkv_, const u16* kin_, const u16* vin_, const u16* shift_k_,
                                                       const u16* ssm_k_, const u16* ssm_D_, const u16* wo_,
                                                       const u16* gw_, char* ws_, u16* out_) {
    u32x16 ka0;
    u32x4 ka1;
    u32x2 ka2;
    asm volatile("s_load_dwordx16 %0, %3, 0\n\ts_load_dwordx4 %1, %3, 64\n\ts_load_dwordx2 %2, %3, 80"
                 : "=&s"(ka0), "=&s"(ka1), "=&s"(ka2) : "s"(__builtin_amdgcn_kernarg_segment_ptr()));
#define x GPTR(const u16, ka0[0], ka0[1])
#define wqkv GPTR(const u16, ka0[2], ka0[3])
#define kin GPTR(const u16, ka0[4], ka0[5])
#define vin GPTR(const u16, ka0[6], ka0[7])
#define shift_k GPTR(const u16, ka0[8], ka0[9])
#define ssm_k GPTR(const u16, ka0[10], ka0[11])
#define ssm_D GPTR(const u16, ka0[12], ka0[13])
#define wo GPTR(const u16, ka0[14], ka0[15])
#define gw GPTR(const u16, ka1[0], ka1[1])
#define ws GPTR(char, ka1[2], ka1[3])
#define out GPTR(u16, ka2[0], ka2[1])
#define wsr __builtin_amdgcn_make_buffer_rsrc(ws, 0, 0x4000000, 0x00020000)
    __shared__ __attribute__((aligned(16))) char smem[LDS_TOTAL];
    const int bid = __builtin_amdgcn_workgroup_id_x();
    const int tid = __builtin_amdgcn_workitem_id_x();
    const int lane = tid & 63;
    const int wave = __builtin_amdgcn_readfirstlane(tid >> 6);
    const int xcd = bid & 7, r5 = bid >> 3, r = r5 & 15;
    const bool isG = r5 >= 16;
    const int hg = (xcd & 1) * 4 + (r & 3);
    const int rb2 = (xcd >> 1) * 4 + (r >> 2);
    const int cq = (xcd & 1) * 2 + (r & 1), rbv = (xcd >> 1) * 8 + (r >> 1);
    const int vid = (cq ^ CQ_X) * 32 + rbv;
    const int sid = rbv * 4 + cq;
    const int rbo = ((xcd + X_SHIFT) & 7) * 4 + (r >> 3) * 2 + (r5 >> 4), oc = r5 & 7;
    const int rto = (rbo >> 1) * 8 + (rbo & 1) * 2;
#define ROWT(i) (rto + ((i) & 1) + ((i) >> 1) * 4)
#define DUE_H ph
    const int w = wave & 3;
    const int g = lane >> 4, n16 = lane & 15, q4 = n16 >> 2, p4 = lane & 3;
    const unsigned lb = (unsigned)(size_t)(lds_char*)smem;
    u64 tag;
    {
        u64 id = llvm_dispatch_id();
        u64 dp = (u64)(size_t)__builtin_amdgcn_dispatch_ptr();
        tag = ((u64)((unsigned)id ^ 0x5bd1e995u) << 32) | (u64)((unsigned)(dp >> 6) ^ (unsigned)(id >> 32) ^ 0x9e3779b9u);
    }
    const unsigned tlo = (unsigned)tag, thi = (unsigned)(tag >> 32);
    u64 code0;
    asm volatile("s_getpc_b64 %0" : "=s"(code0));
    code0 &= ~255ull;
    if (wave == 0) {
        const unsigned cl = lane * 32 + r5;
        if (cl < CODE_LINES) GLT(cl * 128, code0);
    }
    const u32x4 ones = {0xffffffffu, 0xffffffffu, 0xffffffffu, 0xffffffffu};

#define WO_OFF(c) ({ const int rho_ = (c) / 10, ch_ = (c) - rho_ * 10; (unsigned)((rho_ ^ ((rho_ & 8) >> 1)) * (D_DIM * 2) + oc * 160 + ch_ * 16); })
    const int kr = KROT;
#define T_C(i) __builtin_amdgcn_readfirstlane((kr + (i)) >= 10 ? kr + (i) - 10 : kr + (i))
#define T_WV() ({ const int j_ = min(n16, 14); (unsigned)((r * 4 + g) * (NQKV * 2) + j_ * 128 + ((j_ >= 5) + (j_ >= 10) + (xcd & 1)) * 640); })
#define T_XV() ((unsigned)(((xcd >> 1) * 512 + r * 32 + (lane & 31)) * (D_DIM * 2)))
#define T_W(wv, i) GLT(wv, (const char*)wqkv + (size_t)T_C(i) * (64 * NQKV * 2))
#define T_X(xv, i) { const int c0_ = T_C(i), c1_ = T_C((i) + 1); GLT((xv) + (lane < 32 ? c0_ : c1_) * 128, x); }
#define T_PAIR(i) { T_W(wv, i); T_X(xv_, i); T_W(wv, (i) + 1); }

    unsigned e0;
    if (isG) {
        const unsigned t0_ = T_WV() + (kr + (w >> 1)) * (64 * NQKV * 2);
        const unsigned t1_ = ((xcd >> 1) * 512 + r * 32 + (lane & 31)) * (D_DIM * 2) + (kr + (lane >> 5)) * 128;
        e0 = (w & 1) ? t1_ : t0_;
    } else {
        const unsigned c = (wave >= 5 ? wave - 5 : wave) * 64 + lane, row = (c * 3277) >> 16;
        e0 = rbv * (64 * D_DIM * 2) + cq * 320 + row * (D_DIM * 2 - 320) + c * 16;
    }
    asm volatile("" : "+v"(e0));
    asm volatile("s_waitcnt lgkmcnt(0)" : "+s"(ka0), "+s"(ka1), "+s"(ka2) :: "memory");
    if (isG) {
        if (wave >= 4 && wave < 7) GLT(e0, (w & 1) ? x : wqkv);
        BARRIER();
    }
    u32x2 skw;
    if (!isG && wave < 10) {
        const int tens = wave >= 5, i5 = wave - 5 * tens;
        const unsigned lw = lb + (tens ? V_VST : V_KST) + i5 * 1024;
        const char* kb = (const char*)(tens ? vin : kin);
        __builtin_amdgcn_s_sleep(V_DELAY);
        GLS(e0, kb, lw, 0);
        GLS(e0, kb + 16 * D_DIM * 2, lw, 5152);
        GLS(e0, kb + 32 * D_DIM * 2, lw, 2 * 5152);
        GLS(e0, kb + 48 * D_DIM * 2, lw, 3 * 5152);
        asm volatile("global_load_dwordx2 %0, %1, %2" : "=v"(skw) : "v"((cq * 160 + wave * 16 + n16) * 8), "s"(shift_k) : "memory");
        if (wave == 0) { if (lane < 60) GLS(e0, kb - (rbv ? 3 * D_DIM * 2 : 0), lb, V_KST - 32 - 960); }
    }
    if (wave == 11) {
        if (lane < 16) *(u32x4*)(smem + L_CTRL + lane * 16) = (u32x4){0u, 0u, 0u, 0u};
        asm volatile("s_waitcnt lgkmcnt(0)" ::: "memory");
    }
    STAMP(1);
#define CF_OFF() ((unsigned)(lane < 16 ? WS_HF + ROWT(lane & 3) * 128 : lane < 32 ? WS_SF + (oc * 16 + (lane & 15)) * 128 : WS_HN + ((rbo >> 1) * 8 + (lane & 7)) * 128))
#define NOW() ((unsigned)__builtin_amdgcn_s_memrealtime())
#define TICKS_PER_CYCLE(now, c0, r0) ((float)((now) - (r0)) * __builtin_amdgcn_rcpf((float)((unsigned)__builtin_amdgcn_s_memtime() - (c0))))
#define CF_LOOK(v) { const bool k_ = v[0] == tlo && v[1] == thi; cfm |= __builtin_amdgcn_ballot_w64(k_); \
            if (k_) { tv = v[2] | 1; tu = v[3] | 1; } \
            if (!(~cfm & 0xffff) && lane >= 16 && lane < 40 && (lane < 32 ? !(~cfm & 0xffff0000u) : ((cfm >> (lane & ~1)) & 3) == 3)) \
                asm volatile("ds_max_u32 %0, %1\n\tds_max_u32 %0, %2 offset:16" ::"v"(lb + (lane < 32 ? C_TY : C_TM + (lane & 6) * 2)), "v"(tv), "v"(lane < 32 ? 0u : tu) : "memory"); }
#define CF_LOOP(off, need) _Pragma("nounroll") for (int it = 0; it < SPIN_CAP && (~cfm & (need)); ++it) { \
            if (it) __builtin_amdgcn_s_sleep(CF_SLEEP); \
            asm volatile("" ::: "memory"); \
            u32x4 v = {0u, 0u, 0u, 0u}; \
            if (!((cfm >> lane) & 1)) v = __builtin_amdgcn_raw_buffer_load_b128(wsr, off, 0, 16); \
            CF_LOOK(v); }
#define G_LD(n) __builtin_amdgcn_raw_buffer_load_b32(wsr, (n) * 4, WS_G + ROWT(w) * 64, 16)
#define G_CHK(v, n) { if (__builtin_expect(__any(v == 0xffffffffu), 0)) { _Pragma("nounroll") for (int it = 0; it < SPIN_CAP; ++it) { \
            asm volatile("" ::: "memory"); v = G_LD(n); if (!__any(v == 0xffffffffu)) break; __builtin_amdgcn_s_sleep(4); } } }
#define XCH_AT(rt) (lb + (isG ? ((rt) < 2 ? G_Q : G_XCH - 20480) : V_XCH) + (rt) * 10240)
#define Y_OFF (WS_Y + rbo * (D_DIM * 128) + oc * 10240 + w * 1024)
#define Y_LD(i) asm volatile("global_load_dwordx4 %0, %1, %2 sc1" : "=v"(yv[i]) : "v"(l16y), "s"(ws + Y_OFF + ((i) < 2 ? (i) * 4096 : w < 2 ? 8192 : 0)) : "memory")
#define Y_TIME(t) { _Pragma("nounroll") for (int it = 0; it < 20000 && (int)(NOW() - (t)) < 0; ++it) {} }
#define TY_GET() ({ unsigned t_ = 0; _Pragma("nounroll") for (int it = 0; it < SPIN_CAP; ++it) { t_ = __builtin_amdgcn_readfirstlane(lds_ld32(lb + C_TY)); if (t_) break; __builtin_amdgcn_s_sleep(3); } t_; })
#define G_FIRST() unsigned g1 = G_LD(lane & 15)
#define Y_FIRST() { Y_LD(0); Y_LD(1); Y_LD(2); }
#define G_MUL() { G_CHK(g1, lane & 15); _Pragma("unroll") for (int ct = 0; ct < 5; ++ct) gq[ct] *= __uint_as_float(g1); \
        asm volatile("" : "+v"(gq[0]), "+v"(gq[1]), "+v"(gq[2]), "+v"(gq[3]), "+v"(gq[4])); }
#define RELANE() const int lane_ = opaque(lane); const int g = lane_ >> 4, n16 = lane_ & 15, q4 = n16 >> 2, p4 = lane_ & 3
#define SENT4(v) __any(max(max((v)[0], (v)[1]), max((v)[2], (v)[3])) == 0xffffffffu)
#define Q_READ(base) f32x4 gq[5]; { const int l_ = opaque(lane); _Pragma("unroll") for (int ct = 0; ct < 5; ++ct) { \
            const u32x2 q2 = *(const lds_u32x2*)(size_t)(lb + (base) + w * 2560 + (l_ & 15) * 160 + 8 * (l_ >> 4) + ct * 32); \
            gq[ct] = (f32x4){lo_f(q2[0]), hi_f(q2[0]), lo_f(q2[1]), hi_f(q2[1])}; } \
        asm volatile("" : "+v"(gq[0]), "+v"(gq[1]), "+v"(gq[2]), "+v"(gq[3]), "+v"(gq[4]) :: "memory"); }
#define P_STAGE(YE) { \
        RELANE(); \
        FLUSH_LO(); \
        const unsigned l_y = lb + (isG ? G_Y : V_Y); \
        const unsigned yw = opaque(l_y + (w * 8 + (lane_ >> 3)) * Y_PITCH + (lane_ & 7) * 16), yr = opaque(l_y + (4 * g + q4) * Y_PITCH + w * 32 + p4 * 8), xa = opaque(XCH_AT(w) + lane_ * 16); \
        u16* ob = out + (size_t)(ROWT(w) * 16 + n16) * D_DIM + oc * 80; \
        const float g0 = 1.f - __uint_as_float(g1); \
        asm volatile("" : "+v"(ob)); \
_Pragma("nounroll") \
        for (int it = 0; it < SPIN_CAP; ++it) { \
            asm volatile("s_cmp_eq_u32 %3, 0\n\ts_cbranch_scc0 1f\n\ts_waitcnt vmcnt(0)\n1:\n\ts_waitcnt vmcnt(3)" : "+v"(yv[0]), "+v"(yv[1]), "+v"(yv[2]) : "s"(__builtin_amdgcn_readfirstlane(it == 0 ? (unsigned)(YE) : 0u)) : "memory", "scc"); \
            const bool b0 = SENT4(yv[0]), b1 = SENT4(yv[1]), b2 = SENT4(yv[2]); \
            if (!(b0 | b1 | b2)) break; \
            if (it || Y_SL0) __builtin_amdgcn_s_sleep(Y_SLEEP); \
            if (b0) Y_LD(0); \
            if (b1) Y_LD(1); \
            if (b2) Y_LD(2); \
        } \
        STAMP(8); \
        *(lds_u32x4*)(size_t)yw = yv[0]; \
        *(lds_u32x4*)(size_t)(yw + 32 * Y_PITCH) = yv[1]; \
        if (w < 2) *(lds_u32x4*)(size_t)(yw + 64 * Y_PITCH) = yv[2]; \
        lds_add(lb + C_Y, lane_); \
        spin_ge(lb + C_Y, 4); \
        u32x2 y2[5]; \
        const unsigned xc = lds_v32(lb + C_XCH + w * 4); \
_Pragma("unroll") \
        for (int ct = 0; ct < 5; ++ct) y2[ct] = __builtin_bit_cast(u32x2, TRRD((lds_char*)(size_t)(yr + ct * 16 * Y_PITCH))); \
        SB(); \
        f32x4 pa[5], pb[5]; \
        const bool early = __builtin_amdgcn_readfirstlane(xc) >= 2; \
        if (early) { _Pragma("unroll") for (int ct = 0; ct < 5; ++ct) { pa[ct] = *(const lds_f32x4*)(size_t)(xa + ct * 1024); pb[ct] = *(const lds_f32x4*)(size_t)(xa + 5120 + ct * 1024); } SB(); } \
_Pragma("unroll") \
        for (int ct = 0; ct < 5; ++ct) gq[ct] *= (f32x4){lo_f(y2[ct][0]), hi_f(y2[ct][0]), lo_f(y2[ct][1]), hi_f(y2[ct][1])}; \
        asm volatile("" : "+v"(gq[0]), "+v"(gq[1]), "+v"(gq[2]), "+v"(gq[3]), "+v"(gq[4])); \
        STAMP(9); \
        if (!early) { \
            spin_ge(lb + C_XCH + w * 4, 2); \
            _Pragma("unroll") for (int ct = 0; ct < 5; ++ct) { pa[ct] = *(const lds_f32x4*)(size_t)(xa + ct * 1024); pb[ct] = *(const lds_f32x4*)(size_t)(xa + 5120 + ct * 1024); } \
        } \
        STAMP(13); \
        unsigned pk[5][2]; \
_Pragma("unroll") \
        for (int ct = 0; ct < 5; ++ct) { \
            const f32x4 fin = gq[ct] + g0 * (pa[ct] + pb[ct]); \
            pk[ct][0] = pack2(fin[0], fin[1]); \
            pk[ct][1] = pack2(fin[2], fin[3]); \
        } \
_Pragma("unroll") \
        for (int p = 0; p < 2; ++p) { \
            const auto r0 = __builtin_amdgcn_permlane16_swap(pk[2 * p][0], pk[2 * p + 1][0], false, false); \
            const auto r1 = __builtin_amdgcn_permlane16_swap(pk[2 * p][1], pk[2 * p + 1][1], false, false); \
            asm volatile("global_store_dwordx4 %0, %1, off " OUT_POL "\n\ts_nop 1" :: "v"(ob + (2 * p + (g & 1)) * 16 + (g >> 1) * 8), "v"((u32x4){r0[0], r1[0], r0[1], r1[1]}) : "memory"); \
        } \
        asm volatile("global_store_dwordx2 %0, %1, off " OUT_POL :: "v"(ob + 64 + 4 * g), "v"((u32x2){pk[4][0], pk[4][1]}) : "memory"); \
        FLUSH(); \
        return; }

    const int ph = isG ? wave >> 2 : 1 - (wave >> 2), kh = w >> 1, trt = (1 - ph) * 2 + (w & 1);
    unsigned ofo, pc, pd, xa;
    bool wo_open = isG && wave < 4;
#define TAIL_ADDR() { RELANE(); const int hsw = (g & 1) * 4; \
        ofo = opaque(WS_O + (ROWT(trt) * 20 + kh * 10) * 1024 + 2048 + lane_ * 16); \
        pc = opaque(lb + (8 * g + hsw + q4) * 160 + p4 * 8 + (isG ? (kh ? G_WO1 : G_WO0) : V_WO + kh * 51200)); \
        pd = opaque(pc + (4 - 2 * hsw) * 160); \
        xa = opaque(XCH_AT(trt) + kh * 5120 + lane_ * 16); }

    if (__builtin_expect(isG, 1)) {
      if (__builtin_expect(wave >= 4, 0)) {
        const int half = (wave >> 2) - 1;
        if (half) __builtin_amdgcn_s_setprio(C1_PRIO); else __builtin_amdgcn_s_setprio(C0_PRIO);
        const int hf = 1 - half, rb = rb2 * 2 + hf;
        f32x4 acc[4][5];
        unsigned aofs[2];
#pragma unroll
        for (int sub = 0; sub < 2; ++sub) aofs[sub] = lb + L_A + hf * 8192 + n16 * 128 + (((sub * 4 + g) ^ ((n16 >> 1) & 7)) * 16);
        unsigned wofs[4];
#pragma unroll
        for (int i = 0; i < 4; ++i) {
            const int part = i < 3 ? i : (p4 < 3 ? p4 : 2);
            const int Q = i < 3 ? 5 * w + (w & 1) + p4 : 5 * w + ((w & 1) ? 0 : 4);
            const int c2 = (Q >> 1) - 2 * (hg & 1);
            const int cc = ((unsigned)c2 < 8u ? part * 8 + c2 : 24 + part * 2 + (c2 & 1)) ^ (2 * ((g & 1) * 4 + q4));
            wofs[i] = lb + L_W + (8 * g + q4) * W_PITCH + cc * 16 + (Q & 1) * 8;
        }
        bf16x8 af[5], bA[4], bB[4];
        lds_char* wp[2][4];
        lds_char* ap[2];
#pragma unroll
        for (int i = 0; i < 4; ++i) { wp[0][i] = (lds_char*)(size_t)opaque(wofs[i]); wp[1][i] = (lds_char*)(size_t)opaque(wofs[i] + 3 * W_SLOT); }
        ap[0] = (lds_char*)(size_t)opaque(aofs[0]);
        ap[1] = (lds_char*)(size_t)opaque(aofs[1]);
        const unsigned ctl = opaque(lb + C_FREE), ctf = opaque(lb + C_FREE + (wave - 4) * 4);
#define WSL(J, i) (wp[((J) % NW) / 3][i] + (((J) % NW) % 3) * W_SLOT)
#define MFMA16A(c, a, b) asm volatile("v_mfma_f32_16x16x32_bf16 %0, %1, %2, %0" : "+v"(c) : "v"(a), "v"(b))
#define MFMA16Z(c, a, b) asm volatile("v_mfma_f32_16x16x32_bf16 %0, %1, %2, 0" : "=&v"(c) : "v"(a), "v"(b))
#define MFMA16(c, a, b) do { if (first_) MFMA16Z(c, a, b); else MFMA16A(c, a, b); } while (0)
#define BORD(rt) (((rt) + 1 - R0_) & 3)
#define FR0 (JL < 16 ? JL + 1 : 16)
#define STEP(J, bc, bn, R0) { \
            const bool first_ = (J) == 0; \
            constexpr int R0_ = R0; \
            unsigned fl_ = 4, fs_ = 4; \
            _Pragma("unroll") for (int rt = R0; rt < 5; ++rt) { \
                s16x4 b0_, b1_; \
                if ((J) > JL && (J) <= 17 && rt == 2) { fl_ = lds_v32(ctl + (C_FULL - C_FREE) + ((J) - 13) * 4); SB(); } \
                if ((J) >= FR0 && (J) < 19 && rt == 4) { if ((J) > JL && (J) <= 17) fs_ = __builtin_amdgcn_readfirstlane(fl_); asm volatile("v_mov_b32 %0, %1" : "=v"(fl_) : "n"(J)); SB(); } \
                if (rt > 0) MFMA16(acc[0][rt], bc[0], af[rt]); SB(); \
                if ((J) < 19 && rt < 4 + R0) { b0_ = TRRD(WSL((J) + 1, BORD(rt - R0))); SB(); } \
                MFMA16(acc[1][rt], bc[1], af[rt]); SB(); \
                if ((J) < 19 && rt < 4 + R0) { b1_ = TRRD(WSL((J) + 1, BORD(rt - R0)) + 4 * W_PITCH); bn[BORD(rt - R0)] = cat8(b0_, b1_); SB(); } \
                MFMA16(acc[2][rt], af[rt], bc[2]); SB(); \
                bf16x8 an_; \
                if (TAILM && rt == 4 && (J) < 19) { an_ = *(lds_cbf16x8*)(ap[((J) + 1) & 1] + ((((J) + 1) >> 1) % 3) * A_SLOT + rt * 2048); SB(); \
                    if ((J) >= FR0) { lds_st32(ctf, fl_); SB(); } } \
                MFMA16(acc[3][rt], bc[3], af[rt]); SB(); \
                if (TAILM && rt == 4) { if ((J) < 19) af[rt] = an_; } \
                else if ((J) < 19) { af[rt] = *(lds_cbf16x8*)(ap[((J) + 1) & 1] + ((((J) + 1) >> 1) % 3) * A_SLOT + rt * 2048); SB(); } \
            } \
            if (!TAILM && (J) >= FR0 && (J) < 19) lds_st32(ctf, fl_);        \
            if ((J) == JL && JL < 16) asm volatile("s_waitcnt lgkmcnt(0)" ::: "memory"); \
            if ((J) <= JL) BARRIER(); \
            if ((J) > JL && (J) <= 17) { if (__builtin_expect(fs_ < 4, 0)) spin_ge(lb + C_FULL + ((J) - 13) * 4, 4); } \
            SB(); }
#define GEMM(R0) { \
        _Pragma("unroll") for (int i = 0; i < 4; ++i) bA[i] = cat8(TRRD(wp[0][i]), TRRD(wp[0][i] + 4 * W_PITCH)); \
        _Pragma("unroll") for (int rt = R0; rt < 5; ++rt) af[rt] = *(lds_cbf16x8*)(ap[0] + rt * 2048); \
        SB(); \
        GT(0) STEP(0, bA, bB, R0) GT(1) STEP(1, bB, bA, R0) STAMP(5); GT(2) STEP(2, bA, bB, R0) GT(3) STEP(3, bB, bA, R0) GT(4) STEP(4, bA, bB, R0) GT(5) STEP(5, bB, bA, R0) \
        STAMP(6); GT(6) STEP(6, bA, bB, R0) GT(7) STEP(7, bB, bA, R0) GT(8) STEP(8, bA, bB, R0) GT(9) STEP(9, bB, bA, R0) STAMP(14); GT(10) STEP(10, bA, bB, R0) GT(11) STEP(11, bB, bA, R0) \
        GT(12) STEP(12, bA, bB, R0) GT(13) STEP(13, bB, bA, R0) GT(14) STEP(14, bA, bB, R0) GT(15) STEP(15, bB, bA, R0) GT(16) STEP(16, bA, bB, R0) GT(17) STEP(17, bB, bA, R0) \
        TS(0); sent0 = lds_v32(lb + C_OWN); SB(); GT(18) STEP(18, bA, bB, R0) GT(19) }
#define GT(j)
        f32x4 cm0, cm1;
        u32x4 psel, vxa;
        unsigned scw, oo, ox;
#define ATT_CONST() { \
        const int l_ = opaque(lane), g = l_ >> 4, qi = l_ & 15, q4 = qi >> 2, p4 = l_ & 3, par = l_ & 1; \
        _Pragma("unroll") for (int e = 0; e < 4; ++e) { \
            const bool own = 4 * g + e <= qi, pm = (e & 1) == par; \
            cm0[e] = pm && !own ? 0.f : -3.0e38f; \
            cm1[e] = pm && own ? 0.f : -3.0e38f; \
            const bool ow = 4 * g + par + 2 * (e & 1) <= qi;         \
            const unsigned by = (e & 1) ? 0x0302u : 0x0100u; \
            psel[e] = (ow == (e >= 2)) ? (par ? (by << 16) | 0x0c0cu : 0x0c0c0000u | by) : 0x0c0c0c0cu; \
            vxa[e] = p4 == e ? lb + G_VX + w * 1152 + (hf * 4 + e) * 128 + (4 * g + q4) * 8 : lb + L_CTRL; \
        } \
        const int cc = (hg * 80 + w * 20 + (w & 1) * 4) / 8 + (g >> 1), colx = hg * 80 + w * 20 + ((w & 1) ? 0 : 16); \
        oo = WS_O + ((rb * 4 + (g & 1)) * 20 + (cc >> 2)) * 1024 + ((cc & 3) * 16 + qi) * 16; \
        ox = (g * 20 + (colx >> 5)) * 1024 + (((colx >> 3) & 3) * 16 + qi) * 16 + (colx & 4) * 2; \
        asm volatile("" : "+v"(cm0), "+v"(cm1), "+v"(psel), "+v"(vxa), "+v"(oo), "+v"(ox)); SB(); }
        scw = g == 2 ? lb + G_VX + w * 1152 + hf * 512 + n16 * 8 : lb + G_VXD;
        asm volatile("" : "+v"(scw));
        ATT_CONST();
        unsigned sent0;
        unsigned hxo = opaque(WS_HX + ((rb2 + hf) * 8 + hg) * 5120 + w * 1280 + lane * 16), hxp = opaque(hxo + 1024 - lane * 12);
        unsigned hxw = opaque(g >= 2 ? lb + G_VX + w * 1152 + n16 * 8 + (g & 1) * 4 : lb + G_VXD);
        BARRIER();
        STAMP(7);
        CSTAMP(0);
        GEMM(1)
        typedef __attribute__((address_space(3))) volatile unsigned lds_vu32;
#define HINT(at)
        u32x4 kop[5], qop[5];
        u32x16 vv;
        const unsigned hx = lb + G_HX + w * 1152;
        u32x4 ha0 = {0u, 0u, 0u, 0u};
        unsigned hb0 = 0;
        const unsigned far = half * rb2;
#define HX_LD() asm volatile("s_cmp_eq_u32 %5, 0\n\ts_cbranch_scc1 1f\n\tglobal_load_dwordx4 %0, %2, %4 sc1\n\tglobal_load_dword %1, %3, %4 sc1\n1:" \
            : "+v"(ha0), "+v"(hb0) : "v"(hxo), "v"(hxp), "s"(ws), "s"(far) : "memory", "scc")
        if (__builtin_expect(!__builtin_amdgcn_readfirstlane(sent0), 0)) spin_ge(lb + C_OWN, 1);
        if (half) __builtin_amdgcn_s_setprio(L19_PRIO);
        unsigned d0, d1, sq[5];
#define ML(i, rt) { if (rt >= 1) { acc[i][rt] = i == 2 ? __builtin_amdgcn_mfma_f32_16x16x32_bf16(af[rt], bB[2], acc[i][rt], 0, 0, 0) : __builtin_amdgcn_mfma_f32_16x16x32_bf16(bB[i], af[rt], acc[i][rt], 0, 0, 0); } SB(); }
#define PIN2(a, b) asm volatile("" : "+v"(a), "+v"(b)); SB()
#define PK_K(rt) { kop[rt][0] = pack2(acc[1][rt][0], acc[1][rt][1]); kop[rt][1] = pack2(acc[1][rt][2], acc[1][rt][3]); PIN2(kop[rt][0], kop[rt][1]); }
#define PK_V(rt) { vv[2 * rt] = pack2(acc[2][rt][0], acc[2][rt][1]); vv[2 * rt + 1] = pack2(acc[2][rt][2], acc[2][rt][3]); PIN2(vv[2 * rt], vv[2 * rt + 1]); }
#define PK_O(rt) { d0 = pack2(acc[3][rt][0], acc[3][rt][1]); d1 = pack2(acc[3][rt][2], acc[3][rt][3]); \
            asm volatile("ds_write2_b32 %0, %1, %2 offset0:%3 offset1:%4" ::"v"(scw), "v"(d0), "v"(d1), "n"(rt * 32), "n"(rt * 32 + 1) : "memory"); SB(); }
#define PK_S(rt) { const auto sw2 = __builtin_amdgcn_permlane16_swap(d0, d1, false, false); sq[rt] = sw2[0]; kop[rt][2] = sw2[1]; PIN2(sq[rt], kop[rt][2]); }
#define PK_Q(rt) { qop[rt][0] = pack2(acc[0][rt][0], acc[0][rt][1]); qop[rt][1] = pack2(acc[0][rt][2], acc[0][rt][3]); PIN2(qop[rt][0], qop[rt][1]); }
        ML(1, 4) ML(2, 4) ML(3, 4) ML(1, 3)
        PK_K(4) ML(2, 3) PK_V(4) ML(3, 3) PK_O(4) ML(0, 4) PK_S(4)
        if (half) {
            asm volatile("ds_write2_b64 %0, %1, %2 offset1:1" ::"v"(hx + lane * 16), "v"((u32x2){kop[4][0], kop[4][1]}), "v"((u32x2){vv[8], vv[9]}) : "memory");
            asm volatile("ds_write_b32 %0, %1" ::"v"(g < 2 ? hx + 1024 + lane * 4 : lb + L_TOUCH), "v"(kop[4][2]) : "memory");
            lds_st32(lb + C_HX + w * 4, 1);
        } else {
            if (rb2 < 15) {
                __builtin_amdgcn_raw_buffer_store_b128((u32x4){min(kop[4][0], 0xfffffffeu), min(kop[4][1], 0xfffffffeu), min(vv[8], 0xfffffffeu), min(vv[9], 0xfffffffeu)}, wsr, hxo, 0, 16);
                __builtin_amdgcn_raw_buffer_store_b32(min(g < 2 ? kop[4][2] : sq[4], 0xfffffffeu), wsr, hxp, 0, 16);
            }
            lds_add(lb + C_HS, lane);
        }
        SB();
        if (HX_AT == 4) { HX_LD(); SB(); }
        ML(0, 3) ML(1, 2) PK_K(3) ML(2, 2) PK_V(3) ML(3, 2) PK_O(3) ML(0, 2) PK_S(3)
        ML(1, 1) PK_Q(4) ML(2, 1) PK_Q(3) ML(3, 1) PK_K(2) ML(0, 1) PK_V(2)
        GT(20)
        TS(1);
        STAMP(2);
        CSTAMP(1);
        __builtin_amdgcn_s_setprio(T_PRIO);
        if (!half) __builtin_amdgcn_s_setprio(E_PRIO);
        if (HX_AT == 3) { HX_LD(); SB(); }
        PK_O(2) PK_Q(2) PK_S(2) PK_K(1) PK_V(1) PK_O(1) PK_Q(1) PK_S(1)
        if (HX_AT == 2) { HX_LD(); SB(); }
#pragma unroll
        for (int rt = 4; rt >= 1; --rt) {
            kop[rt][3] = 0u;
            qop[rt][2] = g < 2 ? sq[rt] : 0u;
            qop[rt][3] = 0u;
        }
        TS(2);
        bf16x8 vx[5];
#pragma unroll
        for (int rt = 4; rt >= 2; --rt) vx[rt] = cat8(TRRD((lds_char*)(size_t)vxa[rt - 1]), TRRD((lds_char*)(size_t)(vxa[rt - 1] + 128)));
        TS(3);
        STAMP(3);
        const float scale = 0.22360679774997896f * 1.4426950408889634f;
        const f32x4 z4 = {0.f, 0.f, 0.f, 0.f};
        float l0[5], l1[5], mx[5];
        u32x4 hl4;
        unsigned hl1, hx0;
#pragma unroll
        for (int rt = 4; rt >= 1; --rt) {
            if (rt == HL_AT) {
                SB();
                hx0 = lds_v32(lb + C_HX + w * 4);
                hl4 = lds_v128(hx + lane * 16);
                hl1 = lds_v32(hx + 1024 + (lane & 31) * 4);
                SB();
            }
            if (rt == 1) {
                SB();
                asm volatile("s_cmp_eq_u32 %2, 0\n\ts_cbranch_scc1 1f\n\ts_waitcnt vmcnt(0)\n1:" : "+v"(ha0), "+v"(hb0) : "s"(far) : "memory", "scc");
                if (half) {
                    if (__builtin_expect(__any(max(max(max(ha0[0], ha0[1]), max(ha0[2], ha0[3])), hb0) == 0xffffffffu), 0)) {
#pragma nounroll
                        for (int it = 0; it < SPIN_CAP; ++it) {
                            NHRETRY();
                            __builtin_amdgcn_s_sleep(HX_SLEEP);
                            HX_LD();
                            asm volatile("s_waitcnt vmcnt(0)" : "+v"(ha0), "+v"(hb0) :: "memory");
                            if (!__any(max(max(max(ha0[0], ha0[1]), max(ha0[2], ha0[3])), hb0) == 0xffffffffu)) break;
                        }
                    }
                    asm volatile("ds_write_b32 %0, %1" ::"v"(hxw), "v"(hb0) : "memory");
                } else {
                    if (__builtin_expect(!__builtin_amdgcn_readfirstlane(hx0), 0)) {
                        spin_ge(lb + C_HX + w * 4, 1);
                        hl4 = lds_v128(hx + lane * 16);
                        hl1 = lds_v32(hx + 1024 + (lane & 31) * 4);
                    }
                    ha0 = hl4;
                    hb0 = hl1;
                }
                kop[0] = (u32x4){ha0[0], ha0[1], hb0, 0u};
                vv[0] = ha0[2];
                vv[1] = ha0[3];
                vx[1] = cat8(TRRD((lds_char*)(size_t)vxa[0]), TRRD((lds_char*)(size_t)(vxa[0] + 128)));
                SB();
            }
            const f32x4 s0 = __builtin_amdgcn_mfma_f32_16x16x32_bf16(__builtin_bit_cast(bf16x8, kop[rt - 1]), __builtin_bit_cast(bf16x8, qop[rt]), cm0, 0, 0, 0);
            const f32x4 s1 = __builtin_amdgcn_mfma_f32_16x16x32_bf16(__builtin_bit_cast(bf16x8, kop[rt]), __builtin_bit_cast(bf16x8, qop[rt]), cm1, 0, 0, 0);
            if (rt == 1 && rb == 0) {
                l0[rt] = fmax_raw(s1[0], s1[1]);
                l1[rt] = fmax_raw(s1[2], s1[3]);
            } else {
                l0[rt] = fmax_raw(fmax3_raw(s0[0], s0[1], s1[0]), s1[1]);
                l1[rt] = fmax_raw(fmax3_raw(s0[2], s0[3], s1[2]), s1[3]);
            }
            mx[rt] = fmax_raw(l0[rt], l1[rt]);
        }
#define FU(v) __float_as_uint(v)
#define UF(v) __uint_as_float(v)
#define RED4(dst, src, OP) { \
            const auto ra_ = __builtin_amdgcn_permlane16_swap(FU(src[1]), FU(src[2]), false, false); \
            const auto rb_ = __builtin_amdgcn_permlane16_swap(FU(src[3]), FU(src[4]), false, false); \
            const auto rc_ = __builtin_amdgcn_permlane32_swap(FU(OP(UF(ra_[0]), UF(ra_[1]))), FU(OP(UF(rb_[0]), UF(rb_[1]))), false, false); \
            dst = OP(UF(rc_[0]), UF(rc_[1])); }
#define BC4(dst, src) { \
            const auto ra_ = __builtin_amdgcn_permlane32_swap(FU(src), FU(src), false, false); \
            const auto rb_ = __builtin_amdgcn_permlane16_swap(ra_[0], ra_[0], false, false); \
            const auto rc_ = __builtin_amdgcn_permlane16_swap(ra_[1], ra_[1], false, false); \
            dst[1] = UF(rb_[0]); dst[2] = UF(rb_[1]); dst[3] = UF(rc_[0]); dst[4] = UF(rc_[1]); }
#define FADD(a, b) ((a) + (b))
        SB(); TS(4); HINT(1); SB();
        float red;
        RED4(red, mx, fmax_raw);
        red *= scale;
        BC4(mx, red);
        f32x2 pp[5];
        float sm[5];
#pragma unroll
        for (int rt = 4; rt >= 1; --rt) {
            pp[rt] = (f32x2){__builtin_amdgcn_exp2f(__builtin_fmaf(l0[rt], scale, -mx[rt])), __builtin_amdgcn_exp2f(__builtin_fmaf(l1[rt], scale, -mx[rt]))};
            sm[rt] = pp[rt][0] + pp[rt][1];
        }
        RED4(red, sm, FADD);
        red = __builtin_amdgcn_rcpf(red);
        BC4(sm, red);
        SB(); TS(5);
        const unsigned ty0 = *(lds_vu32*)(size_t)(lb + C_TY), ow2 = *(lds_vu32*)(size_t)(lb + C_OW2);
        SB();
        f32x4 o1[5], o2 = z4;
#pragma unroll
        for (int rt = 4; rt >= 1; --rt) {
            const unsigned pk = pack2(pp[rt][0] * sm[rt], pp[rt][1] * sm[rt]);
            const u32x4 pop = {__builtin_amdgcn_perm(pk, pk, psel[0]), __builtin_amdgcn_perm(pk, pk, psel[1]), __builtin_amdgcn_perm(pk, pk, psel[2]), __builtin_amdgcn_perm(pk, pk, psel[3])};
            const u32x4 vop = {vv[2 * rt - 2], vv[2 * rt - 1], vv[2 * rt], vv[2 * rt + 1]};
            o1[rt] = __builtin_amdgcn_mfma_f32_16x16x32_bf16(__builtin_bit_cast(bf16x8, vop), __builtin_bit_cast(bf16x8, pop), z4, 0, 0, 0);
            o2 = __builtin_amdgcn_mfma_f32_16x16x32_bf16(vx[rt], __builtin_bit_cast(bf16x8, pop), o2, 0, 0, 0);
        }
        SB(); TS(6);
        u32x4 yv[3];
        unsigned g1;
        const unsigned l16y = lane * 16;
#define G_ASK() asm volatile("global_load_dword %0, %1, %2 sc1" : "=v"(g1) : "v"((lane & 15) * 4), "s"(ws + WS_G + ROWT(w) * 64) : "memory")
        if (__builtin_expect(!__builtin_amdgcn_readfirstlane(ow2), 0)) spin_ge(lb + C_OW2, 1);
        unsigned ty = half ? __builtin_amdgcn_readfirstlane(ty0) : 0;
        const unsigned yearly = __builtin_amdgcn_readfirstlane(YEARLY && ty && (int)(NOW() - ty) >= 0);
        if (yearly) { G_ASK(); Y_FIRST(); }
        SB();
#pragma unroll
        for (int rt = 3; rt >= 1; rt -= 2) {
            const auto r0 = __builtin_amdgcn_permlane16_swap(min(pack2(o1[rt][0], o1[rt][1]), 0xfffffffeu), min(pack2(o1[rt + 1][0], o1[rt + 1][1]), 0xfffffffeu), false, false);
            const auto r1 = __builtin_amdgcn_permlane16_swap(min(pack2(o1[rt][2], o1[rt][3]), 0xfffffffeu), min(pack2(o1[rt + 1][2], o1[rt + 1][3]), 0xfffffffeu), false, false);
            __builtin_amdgcn_raw_buffer_store_b128((u32x4){r0[0], r1[0], r0[1], r1[1]}, wsr, oo + (rt - 1) * 20480, 0, 16);
        }
        __builtin_amdgcn_raw_buffer_store_b64((u32x2){min(pack2(o2[0], o2[1]), 0xfffffffeu), min(pack2(o2[2], o2[3]), 0xfffffffeu)}, wsr, ox, WS_O + rb * 4 * 20480, 16);
        HINT(4);
        TS(7);
        TSFLUSH();
        if (!half || yearly) lds_add(lb + C_OS1, lane);
        if (half) {
            STAMP(4);
            if (!yearly) {
                const bool soon = ty && (int)(NOW() + Y_SOON - ty) >= 0;
                if (!soon) {
                    lds_add(lb + C_OS1, lane);
                    if (w == 0) {
                        spin_mid(lb + C_LK, 1);
                        if (!__builtin_amdgcn_readfirstlane(lds_ld32(lb + C_TY))) {
                            u64 cfm = ~0xffff0000ull;
                            unsigned tv = 0, tu = 0;
                            const unsigned cfo = CF_OFF();
                            CF_LOOP(cfo, ~0ull);
                        }
                    }
                    ty = TY_GET();
                }
                G_ASK();
                Y_TIME(ty);
                Y_FIRST();
                if (soon) lds_add(lb + C_OS1, lane);
            }
            spin_all(lb + C_FREE + (lane & 7) * 4, 18);
            spin_ge(lb + C_WO, 4);
            Q_READ(G_Q);
            spin_ge(lb + C_OST, 4);
            lds_add(lb + C_QR, lane);
            asm volatile("s_cmp_eq_u32 %1, 0\n\ts_cbranch_scc0 1f\n\ts_waitcnt vmcnt(3)\n1:\n\ts_waitcnt vmcnt(6)" : "+v"(g1) : "s"(yearly) : "memory", "scc");
            G_MUL();
            P_STAGE(yearly);
        }
        lds_add(lb + C_OST, lane);
        STAMP(4);
        TAIL_ADDR();
      } else {
        __builtin_amdgcn_s_setprio(L_PRIO);
        unsigned ev[9];
        {
            const int hi = lane >> 5, pos = lane & 31;
#pragma unroll
            for (int v = 0; v < 2; ++v) {
                const int row = 2 * w + hi + 8 * v;
                int L = pos ^ (2 * (v * 4 + (row & 3)));
                L = L > 29 ? L - 2 : L;
                const int part = L < 24 ? L >> 3 : (L - 24) >> 1;
                const int chunk = L < 24 ? (L & 7) + 2 * (hg & 1) : (L & 1) + ((hg & 1) ? 0 : 8);
                ev[v] = row * (NQKV * 2) + chunk * 16 + part * 1280 + hg * 160;
                ev[v + 2] = ev[v] + 16 * NQKV * 2;
            }
            const int ch = (lane & 7) ^ ((4 * (w & 1) + g) & 7);
#pragma unroll
            for (int j = 0; j < 4; ++j) ev[4 + j] = (rb2 * 128 + 8 * w + (lane >> 3) + 32 * j) * (D_DIM * 2) + ch * 16;
            ev[8] = 0;
        }
        ring_issue<4, 0, ring_cum(-1, 4)>(ev, x, wqkv, kr, lb + w * 1024);
        SB();
        waitvm<ring_cum(-1, 4) - ring_need(-1, 4)>();
        BARRIER();
        ring_issue<4, ring_cum(-1, 4), ring_cum(0, 4)>(ev, x, wqkv, kr, lb + w * 1024);
        SB();
        unsigned wf[2][3], qf[3];
#pragma unroll
        for (int v = 0; v < 2; ++v)
#pragma unroll
            for (int j = 0; j < 3; ++j) wf[v][j] = opaque(WO_OFF((((w + 2 * v) & 3) + 4 * j) * 64 + lane));
#pragma unroll
        for (int j = 0; j < 3; ++j) { const int c = min(j * 64 + lane, 159); const int row = c / 10, ch = c - row * 10; qf[j] = opaque(row * (D_DIM * 2) + ch * 16); }
        const unsigned sfo = opaque(CF_OFF());
        TAIL_ADDR();
        const unsigned fra = opaque(lb + C_FREE + (lane & 7) * 4);
        SB();
        unsigned c0, r0;
        ring_run17<4, 0>(ev, x, wqkv, kr, lb + w * 1024, c0, r0);
        STAMP(7);
#define WO_PART(chunk, v) { const char* wb_ = (const char*)wo + (size_t)(chunk) * (64 * D_DIM * 2); const unsigned sl_ = lb + ((w + 2 * (v)) & 3) * 1024; \
            constexpr int pos_ = (chunk) < 5 ? G_WO0 + (chunk) * 10240 : G_WO1 + ((chunk) - 5) * 10240; \
            GLS(wf[v][0], wb_, sl_, pos_); GLS(wf[v][1], wb_, sl_, pos_ + 4096); if (((w + 2 * (v)) & 3) < 2) GLS(wf[v][2], wb_, sl_, pos_ + 8192); }
#define FREED(J) { spin_all(fra, J); }
#define Q_PART() { _Pragma("unroll") for (int j = 0; j < 3; ++j) \
            if (j < 2 || lane < 32) GLS(qf[j], (const char*)x + (size_t)__builtin_amdgcn_readfirstlane(ROWT(w) * 16 * (D_DIM * 2) + oc * 160), lb + w * 2560, G_Q + j * 1024); }
        if (w == 2) {
            const unsigned t_ = NOW();
            const float f_ = TICKS_PER_CYCLE(t_, c0, r0);
            if (lane == 0) __builtin_amdgcn_raw_buffer_store_b128((u32x4){tlo, thi, t_ + (unsigned)(f_ * D0), t_ + (unsigned)(f_ * D1)}, wsr, WS_HN + (rb2 * 8 + hg) * 128, 0, 16);
            lds_st32(lb + C_TR + 4, t_ + (unsigned)(f_ * D0) + FB_T);
            lds_st32(lb + C_TR + 8, t_ + (unsigned)(f_ * D1) + FB_T);
        }
        u32x2 ov;
        const unsigned owo = WS_HG + (min(rb2 + (lane & 1), 15) * 8 + hg) * 128, ow8 = WS_HF + (rb2 * 8 + (lane & 7)) * 128;
        if (w == 1) asm volatile("s_mov_b64 exec, 3\n\tglobal_load_dwordx2 %0, %1, %2 sc1\n\ts_mov_b64 exec, -1" : "=v"(ov) : "v"(owo), "s"(ws) : "memory");
        WO_PART(0, 0);
        WO_PART(5, 1);
        WAIT_VM(9);
        BARRIER();
        WO_PART(1, 0);
        WO_PART(2, 1);
        asm volatile("s_waitcnt vmcnt(10)" : "+v"(ov) :: "memory");
        const bool own_ok = w != 1 || !(__builtin_amdgcn_ballot_w64(ov[0] != tlo || ov[1] != thi) & 3);
        if (w == 1 && own_ok) lds_st32(lb + C_OWN, 1);
        BARRIER();
        if (!own_ok) {
            poll_flags(wsr, owo, tlo, thi);
            lds_st32(lb + C_OWN, 1);
        }
#define OW2_LD() if (w == 0) asm volatile("s_mov_b64 exec, 0xff\n\tglobal_load_dwordx2 %0, %1, %2 sc1\n\ts_mov_b64 exec, -1" : "=v"(ov) : "v"(ow8), "s"(ws) : "memory")
#define OW2_USE() if (w == 0) { asm volatile("s_waitcnt vmcnt(5)" : "+v"(ov) :: "memory"); \
            if (__builtin_amdgcn_ballot_w64(ov[0] != tlo || ov[1] != thi) & 0xff) poll_flags(wsr, ow8, tlo, thi); \
            lds_st32(lb + C_OW2, 1); }
        if (OW2_AT == 0) OW2_LD();
        __builtin_amdgcn_s_setprio(WO_PRIO);
        STAMP(2);
        if (HS_POS == 0) spin_mid(lb + C_HS, HS_N);
        WO_PART(3, 0);
        WO_PART(4, 1);
        if (OW2_AT == 0) OW2_USE();
        if (HS_POS == 1) spin_mid(lb + C_HS, HS_N);
        if (OW2_AT == 1) OW2_LD();
        u32x4 fv;
        u64 cfm = ~0ull << 40;
        unsigned tv = 0, tu = 0;
#define LOOK_LD() if (w == 3) asm volatile("s_mov_b32 exec_hi, 0xff\n\tglobal_load_dwordx4 %0, %1, %2 sc1\n\ts_mov_b32 exec_hi, -1" : "=v"(fv) : "v"(sfo), "s"(ws) : "memory")
        if (LOOK_AT == 0) LOOK_LD();
        WO_PART(6, 0);
        WO_PART(7, 1);
        if (OW2_AT == 1) OW2_USE();
        if (LOOK_AT == 1) LOOK_LD();
        spin_all(fra, 17);
        WO_PART(8, 0);
        WO_PART(9, 1);
        if (LOOK_AT == 2) LOOK_LD();
        Q_PART();
        STAMP(3);
        if (w == 3) {
            tv = lds_ld32(lb + C_TR + 4) | 1;
            tu = lds_ld32(lb + C_TR + 8) | 1;
            cfm |= 0xffull << 32;
            asm volatile("s_waitcnt vmcnt(%1)" : "+v"(fv) : "n"(13 - 5 * LOOK_AT) : "memory");
            CF_LOOK(fv);
        }
        spin_all(fra, 18);
        if (w == 3 && (~cfm & 0xffff)) {
            WAIT_VM(0);
            lds_add(lb + C_WO, lane);
            wo_open = false;
            CF_LOOP(sfo, 0xffffull);
        }
        if (w == 3) lds_st32(lb + C_LK, 1);
        STAMP(14);
        STAMP(4);
      }
    } else {
        __builtin_amdgcn_s_setprio(3);
        const int c5 = wave < 4 ? wave : 4;
        const int d = vid * 5 + c5;
        unsigned Dw = *(const __attribute__((address_space(4))) unsigned*)(size_t)(ssm_D + (d & ~1));
        unsigned hp0 = *(const __attribute__((address_space(4))) unsigned*)(size_t)(ssm_k + (vid * 5 + 4) * S_LEN + w * 512 - 2);
        BARRIER();
        if (wave <= 4) hp0 = 0;
        asm volatile("" : "+s"(Dw), "+s"(hp0));
#define WO_CHUNK(chunk, lds) { const char* wb_ = (const char*)wo + (size_t)(chunk) * (64 * D_DIM * 2); \
            _Pragma("unroll") for (int p = 0; p < 10; ++p) GLS(wof[p], wb_, lds, p * 1024); }
#define KV_DONE() do { if (lds_add_rtn(lb + C_KVD, lane) == 9) { \
            if (lane == 0) __hip_atomic_store((u64*)(ws + WS_XF + (cq * 32 + rbv) * 128), tag, __ATOMIC_RELAXED, __HIP_MEMORY_SCOPE_AGENT); lds_add(lb + C_KVD, lane); } } while (0)
        u32x4 hv[4];
        const unsigned l16 = lane * 16;
        const char* hsrc = (const char*)ssm_k + (size_t)d * (S_LEN * 2) + (wave < 4 ? 0 : w * 1024);
#define H_LD(i) asm volatile("global_load_dwordx4 %0, %1, %2 offset:%3" : "=v"(hv[i]) : "v"(l16), "s"(hsrc), "n"((i) * 1024) : "memory")
        if (wave < 10) {
            const int m0 = (g & 1) * 2 + (g >> 1);
            const unsigned kp = opaque(lb + V_KST + (8 * m0 + q4) * 320 + (m0 >> 1) * 32 + wave * 32 + p4 * 8);
            const unsigned km = opaque(kp - 1280 - ((m0 & 1) ? 0 : 32));
            const unsigned sw_ = opaque(lb + V_STG + wave * 2048 + n16 * 128 + ((m0 + n16) & 7) * 16);
            const unsigned sr_ = opaque(lb + V_STG + wave * 2048 + (lane >> 3) * 128 + ((lane + (lane >> 3)) & 7) * 16);
            const unsigned e1 = opaque(WS_KV + (cq * 160 + wave * 16 + (lane >> 3)) * KV_PITCH + rbv * 128 + (lane & 7) * 16), e2 = opaque(e1 + 8 * KV_PITCH);
            asm volatile("s_waitcnt vmcnt(0)" : "+v"(skw) :: "memory");
            lds_add(lb + C_KV, lane);
            const f32x2 s0 = {lo_f(skw[0]), lo_f(skw[0])}, s1 = {hi_f(skw[0]), hi_f(skw[0])}, s2 = {lo_f(skw[1]), lo_f(skw[1])}, s3 = {hi_f(skw[1]), hi_f(skw[1])};
            if (H_EARLY == 1 && wave < 8) { H_LD(0); if (wave < 4) { H_LD(1); H_LD(2); H_LD(3); } }
            spin_ge(lb + C_KV, 10);
            if (H_EARLY == 2 && wave < 8) { H_LD(0); if (wave < 4) { H_LD(1); H_LD(2); H_LD(3); } }
            STAMP(2);
            u32x2 kq[2][3], vq[2][2];
#pragma unroll
            for (int i = 0; i < 2; ++i) {
                kq[i][0] = __builtin_bit_cast(u32x2, TRRD((lds_char*)(size_t)(km + i * 10304)));
#pragma unroll
                for (int e = 0; e < 2; ++e) kq[i][e + 1] = __builtin_bit_cast(u32x2, TRRD((lds_char*)(size_t)(kp + i * 10304 + e * 1280)));
#pragma unroll
                for (int e = 0; e < 2; ++e) vq[i][e] = __builtin_bit_cast(u32x2, TRRD((lds_char*)(size_t)(kp + i * 10304 + (V_VST - V_KST) + e * 1280)));
            }
            if (rbv == 0 && m0 == 0) kq[0][0] = (u32x2){0u, 0u};
            f32x2 kf[12], res[8];
#pragma unroll
            for (int e = 1; e < 12; ++e) {
                const unsigned u0 = kq[0][e >> 2][(e >> 1) & 1], u1 = kq[1][e >> 2][(e >> 1) & 1];
                kf[e] = (e & 1) ? (f32x2){hi_f(u0), hi_f(u1)} : (f32x2){lo_f(u0), lo_f(u1)};
            }
#pragma unroll
            for (int e = 0; e < 8; ++e) {
                const unsigned u0 = vq[0][e >> 2][(e >> 1) & 1], u1 = vq[1][e >> 2][(e >> 1) & 1];
                f32x2 a = kf[e + 1] * s3;
                a = kf[e + 2] * s2 + a;
                a = kf[e + 3] * s1 + a;
                a = kf[e + 4] * s0 + a;
                res[e] = a * ((e & 1) ? (f32x2){hi_f(u0), hi_f(u1)} : (f32x2){lo_f(u0), lo_f(u1)});
            }
#pragma unroll
            for (int i = 0; i < 2; ++i) {
                u32x4 pk;
#pragma unroll
                for (int e = 0; e < 4; ++e) pk[e] = pack2(res[2 * e][i], res[2 * e + 1][i]);
                *(lds_vu32x4*)(size_t)(sw_ ^ (i * 64)) = pk;
            }
            const u32x4 pk0 = lds_v128(sr_), pk1 = lds_v128(sr_ + 1024);
            __builtin_amdgcn_raw_buffer_store_b128(pk0, wsr, e1, 0, 16);
            __builtin_amdgcn_raw_buffer_store_b128(pk1, wsr, e2, 0, 16);
            lds_add(lb + C_KVST, lane);
            STAMP(3);
        }
        if (wave >= 8) {
            const unsigned wv = T_WV(), xv_ = T_XV();
            unsigned wof[10], qf[3];
#define AUX_ADDR() { _Pragma("unroll") for (int p = 0; p < 10; ++p) wof[p] = opaque(WO_OFF(p * 64 + lane)); \
                _Pragma("unroll") for (int j = 0; j < 3; ++j) { const int c = min(j * 64 + lane, 159); const int row = c / 10, ch = c - row * 10; qf[j] = opaque(row * (D_DIM * 2) + ch * 16); } \
                SB(); }
            if (wave == 10) {
                T_PAIR(2);
                AUX_ADDR();
                if (T4_W) spin_ge(lb + T4_W, T4_N);
                T_PAIR(4);
                if (WO_MALL && lane < 50) GLT((unsigned)((vid * 50 + lane) * 128), wo);
            } else if (wave == 11) {
                if (lane < 56) {
#pragma unroll
                    for (int c5 = 0; c5 < 5; ++c5)
                        *(lds_u32x4*)(size_t)(lb + (lane < 16 ? V_KV(c5) + lane * 16 : V_F + c5 * XC_BYTES + 15 * XC_ROW + 256 + (lane < 36 ? XC_COPY + (lane - 16) * 16 : XC_B + 4 + (lane - 36) * 16))) = (u32x4){0u, 0u, 0u, 0u};
                }
                const unsigned x4o = opaque(WS_KV + d * KV_PITCH + lane * 16);
                AUX_ADDR();
                spin_ge(lb + C_KVD, 11);
                if (POLL_DELAY > 0) __builtin_amdgcn_s_sleep(POLL_DELAY);
                const unsigned xfo = WS_XF + ((cq ^ CQ_X) * 32 + (lane & 31)) * 128;
                STAMP(3);
                const u32x2 fv = __builtin_amdgcn_raw_buffer_load_b64(wsr, xfo, 0, XF_AUX);
                const bool ok0_ = __all(fv[0] == tlo && fv[1] == thi);
                STAMP(4);
                if (!ok0_) {
                    lds_st32(lb + C_GO, 1);
#pragma nounroll
                    for (int i = 0; i < SPIN_CAP; ++i) {
                        asm volatile("" ::: "memory");
                        const u32x2 v = __builtin_amdgcn_raw_buffer_load_b64(wsr, xfo, 0, XF_AUX);
                        if (__all(v[0] == tlo && v[1] == thi)) break;
                        __builtin_amdgcn_s_sleep(POLL_SLEEP);
                    }
                }
                asm volatile("s_mov_b32 m0, %2\n\ts_nop 0\n\tglobal_load_lds_dwordx4 %0, %1 sc1\n\tglobal_load_lds_dwordx4 %0, %1 offset:1024 sc1\n\t"
                             "global_load_lds_dwordx4 %0, %1 offset:2048 sc1\n\tglobal_load_lds_dwordx4 %0, %1 offset:3072 sc1" ::"v"(x4o), "s"(ws), "s"(lb + V_KV4 + 256) : "memory");
                lds_add(lb + C_KVR, lane);
                STAMP(2);
                WAIT_VM(0);
                lds_st32(lb + C_TR, NOW());
                lds_st32(lb + C_TR + 4, (unsigned)__builtin_amdgcn_s_memtime());
                lds_st32(lb + C_HR, 4);
                STAMP(12);
                spin_ge(lb + C_XD, 4);
                lds_st32(lb + C_GO, 1);
            } else {
                WAIT_VM(0);
                KV_DONE();
                STAMP(11);
                const unsigned c8 = (wave - 8) * 80 + (lane >> 3), so = opaque(WS_Y + vid * 640 + (lane & 7) * 16);
                unsigned sy[10];
#pragma unroll
                for (int i = 0; i < 10; ++i) { const unsigned c = c8 + i * 8, b = c / 5; sy[i] = opaque(so + b * (D_DIM * 128) + (c - b * 5) * 128); }
#define O_FLAG() { if (lds_add_rtn(lb + C_OSC, lane) == 1) { if (lane == 0) __hip_atomic_store((u64*)(ws + WS_HF + sid * 128), tag, __ATOMIC_RELAXED, __HIP_MEMORY_SCOPE_AGENT); } }
                spin_ge(lb + C_KVD, 11);
#pragma unroll
                for (int i = 0; i < 3; ++i) if (i < 2 || lane < 32) __builtin_amdgcn_raw_buffer_store_b128(ones, wsr, WS_HX + sid * 5120 + (wave - 8) * 2560 + lane * 16 + i * 1024, 0, 16);
                if (lane < 2) __builtin_amdgcn_raw_buffer_store_b128(ones, wsr, WS_G + sid * 64 + (wave - 8) * 32 + lane * 16, 0, 16);
#pragma unroll
                for (int i = 0; i < OS_PRE; ++i) __builtin_amdgcn_raw_buffer_store_b128(ones, wsr, WS_O + sid * 20480 + (wave - 8) * 10240 + lane * 16 + i * 1024, 0, 16);
                waitvm<OS_PRE>();
                if (lds_add_rtn(lb + C_HGC, lane) == 1) { if (lane == 0) __hip_atomic_store((u64*)(ws + WS_HG + sid * 128), tag, __ATOMIC_RELAXED, __HIP_MEMORY_SCOPE_AGENT); }
                AUX_ADDR();
                if (OS_PRE == 10) { WAIT_VM(0); O_FLAG(); STAMP(11); }
                if (PRIO_EARLY) __builtin_amdgcn_s_setprio(AUX_PRIO);
                spin_ge(lb + SENT_W, SENT_N);
#pragma unroll
                for (int i = OS_PRE; i < 10; ++i) __builtin_amdgcn_raw_buffer_store_b128(ones, wsr, WS_O + sid * 20480 + (wave - 8) * 10240 + lane * 16 + i * 1024, 0, 16);
#pragma unroll
                for (int i = 0; i < 10; ++i) {
                    if (OS_PRE < 10 && i == YS_N) { waitvm<YS_N>(); O_FLAG(); STAMP(11); }
                    __builtin_amdgcn_raw_buffer_store_b128(ones, wsr, sy[i], 0, 16);
                }
                STAMP(4);
                if (WO2_FIRST) { WO_CHUNK(2 + w, lb + V_WO + (2 + w) * 10240); WAIT_VM(10); } else WAIT_VM(0);
                if (lds_add_rtn(lb + C_OS1, lane) == 1) {
                    lds_st32(lb + C_OWN, 1);
                    spin_ge(lb + C_HR, 4);
                    const unsigned r0 = lds_ld32(lb + C_TR), c0 = lds_ld32(lb + C_TR + 4);
#pragma nounroll
                    for (int it = 0; it < 1000 && (int)(NOW() - __builtin_amdgcn_readfirstlane(r0)) < 60; ++it) __builtin_amdgcn_s_sleep(2);
                    const unsigned t_ = NOW();
                    const float f_ = TICKS_PER_CYCLE(t_, c0, r0);
                    if (lane == 0) __builtin_amdgcn_raw_buffer_store_b128((u32x4){tlo, thi, r0 + (unsigned)(f_ * DY), 0u}, wsr, WS_SF + vid * 128, 0, 16);
                }
                STAMP(12);
            }
            __builtin_amdgcn_s_setprio(AUX_PRIO);
            if (wave == 10) {
                if (!X_TALL) spin_ge(lb + GO_W, GO_N);
                T_PAIR(6);
                if (T8_AT == 1) WAIT_VM(1); else if (T8_AT == 2) spin_ge(lb + C_XD, 4);
                T_PAIR(8);
                spin_mid(lb + WOT_W, WOT_N);
#pragma unroll
                for (int i = 0; i < 4; ++i) {
                    const int l0 = r * 240 + i * 60 + lane;
                    waitvm<WOT_PACE>();
                    if (lane < 60) GLT((unsigned)((l0 < 1280 ? l0 : l0 + 2560) * 128), wo);
                }
                {
                    const unsigned cl = lane * 16 + r;
                    if (cl < CODE_LINES) GLT(cl * 128, code0);
                }
                WAIT_VM(0);
                lds_st32(lb + C_WT, 1);
                STAMP(4);
                WO_CHUNK(4, lb + V_WO + 4 * 10240);
            }
            if (wave == 11 || (wave < 10 && !WO2_FIRST)) {
                spin_mid(lb + WO2_W, WO2_N);
                WO_CHUNK(2 + w, lb + V_WO + (2 + w) * 10240);
            }
#pragma unroll
            for (int j = 0; j < 3; ++j)
                if (j < 2 || lane < 32) GLS(qf[j], (const char*)x + (size_t)(ROWT(w) * 16) * (D_DIM * 2) + oc * 160, lb + w * 2560, V_QS + j * 1024);
            WAIT_VM(0);
            STAMP(5);
            Q_READ(V_QS);
            lds_add(lb + C_QR, lane);
            if (w < 2) {
                spin_mid(lb + (w ? C_QR : C_RED2), 4);
                spin_mid(lb + C_WT, 1);
                WO_CHUNK(w, lb + V_WO + w * 10240);
            }
            WAIT_VM(0);
            lds_add(lb + C_WO, lane);
            STAMP(6);
            if (wave == 10) {
                u64 cfm = ~0ull << 40;
                unsigned tv = 0, tu = 0;
                const unsigned cfo = CF_OFF();
                if (VL_DELAY > 0) __builtin_amdgcn_s_sleep(VL_DELAY);
                CF_LOOP(cfo, ~0ull);
                STAMP(10);
            }
            const unsigned ty = TY_GET();
            G_FIRST();
            G_MUL();
            spin_mid(lb + C_CONVN, 8);
            __builtin_amdgcn_s_setprio(3);
            u32x4 yv[3];
            const unsigned l16y = lane * 16;
            Y_TIME(ty);
            Y_FIRST();
            STAMP(7);
            P_STAGE(false);
        } else {
            const unsigned xb = lb + V_F + c5 * XC_BYTES;
            const unsigned hb = lb + V_KV(c5) + 256;
            const float Dd = (d & 1) ? hi_f(Dw) : lo_f(Dw);
            u32x4 gx[5], gv[5];
            if (H_EARLY) WAIT_VM(0);
            else {
                spin_ge(lb + C_KVST, 10);
                H_LD(0);
                if (wave < 4) { H_LD(1); H_LD(2); H_LD(3); WAIT_VM(4); }
                else if (GATE_EARLY) {
                    const unsigned go = (sid * 16 + w * 4 + g) * (D_DIM * 2) + n16 * 16;
#pragma unroll
                    for (int i = 0; i < 5; ++i) {
                        asm volatile("global_load_dwordx4 %0, %1, %2 offset:%3" : "=v"(gx[i]) : "v"(go), "s"(x), "n"(i * 256) : "memory");
                        asm volatile("global_load_dwordx4 %0, %1, %2 offset:%3" : "=v"(gv[i]) : "v"(n16 * 16), "s"(gw), "n"(i * 256) : "memory");
                    }
                    WAIT_VM(11);
                } else WAIT_VM(1);
            }
            KV_DONE();
            STAMP(4);
            {
                const unsigned da = xb + (3 - g) * XC_ROW + (15 - n16) * 16 + (wave < 4 ? 0 : -w * (4 * XC_ROW));
                u32x4 ra[4], rb[4];
                asm volatile("s_cmp_eq_u32 %4, 0\n\ts_cbranch_scc0 1f\n\ts_waitcnt vmcnt(0)\n1:\n\ts_waitcnt vmcnt(10)" : "+v"(hv[0]), "+v"(hv[1]), "+v"(hv[2]), "+v"(hv[3]) : "s"(GATE_EARLY * (wave >> 2)) : "memory", "scc");
#define H_PREV(i) __builtin_amdgcn_update_dpp((i) == 0 ? hp0 : __builtin_amdgcn_readlane(hv[(i) == 0 ? 0 : (i) - 1][3], 63), hv[i][3], 0x138, 0xf, 0xf, false)
#define BFI16(lo, hi) (((lo) & 0xffffu) | ((hi) & 0xffff0000u))
#define H_PREP(i) { ra[i] = (u32x4){__builtin_amdgcn_alignbit(hv[i][3], hv[i][3], 16), __builtin_amdgcn_alignbit(hv[i][2], hv[i][2], 16), __builtin_amdgcn_alignbit(hv[i][1], hv[i][1], 16), __builtin_amdgcn_alignbit(hv[i][0], hv[i][0], 16)}; \
                    rb[i] = (u32x4){BFI16(hv[i][3], hv[i][2]), BFI16(hv[i][2], hv[i][1]), BFI16(hv[i][1], hv[i][0]), BFI16(hv[i][0], H_PREV(i))}; \
                    *(lds_u32x4*)(size_t)(da + (3 - (i)) * (4 * XC_ROW) + XC_COPY) = ra[i]; *(lds_u32x4*)(size_t)(da + (3 - (i)) * (4 * XC_ROW) + XC_B + 4) = rb[i]; }
#define H_DUP(i) { *(lds_u32x4*)(size_t)(da + (3 - (i)) * (4 * XC_ROW) + XC_COPY - 32) = ra[i]; *(lds_u32x4*)(size_t)(da + (3 - (i)) * (4 * XC_ROW) + XC_B + 4 - 32) = rb[i]; }
                H_PREP(0)
                if (wave < 4) {
                    H_PREP(1) H_PREP(2) H_PREP(3)
                    if (n16 == 15) { H_DUP(0) H_DUP(1) H_DUP(2) if (g < 3) H_DUP(3) }
                } else {
                    if (n16 == 15 && (w < 3 || g < 3)) H_DUP(0)
                }
            }
            STAMP(9);
            const int j0 = wave >= 4 ? 2 * w : 0;
            const int m16 = HYB ? n16 : (n16 & 3) | ((n16 & 4) << 1) | ((n16 & 8) >> 1);
            const unsigned kp = hb + 2 * (256 * j0 + 128 * (g >> 1) + 8 * m16 + 4 * (g & 1));
            const int ts = 256 * (j0 + (g & 1)) + 128 * (g >> 1) + 8 * m16;
            const unsigned yo = WS_Y + d * 128 + (HYB ? (ts >> 6) * (D_DIM * 128) + (ts & 63) * 2 : ((ts >> 7) * 2 + ((ts >> 5) & 1)) * (D_DIM * 128) + (((ts >> 6) & 1) * 32 + (ts & 31)) * 2);
            if (wave < 4) {
                const int b1 = n16 >> 3, phi = n16 & 7;
                const unsigned xp = xb + ((phi & 1) ? XC_COPY - 4 : XC_B) + (1 - b1) * XC_ROW + 2 * (8 - 2 * (phi >> 1) + 8 * g);
                const unsigned hp = hb + 2 * (8 * (m16 + g) - 120);
                const int sx4 = (4 - w) * XC_BYTES + (w >> 1) * XC_ROW + (w & 1) * 128, sh4 = V_KV4 - w * KV_BYTES + w * 128;
                unsigned c0_, c1_;
                __builtin_amdgcn_s_setprio(CONV_PRIO);
                spin_ge(lb + C_KVR, 5);
                STAMP(6);
                CSTAMP(0);
                TS(11);
                asm volatile(CONV_ASM
                             : [cnt] "=&s"(c0_), [tmp] "=&s"(c1_)
                             : [xp] "v"(xp), [hp] "v"(hp), [sx4] "s"(sx4), [sh4] "s"(sh4), [kvl] "s"(hb), [kvr] "v"(kp), [scr] "v"(lb + V_SCR + w * 8192 + l16),
                               [ctl] "v"(lb + L_CTRL), [dd] "s"(Dd), [cap] "s"(SPIN_CAP), [xo] "v"(WS_KV + d * KV_PITCH + l16), [wsp] "s"(ws), [yo] "v"(yo)
                             : CONV_CLOB);
                __builtin_amdgcn_s_setprio(3);
                TAIL_ADDR();
                CSTAMP(1);
            } else {
                lds_add(lb + C_KVR, lane);
                const int grow = sid * 16 + w * 4 + g;
                const unsigned go = opaque(grow * (D_DIM * 2) + n16 * 16);
                if (!GATE_EARLY) {
#pragma unroll
                    for (int i = 0; i < 5; ++i) gv[i] = *(const u32x4*)((const char*)gw + n16 * 16 + i * 256);
                }
                unsigned wof[10];
#pragma unroll
                for (int p = 0; p < 10; ++p) wof[p] = opaque(WO_OFF(p * 64 + lane));
                TAIL_ADDR();
                SB();
                __builtin_amdgcn_s_setprio(AUX_PRIO);
                {
                    if (GATE_LATE) spin_mid(lb + GATE_W, GATE_N);
                    if (GATE_EARLY) asm volatile("s_waitcnt vmcnt(0)" : "+v"(gx[0]), "+v"(gx[1]), "+v"(gx[2]), "+v"(gx[3]), "+v"(gx[4]), "+v"(gv[0]), "+v"(gv[1]), "+v"(gv[2]), "+v"(gv[3]), "+v"(gv[4]) :: "memory");
                    else {
#pragma unroll
                        for (int i = 0; i < 5; ++i) gx[i] = *(const u32x4*)((const char*)x + go + i * 256);
                    }
                    float sa[4] = {0.f, 0.f, 0.f, 0.f};
#pragma unroll
                    for (int i = 0; i < 5; ++i)
#pragma unroll
                        for (int e = 0; e < 4; ++e) sa[e] = __builtin_fmaf(hi_f(gx[i][e]), hi_f(gv[i][e]), __builtin_fmaf(lo_f(gx[i][e]), lo_f(gv[i][e]), sa[e]));
                    float sacc = (sa[0] + sa[1]) + (sa[2] + sa[3]);
                    sacc += __builtin_amdgcn_update_dpp(0.f, sacc, 0x128, 0xf, 0xf, true);
                    sacc += __builtin_amdgcn_update_dpp(0.f, sacc, 0x124, 0xf, 0xf, true);
                    sacc += __builtin_amdgcn_update_dpp(0.f, sacc, 0x122, 0xf, 0xf, true);
                    sacc += __builtin_amdgcn_update_dpp(0.f, sacc, 0x121, 0xf, 0xf, true);
                    const float gres = rbf(__builtin_amdgcn_rcpf(1.f + __expf(-rbf(sacc))));
                    spin_ge(lb + C_HGC, 2);
                    if (n16 == 0) __builtin_amdgcn_raw_buffer_store_b32(__float_as_uint(gres), wsr, WS_G + grow * 4, 0, 16);
                    STAMP(6);
                }
                spin_ge(lb + C_RED, 4);
                STAMP(5);
                f32x4 acc[2];
#pragma unroll
                for (int e = 0; e < 2; ++e) {
                    const u32x2 kk = *(const lds_u32x2*)(size_t)(kp + e * 512);
                    acc[e] = (f32x4){lo_f(kk[0]) * Dd, hi_f(kk[0]) * Dd, lo_f(kk[1]) * Dd, hi_f(kk[1]) * Dd};
#pragma unroll
                    for (int o = 0; o < 4; ++o) acc[e] += *(const lds_f32x4*)(size_t)(lb + V_SCR + o * 8192 + (2 * w + e) * 1024 + l16);
                }
                lds_add(lb + C_RED2, lane);
                const auto r0 = __builtin_amdgcn_permlane16_swap(min(pack2(acc[0][0], acc[0][1]), 0xfffffffeu), min(pack2(acc[1][0], acc[1][1]), 0xfffffffeu), false, false);
                const auto r1 = __builtin_amdgcn_permlane16_swap(min(pack2(acc[0][2], acc[0][3]), 0xfffffffeu), min(pack2(acc[1][2], acc[1][3]), 0xfffffffeu), false, false);
                spin_mid(lb + C_OWN, 1);
                __builtin_amdgcn_raw_buffer_store_b128((u32x4){r0[0], r1[0], r0[1], r1[1]}, wsr, yo, 0, 16);
                STAMP(7);
                spin_ge(lb + C_RED2, 4);
                spin_mid(lb + C_WT, 1);
                WO_CHUNK(6 + w, lb + V_WO + (6 + w) * 10240);
                WAIT_VM(0);
                lds_add(lb + C_WO, lane);
            }
            lds_add(lb + C_CONVN, lane);
            STAMP(12);

        }
    }

#define NPOLL(ph, it)
#define NRETRY()
#define V_WAITC 4
#define W0_DELAY 0
#define W1_DELAY 0
#define H_DELAY 0
#define WA0 2
#define WA1 7
#define PRE_PRIO 0
#define PASS1_PRIO 2
    {
        FLUSH_LO();
        const f32x4 z4 = {0.f, 0.f, 0.f, 0.f};
        STAMP(12);
        __builtin_amdgcn_s_setprio(ENT_PRIO);
        if (wo_open) { WAIT_VM(0); lds_add(lb + C_WO, lane); }
        unsigned ta, tb;
#pragma nounroll
        for (int it = 0; it < SPIN_CAP; ++it) {
            u32x2 t_;
            asm volatile("ds_read_b64 %0, %1\n\ts_waitcnt lgkmcnt(0)" : "=v"(t_) : "v"(lb + C_TM + DUE_H * 16 + kh * 8) : "memory");
            ta = __builtin_amdgcn_readfirstlane(t_[0]); tb = __builtin_amdgcn_readfirstlane(t_[1]);
            if (ta && tb) break;
            __builtin_amdgcn_s_sleep(3);
        }
        const bool sw = (int)(ta - tb) > 0;
        const unsigned t0 = (sw ? tb : ta) - (isG ? G_LEAD : 0), t1 = (sw ? ta : tb) - (isG ? G_LEAD : 0);
        const int k0 = sw * 5120, k1 = 5120 - k0;
        spin_mid(lb + C_WO, isG ? 4 : 8);
        lds_char* bc[2] = {(lds_char*)(size_t)(pc + k0 * 5), (lds_char*)(size_t)(pc + k1 * 5)};
        lds_char* bd[2] = {(lds_char*)(size_t)(pd + k0 * 5), (lds_char*)(size_t)(pd + k1 * 5)};
#define WO_RD(s, ct) cat8(TRRD(bc[(s) / 5] + ((s) % 5) * 5120 + (ct) * 32), TRRD(bd[(s) / 5] + ((s) % 5) * 5120 + (ct) * 32))
#define WO_PRE(s0) { _Pragma("unroll") for (int s = s0; s < s0 + NBUF; ++s) _Pragma("unroll") for (int ct = 0; ct < 5; ++ct) wB[s % NBUF][ct] = WO_RD(s, ct); SB(); }
        bf16x8 wB[NBUF][5];
        WO_PRE(0);
        STAMP(14);
#define T_WAIT(t) { _Pragma("nounroll") for (int it = 0; it < 20000 && (int)(NOW() - (t)) < 0; ++it) {} }
        T_WAIT(t0);
        if (isG) spin_ge(lb + C_OS1, 8);
        __builtin_amdgcn_s_setprio(T_PRIO);
        const unsigned m = __builtin_amdgcn_readfirstlane((int)(NOW() + T_JOIN - t1) >= 0 ? 15 : 3);
        STAMP(8);
        u32x4 of[10];
        const char *ws0 = ws + k0, *ws1 = ws + k1;
#define O_LD(s) asm volatile("global_load_dwordx4 %0, %1, %2 offset:%3 sc1" : "=v"(of[s]) : "v"(ofo), "s"((s) < 5 ? ws0 : ws1), "n"(((s) % 5) * 1024 - 2048) : "memory")
#define O_WAIT(s) { if ((s) >= 5) asm volatile("s_waitcnt vmcnt(%1)" : "+v"(of[s]) : "n"(9 - (s)) : "memory"); \
            else asm volatile("s_cmp_eq_u32 %1, 15\n\ts_cbranch_scc1 1f\n\ts_waitcnt vmcnt(%2)\n1:\n\ts_waitcnt vmcnt(%3)" : "+v"(of[s]) : "s"(m), "n"(4 - (s) % 5), "n"(9 - (s)) : "memory", "scc"); }
#pragma unroll
        for (int s = 0; s < 5; ++s) O_LD(s);
        if (m == 15) {
#pragma unroll
            for (int s = 5; s < 10; ++s) O_LD(s);
        }
        __builtin_amdgcn_s_waitcnt(0xc07f);
        SB();
        if (ph) __builtin_amdgcn_s_setprio(PASS1_PRIO); else __builtin_amdgcn_s_setprio(PASS_PRIO);
        STAMP(9);
        f32x4 acc[2][5];
#define GROUP(i) { _Pragma("unroll") for (int s = 5 * i; s < 5 * i + 5; ++s) _Pragma("unroll") for (int ct = 0; ct < 5; ++ct) { \
                if (ct == 0) O_WAIT(s); \
                acc[i][ct] = __builtin_amdgcn_mfma_f32_16x16x32_bf16(wB[s % NBUF][ct], __builtin_bit_cast(bf16x8, of[s]), s % 5 ? acc[i][ct] : z4, 0, 0, 0); SB(); \
                if (s + NBUF < 10) { wB[s % NBUF][ct] = WO_RD(s + NBUF, ct); SB(); } } }
#define AGAIN(i) if (__builtin_expect(__any(acc[i][0][0] != acc[i][0][0]), 0)) { \
            NRETRY(); \
            _Pragma("unroll") for (int s = 5 * i; s < 5 * i + 5; ++s) O_LD(s); \
            WO_PRE(5 * i); \
            _Pragma("nounroll") \
            for (int it = 0; it < SPIN_CAP; ++it) { \
                asm volatile("s_waitcnt vmcnt(0)" : "+v"(of[5 * i]), "+v"(of[5 * i + 1]), "+v"(of[5 * i + 2]), "+v"(of[5 * i + 3]), "+v"(of[5 * i + 4]) :: "memory"); \
                bool any_ = false; \
                _Pragma("unroll") for (int s = 5 * i; s < 5 * i + 5; ++s) if (SENT4(of[s])) { \
                    asm volatile("global_load_dwordx4 %0, %1, %2 offset:%3 sc1" : "+v"(of[s]) : "v"(ofo), "s"((s) < 5 ? ws0 : ws1), "n"(((s) % 5) * 1024 - 2048) : "memory"); any_ = true; } \
                if (!any_) break; \
            } \
            _Pragma("unroll") for (int s = 5 * i; s < 5 * i + 5; ++s) _Pragma("unroll") for (int ct = 0; ct < 5; ++ct) { \
                acc[i][ct] = __builtin_amdgcn_mfma_f32_16x16x32_bf16(wB[s % NBUF][ct], __builtin_bit_cast(bf16x8, of[s]), s % 5 ? acc[i][ct] : z4, 0, 0, 0); SB(); \
                if (s + NBUF < 5 * i + 5) { wB[s % NBUF][ct] = WO_RD(s + NBUF, ct); SB(); } } }
        GROUP(0);
        if (m != 15) {
            T_WAIT(t1);
#pragma unroll
            for (int s = 5; s < 10; ++s) O_LD(s);
            __builtin_amdgcn_s_waitcnt(0xc07f);
            SB();
        }
        STAMP(10);
        unsigned fr = 4;
        if (!isG || ph) fr = lds_v32(lb + (isG ? C_QR : C_CONVN));
        SB();
        GROUP(1);
        AGAIN(0);
        AGAIN(1);
        __builtin_amdgcn_s_setprio(T_PRIO);
        STAMP(11);
        if (__builtin_amdgcn_readfirstlane(fr) < (isG ? 4 : 8)) spin_ge(lb + (isG ? C_QR : C_CONVN), isG ? 4 : 8);
#pragma unroll
        for (int ct = 0; ct < 5; ++ct) *(lds_f32x4*)(size_t)(xa + ct * 1024) = acc[0][ct] + acc[1][ct];
        lds_add(lb + C_XCH + trt * 4, lane);
        FLUSH();
    }
}

#undef x
#undef kin
#undef vin
#undef shift_k
#undef ssm_k
#undef ssm_D
#undef wqkv
#undef wo
#undef gw
#undef ws
#undef out
#undef wsr
extern "C" void kernel_launch(void* const* d_in, const int* in_sizes, int n_in, void* d_out, int out_size,
                              void* d_ws, size_t ws_size, hipStream_t stream) {
    const u16* q = (const u16*)d_in[0];
    const u16* k = (const u16*)d_in[1];
    const u16* v = (const u16*)d_in[2];
    const u16* shift_k = (const u16*)d_in[3];
    const u16* ssm_k = (const u16*)d_in[4];
    const u16* ssm_D = (const u16*)d_in[5];
    const u16* wqkv = (const u16*)d_in[7];
    const u16* wo = (const u16*)d_in[8];
    const u16* gw = (const u16*)d_in[9];
    fused_kernel<<<256, 768, 0, stream>>>(q, wqkv, k, v, shift_k, ssm_k, ssm_D, wo, gw, (char*)d_ws, (u16*)d_out);
}
```

```cpp
#define CODE_LINES 370
#define V_DELAY 6
#define MAP1 1
#define HINT_AT -1
#define VY_DELAY 10
#define GY_DELAY 4
#include <hip/hip_runtime.h>
#include <hip/hip_bf16.h>
#include <stdint.h>

#define S_LEN 2048
#define D_DIM 640
#define NQKV 1920

typedef unsigned short u16;
typedef unsigned long long u64;
typedef __attribute__((ext_vector_type(8))) short bf16x8;
typedef __attribute__((ext_vector_type(4))) short s16x4;
typedef __attribute__((ext_vector_type(4))) float f32x4;
typedef __attribute__((ext_vector_type(2))) float f32x2;
typedef __attribute__((ext_vector_type(4))) unsigned u32x4;
typedef __attribute__((ext_vector_type(2))) unsigned u32x2;
typedef __attribute__((ext_vector_type(2))) __bf16 bf16x2_t;
typedef __attribute__((address_space(3))) s16x4 lds_s16x4;
typedef __attribute__((address_space(3))) const s16x4 lds_cs16x4;
typedef __attribute__((address_space(3))) const bf16x8 lds_cbf16x8;
typedef __attribute__((address_space(3))) char lds_char;

#define __float_as_uint(f) __builtin_bit_cast(unsigned, (float)(f))
#define __uint_as_float(u) __builtin_bit_cast(float, (unsigned)(u))
#define __any(c) (__builtin_amdgcn_ballot_w64(c) != 0)
#define __all(c) (__builtin_amdgcn_ballot_w64(!(c)) == 0)
extern "C" __device__ unsigned long long llvm_dispatch_id() __asm("llvm.amdgcn.dispatch.id");

__device__ __forceinline__ float bf2f(u16 x) { return __uint_as_float(((unsigned)x) << 16); }
__device__ __forceinline__ u16 f2bf(float f) {
    __hip_bfloat16 b = __float2bfloat16(f);
    return *reinterpret_cast<u16*>(&b);
}
__device__ __forceinline__ float rbf(float f) { return bf2f(f2bf(f)); }
__device__ __forceinline__ float lo_f(unsigned u) { return __uint_as_float(u << 16); }
__device__ __forceinline__ float hi_f(unsigned u) { return __uint_as_float(u & 0xffff0000u); }
__device__ __forceinline__ unsigned pack2(float a, float b) {
    return __builtin_bit_cast(unsigned, __builtin_convertvector((f32x2){a, b}, bf16x2_t));
}
__device__ __forceinline__ float dot2(unsigned a, unsigned b, float c) {
    return __builtin_amdgcn_fdot2_f32_bf16(__builtin_bit_cast(bf16x2_t, a), __builtin_bit_cast(bf16x2_t, b), c, false);
}

#define SB() __builtin_amdgcn_sched_barrier(0)
#define WAIT_VM(n) asm volatile("s_waitcnt vmcnt(" #n ")" ::: "memory")
#define RAW_BARRIER() do { asm volatile("s_waitcnt lgkmcnt(0)" ::: "memory"); __builtin_amdgcn_s_barrier(); } while (0)

#define KV_PITCH 4224
#define WS_KV 4096
#define WS_O (WS_KV + D_DIM * KV_PITCH)
#define WS_Y (WS_O + D_DIM * S_LEN * 2)
#define WS_G (WS_Y + D_DIM * S_LEN * 2)
#define WS_XF (WS_G + S_LEN * 4)
#define WS_SF (WS_XF + 128 * 128)
#define WS_HF (WS_SF + 128 * 128)
#define WS_HN (WS_HF + 128 * 128)
#define WS_HG (WS_HN + 128 * 128)
#define WS_HX (WS_HG + 128 * 128)
#define WS_END (WS_HX + 128 * 5120)
#define WS_DBG (WS_END + 4096)

#define L_A 0
#define A_SLOT 18432
#define L_W 55296
#define W_SLOT 16384
#define W_PITCH 512
#define NW 6
#define G_XCH 69632
#define G_Y 0
#define G_WO1 18432
#define G_WO0 (G_XCH + 20480)
#define G_Q (G_WO0 + 51200)
#define G_VX 153600
#define G_HX 158208
#define G_VXD (G_HX + 4608)
#define KV_BYTES 4352
#define V_F (4 * KV_BYTES)
#define XC_ROW 288
#define XC_B 12
#define XC_COPY 4944
#define XC_BYTES (XC_COPY + 17 * XC_ROW)
#define V_KV4 (V_F + 5 * XC_BYTES)
#define V_KV(c) ((c) < 4 ? (c) * KV_BYTES : V_KV4)
#define V_WO (V_KV4 + KV_BYTES - 10240)
#define V_KST (V_WO + 10240 + 1312)
#define V_VST (V_KST + 20608)
#define V_STG 113536
#define V_QS (V_WO + 10240)
#define V_SCR 130560
#define V_XCH 0
#define V_Y 40960
#define Y_PITCH 160
#define L_TOUCH 163328
#define L_CTRL 163584
#define LDS_TOTAL 163840
static_assert(V_VST + 20608 <= V_STG && V_STG % 128 == 0 && V_STG + 20480 <= L_TOUCH && V_WO >= V_F + 4 * XC_BYTES && V_WO + 10 * 10240 <= L_TOUCH &&
              V_WO + 6 * 10240 <= V_SCR && V_SCR + 32768 <= L_TOUCH && V_Y + 80 * Y_PITCH <= V_WO && V_WO % 16 == 0, "lds");
static_assert(G_Q + 10240 <= G_VX && G_WO1 + 51200 <= G_XCH && L_W + NW * W_SLOT <= G_VX && G_VX + 4608 <= G_HX && G_VXD + 520 <= L_CTRL && G_Y + 80 * Y_PITCH <= A_SLOT && G_Q + 20480 <= G_VXD, "lds");
#define C_FREE (L_CTRL + 16)
#define C_OST (L_CTRL + 48)
#define C_SENT (L_CTRL + 52)
#define C_WO (L_CTRL + 56)
#define C_KV (L_CTRL + 60)
#define C_KVST (L_CTRL + 64)
#define C_CONVN (L_CTRL + 68)
#define C_Y (L_CTRL + 72)
#define C_KVR (L_CTRL + 76)
#define C_XD (L_CTRL + 80)
#define C_RED (L_CTRL + 84)
#define C_OWN (L_CTRL + 88)
#define C_QR (L_CTRL + 92)
#define C_TY (L_CTRL + 96)
#define C_TR (L_CTRL + 112)
#define C_OS1 (L_CTRL + 104)
#define C_OSC (L_CTRL + 108)
#define C_SFC (L_CTRL + 136)
#define C_GO (L_CTRL + 140)
#define C_HR (L_CTRL + 144)
#define C_RED2 (L_CTRL + 148)
#define C_KVD (L_CTRL + 152)
#define C_WT (L_CTRL + 156)
#define C_HX (L_CTRL + 160)
#define C_XCH (L_CTRL + 176)
#define C_FULL (L_CTRL + 208)
#define C_HS (L_CTRL + 100)
#define C_OW2 (L_CTRL + 224)
#define C_HGC (L_CTRL + 228)
#define C_LK (L_CTRL + 52)
#define C_TM (L_CTRL + 192)

#define KNOB(n, v)
#define SPIN_CAP 4000000
#define GH 0
#define WO_PRIO 0
#define T_PRIO 3
#define C0_PRIO 2
#define C1_PRIO 1
#define L_PRIO 3
#define O_SLEEP 4
#define O_DELAY 0
#define POLL_SLEEP 12
#define Y_SLEEP 8
#define W_SLEEP 4
#define X_SLEEP 2
#define X_DELAY 0
#define CONV_PRIO 2
#define NBUF 4
#define WATCH_RT 2
#define JL 17
#define RING_E0 70
#define RING_F 105
#define WOT_SPLIT 0
#define GO_W C_KVD
#define GO_N 11
#define SENT_PACE 10
#define GATE_LATE 0
#define WOT_PACE 7
#define SENT_W C_XD
#define SENT_N 4
#define O_AHEAD 4
#define PASS_PRIO 1
#define G_OST 8
#define V_ODELAY 0
#define W_LATE 1
#define Y_SL0 0
#define SF_DELAY 8
#define WO_MALL 1
#define T4_EARLY 0
#define POLL_DELAY 24
#define T4_W C_KV
#define T4_N 10
#define SENT_DELAY 0
#define CF_W C_RED2
#define CF_N 4
#define CF_DELAY 0
#define NB3 6
#define GW0_DELAY 4
#define GW1_DELAY 4
#define VW0_DELAY 0
#define VW1_DELAY 6
#define ROT_OC 3
#define AUX_PRIO 1
#define RING_T 5
#define H_EARLY 0
#define T8_AT 1
#define RING_J 7
#define RING_F2 105
#define LATE_W C_RED
#define LATE_N 4
#define WOT_W C_XD
#define WOT_N 4
#define GATE_W C_XD
#define GATE_N 4
#define WO2_W C_XD
#define WO2_N 4
#define V_BOTH true
#define NRES 4
#define OS_EARLY 1
#define OS_DELAY 0
#define WO2_FIRST 0
#define HINT_W 3
#define D0 5800
#define D1 6400
#define DY 7500
#define CF_SLEEP 6
#define V_MORE 2
#define G_MORE 1
#define SELF_AT 0
#define LOOK_AT 2
#define VL_DELAY 0
#define ENT_PRIO 1
#define G_GATE0 8
#define G_GATE1 8
#define YEARLY 1
#define Y_SOON 10
#define T_JOIN 0
#define X_NOOS 0
#define X_NOYS 0
#define X_NOG 0
#define X_TALL 0
#define L_FIRST 1
#define X_SHIFT 0
#define D_AHEAD 9000
#define HALO_EARLY 1
#define OUT_P 0
#define OUT_POL ""
#define G_LEAD 0
#define XF_AUX 16
#define CQ_X 2
#define HYB 0
#define RETOUCH 1
#define HX_AT 3
#define HX_SLEEP 2
#define NHRETRY()
#define HL_AT 2
#define HS_N 4
#define HS_POS 1
#define FB_T 30
#define TAILM 1
#define L19_PRIO 3
#define E_PRIO 3
#define GATE_EARLY 0
#define PRIO_EARLY 0
#define HG_AT 6
#define OW2_AT 0
#define PF_SLEEP 6
#define OS_PRE 10
#define YS_N 5
#define KROT ((0x27057250u >> (xcd * 4)) & 15)

#define STAMP(id)
#define FLUSH()
#define CSTAMP(i)

#define FLUSH_LO()
#define TS(i)
#define TSFLUSH()
__device__ __forceinline__ unsigned lds_ld32(unsigned a) {
    unsigned v;
    asm volatile("ds_read_b32 %0, %1\n\ts_waitcnt lgkmcnt(0)" : "=v"(v) : "v"(a) : "memory");
    return v;
}
__device__ __forceinline__ unsigned lds_min4(unsigned a) {
    u32x4 v;
    asm volatile("ds_read_b128 %0, %1\n\ts_waitcnt lgkmcnt(0)" : "=v"(v) : "v"(a) : "memory");
    return min(min(v[0], v[1]), min(v[2], v[3]));
}
__device__ __forceinline__ unsigned lds_min8(unsigned a) {
    u32x4 v, u;
    asm volatile("ds_read_b128 %0, %2\n\tds_read_b128 %1, %2 offset:16\n\ts_waitcnt lgkmcnt(0)" : "=&v"(v), "=&v"(u) : "v"(a) : "memory");
    return min(min(min(v[0], v[1]), min(v[2], v[3])), min(min(u[0], u[1]), min(u[2], u[3])));
}
__device__ __forceinline__ void lds_st32(unsigned a, unsigned v) {
    asm volatile("ds_write_b32 %0, %1" ::"v"(a), "v"(v) : "memory");
}
__device__ __forceinline__ unsigned lds_add_rtn(unsigned a, int lane) {
    unsigned old = 0;
    if (lane == 0) {
        unsigned one = 1;
        asm volatile("ds_add_rtn_u32 %0, %1, %2\n\ts_waitcnt lgkmcnt(0)" : "=v"(old) : "v"(a), "v"(one) : "memory");
    }
    return __builtin_amdgcn_readfirstlane(old);
}
__device__ __forceinline__ void lds_add(unsigned a, int lane, unsigned n = 1) {
    if (lane == 0) asm volatile("ds_add_u32 %0, %1" ::"v"(a), "v"(n) : "memory");
}
__device__ __forceinline__ void spin_min4(unsigned a, unsigned target) {
#pragma nounroll
    for (int i = 0; i < SPIN_CAP; ++i) {
        if (__builtin_amdgcn_readfirstlane(lds_min4(a)) >= target) break;
        __builtin_amdgcn_s_sleep(1);
    }
}
__device__ __forceinline__ void spin_min8(unsigned a, unsigned target) {
#pragma nounroll
    for (int i = 0; i < SPIN_CAP; ++i) {
        if (__builtin_amdgcn_readfirstlane(lds_min8(a)) >= target) break;
        __builtin_amdgcn_s_sleep(1);
    }
}
__device__ __forceinline__ void spin_ge(unsigned a, unsigned target) {
#pragma nounroll
    for (int i = 0; i < SPIN_CAP; ++i) {
        if (__builtin_amdgcn_readfirstlane(lds_ld32(a)) >= target) break;
        __builtin_amdgcn_s_sleep(1);
    }
}
typedef __attribute__((address_space(3))) volatile u32x4 lds_vu32x4;
typedef __attribute__((address_space(3))) volatile unsigned lds_vu32_t;
__device__ __forceinline__ u32x4 lds_v128(unsigned a) { return *(lds_vu32x4*)(size_t)a; }
__device__ __forceinline__ unsigned lds_v32(unsigned a) { return *(lds_vu32_t*)(size_t)a; }
__device__ __forceinline__ float fmax_raw(float a, float b) { return __builtin_elementwise_maximum(a, b); }
__device__ __forceinline__ float fmax3_raw(float a, float b, float c) { return __builtin_elementwise_maximum(__builtin_elementwise_maximum(a, b), c); }
__device__ __forceinline__ unsigned opaque(unsigned v) { asm volatile("" : "+v"(v)); return v; }
__device__ __forceinline__ bf16x8 cat8(s16x4 a, s16x4 b) { return __builtin_shufflevector(a, b, 0, 1, 2, 3, 4, 5, 6, 7); }

__device__ __forceinline__ void poll_flags(__amdgpu_buffer_rsrc_t rs, unsigned off, unsigned tlo, unsigned thi) {
#pragma nounroll
    for (int i = 0; i < SPIN_CAP; ++i) {
        __builtin_amdgcn_s_sleep(PF_SLEEP);
        asm volatile("" ::: "memory");
        const u32x2 v = __builtin_amdgcn_raw_buffer_load_b64(rs, off, 0, 16);
        if (__all(v[0] == tlo && v[1] == thi)) break;
        __builtin_amdgcn_s_sleep(POLL_SLEEP);
    }
    __builtin_amdgcn_fence(__ATOMIC_ACQUIRE, "wavefront");
}

__device__ __forceinline__ void spin_slow(unsigned a, unsigned target) {
#pragma nounroll
    for (int i = 0; i < SPIN_CAP; ++i) {
        if (__builtin_amdgcn_readfirstlane(lds_ld32(a)) >= target) break;
        __builtin_amdgcn_s_sleep(6);
    }
}
__device__ __forceinline__ void spin_mid(unsigned a, unsigned target) {
#pragma nounroll
    for (int i = 0; i < SPIN_CAP; ++i) {
        if (__builtin_amdgcn_readfirstlane(lds_ld32(a)) >= target) break;
        __builtin_amdgcn_s_sleep(3);
    }
}
__device__ __forceinline__ void spin_all(unsigned a, unsigned target) {
#pragma nounroll
    for (int i = 0; i < SPIN_CAP; ++i) {
        if (__all(lds_ld32(a) >= target)) break;
        __builtin_amdgcn_s_sleep(1);
    }
}

template <int N> __device__ __forceinline__ void waitvm() { asm volatile("s_waitcnt vmcnt(%0)" ::"n"(N) : "memory"); }
#define BARRIER() asm volatile("s_barrier" ::: "memory")
#define GLS(voff, sbase, lw, ldsoff) asm volatile("s_add_u32 m0, %2, %3\n\ts_nop 0\n\tglobal_load_lds_dwordx4 %0, %1" ::"v"(voff), "s"(sbase), "s"(lw), "n"(ldsoff) : "memory", "scc")
#define GLT(voff, sbase) asm volatile("s_mov_b32 m0, %2\n\ts_nop 0\n\tglobal_load_lds_dword %0, %1" ::"v"(voff), "s"(sbase), "s"(lb + L_TOUCH) : "memory")
#define TRRD(p) __builtin_amdgcn_ds_read_tr16_b64_v4i16((lds_s16x4*)(p))

constexpr int ring_total(int NA) { return 10 * (8 + NA); }
constexpr int ring_need(int J, int NA) { return J < 0 ? NA + 8 : (J & 1) ? (NA + 8) * ((J + 3) / 2) : (NA + 8) * ((J + 2) / 2) + 4 + NA; }
constexpr int ring_isA(int p, int NA) { return p < NA ? 1 : p < NA + 8 ? 0 : (p % (8 + NA) >= 4 && p % (8 + NA) < 4 + NA); }
constexpr int ring_tile(int p, int NA) { return p < NA ? 0 : p < NA + 8 ? (p - NA) / 4 : ring_isA(p, NA) ? p / (8 + NA) : 2 * (p / (8 + NA)) + (p % (8 + NA) >= 4); }
constexpr int ring_sub(int p, int NA) { return p < NA ? p : p < NA + 8 ? (p - NA) % 4 : p % (8 + NA) < 4 ? p % (8 + NA) : p % (8 + NA) < 4 + NA ? p % (8 + NA) - 4 : p % (8 + NA) - 4 - NA; }
constexpr int ring_cum(int J, int NA) {
    if (J > JL || J >= 16) return ring_total(NA);
    int c = NA + 8 + (8 + NA) * (2 * RING_E0 + RING_F * (J + 1) + (J > RING_J ? (RING_F2 - RING_F) * (J - RING_J) : 0)) / 200;
    if (c > ring_total(NA)) c = ring_total(NA);
    if (c < ring_need(J, NA)) c = ring_need(J, NA);
    while (ring_isA(c - 1, NA) ? 2 * (ring_tile(c - 1, NA) - 3) + 1 > J - 1 : ring_tile(c - 1, NA) - NW > J - 1) --c;
    return c;
}
static_assert(JL >= 12 && JL <= 17, "behind the last barrier (the compute waves have read the operands of step JL + 1) every slot is free");
template <int NA, int P0, int P1>
__device__ __forceinline__ void ring_issue(const unsigned (&ev)[9], const void* xp, const void* wp, int kr, unsigned lw) {
    if constexpr (P0 < P1) {
        constexpr int t = ring_tile(P0, NA), sub = ring_sub(P0, NA);
        if constexpr (ring_isA(P0, NA)) {
            const int c = __builtin_amdgcn_readfirstlane(kr + t >= 10 ? kr + t - 10 : kr + t);
            GLS(ev[4 + sub], (const char*)xp + c * 128, lw, L_A + 2048 + (t % 3) * A_SLOT + sub * 4096);
        } else {
            const int c = __builtin_amdgcn_readfirstlane(2 * kr + t >= 20 ? 2 * kr + t - 20 : 2 * kr + t);
            GLS(ev[sub], (const char*)wp + (size_t)c * (32 * NQKV * 2), lw, L_W + (t % NW) * W_SLOT + sub * 4096);
        }
        ring_issue<NA, P0 + 1, P1>(ev, xp, wp, kr, lw);
    }
}
template <int NA, int J>
__device__ __forceinline__ void ring_run17(const unsigned (&ev)[9], const void* xp, const void* wp, int kr, unsigned lw, unsigned& c0, unsigned& r0) {
    if constexpr (J > 0) ring_issue<NA, (J < 0 ? 0 : ring_cum(J - 1, NA)), ring_cum(J, NA)>(ev, xp, wp, kr, lw);
    if constexpr (J < 16) {
        waitvm<ring_cum(J, NA) - ring_need(J, NA)>();
        BARRIER();
        if constexpr (J == 9) { c0 = (unsigned)__builtin_amdgcn_s_memtime(); r0 = (unsigned)__builtin_amdgcn_s_memrealtime(); }
        ring_run17<NA, J + 1>(ev, xp, wp, kr, lw, c0, r0);
    }
}
constexpr int ring_cum2(int J, int NA) {
    int c = ring_cum(JL, NA) + (J - JL - 1) * RING_T;
    if (c < ring_need(J, NA)) c = ring_need(J, NA);
    if (J == 17 || c > ring_total(NA)) c = ring_total(NA);
    return c;
}
template <int NA, int J>
__device__ __forceinline__ void ring_run(const unsigned (&ev)[9], const void* xp, const void* wp, int kr, unsigned lw, unsigned full, int lane) {
    if constexpr (J <= JL) {
        ring_issue<NA, (J < 0 ? 0 : ring_cum(J - 1, NA)), ring_cum(J, NA)>(ev, xp, wp, kr, lw);
        waitvm<ring_cum(J, NA) - ring_need(J, NA)>();
        BARRIER();
        ring_run<NA, J + 1>(ev, xp, wp, kr, lw, full, lane);
    } else if constexpr (J <= 17) {
        ring_issue<NA, (J == JL + 1 ? ring_cum(JL, NA) : ring_cum2(J - 1, NA)), ring_cum2(J, NA)>(ev, xp, wp, kr, lw);
        waitvm<ring_cum2(J, NA) - ring_need(J, NA)>();
        lds_add(full + (J - 13) * 4, lane);
        ring_run<NA, J + 1>(ev, xp, wp, kr, lw, full, lane);
    }
}

#define CONV_ASM \
    "s_mov_b32 m0, %[kvl]\n" \
    "v_mov_b32 v50, 1\n" \
    "global_load_lds_dwordx4 %[xo], %[wsp] offset:0 sc1\n" \
    "global_load_lds_dwordx4 %[xo], %[wsp] offset:1024 sc1\n" \
    "global_load_lds_dwordx4 %[xo], %[wsp] offset:2048 sc1\n" \
    "global_load_lds_dwordx4 %[xo], %[wsp] offset:3072 sc1\n" \
    "s_mov_b64 exec, 1\n" \
    "ds_add_u32 %[ctl], v50 offset:80\n" \
    "s_mov_b64 exec, -1\n" \
    "v_add_u32 v84, %[sx4], %[xp]\n" \
    "v_add_u32 v51, %[sh4], %[hp]\n" \
    "v_add_u32 v160, 0, %[xp]\n" \
    "v_add_u32 v161, 1152, %[xp]\n" \
    "v_add_u32 v85, 1152, v84\n" \
    "v_add_u32 v162, 2304, %[xp]\n" \
    "v_add_u32 v86, 2304, v84\n" \
    "v_add_u32 v163, 3456, %[xp]\n" \
    "v_add_u32 v87, 3456, v84\n" \
    "ds_read2_b32 v[88:89], v87 offset0:144 offset1:145\n" \
    "ds_read2_b32 v[90:91], v87 offset0:146 offset1:147\n" \
    "ds_read2_b32 v[92:93], v87 offset0:0 offset1:1\n" \
    "ds_read2_b32 v[94:95], v87 offset0:2 offset1:3\n" \
    "ds_read2_b32 v[96:97], v86 offset0:144 offset1:145\n" \
    "ds_read2_b32 v[98:99], v86 offset0:146 offset1:147\n" \
    "ds_read2_b32 v[100:101], v86 offset0:0 offset1:1\n" \
    "ds_read2_b32 v[102:103], v86 offset0:2 offset1:3\n" \
    "ds_read2_b32 v[104:105], v85 offset0:144 offset1:145\n" \
    "ds_read2_b32 v[106:107], v85 offset0:146 offset1:147\n" \
    "ds_read2_b32 v[108:109], v85 offset0:0 offset1:1\n" \
    "ds_read2_b32 v[110:111], v85 offset0:2 offset1:3\n" \
    "ds_read2_b32 v[112:113], v84 offset0:144 offset1:145\n" \
    "ds_read2_b32 v[114:115], v84 offset0:146 offset1:147\n" \
    "ds_read2_b32 v[116:117], v84 offset0:0 offset1:1\n" \
    "ds_read2_b32 v[118:119], v84 offset0:2 offset1:3\n" \
    "s_mov_b32 %[cnt], 0\n" \
    "Lsp_%=:\n" \
    "ds_read_b32 v50, %[ctl] offset:144\n" \
    "s_add_u32 %[cnt], %[cnt], 1\n" \
    "s_waitcnt lgkmcnt(0)\n" \
    "v_readfirstlane_b32 %[tmp], v50\n" \
    "s_cmp_ge_u32 %[cnt], %[cap]\n" \
    "s_cbranch_scc1 Lgo_%=\n" \
    "s_cmp_lt_u32 %[tmp], 4\n" \
    "s_cbranch_scc1 Lsp_%=\n" \
    "Lgo_%=:\n" \
    "ds_read_b128 v[148:151], v51 offset:3584\n" \
    "ds_read_b128 v[144:147], v51 offset:3072\n" \
    "ds_read_b128 v[140:143], v51 offset:2560\n" \
    "ds_read_b128 v[136:139], v51 offset:2048\n" \
    "ds_read_b128 v[132:135], v51 offset:1536\n" \
    "ds_read_b128 v[128:131], v51 offset:1024\n" \
    "ds_read_b128 v[124:127], v51 offset:512\n" \
    "ds_read_b128 v[120:123], v51 offset:0\n" \
    "s_waitcnt lgkmcnt(7)\n" \
    "v_mfma_f32_16x16x32_bf16 v[80:83], v[88:91], v[148:151], 0\n" \
    "ds_read_b128 v[148:151], v51 offset:3648\n" \
    "s_waitcnt lgkmcnt(7)\n" \
    "v_mfma_f32_16x16x32_bf16 v[76:79], v[88:91], v[144:147], 0\n" \
    "ds_read_b128 v[156:159], v51 offset:576\n" \
    "s_waitcnt lgkmcnt(7)\n" \
    "v_mfma_f32_16x16x32_bf16 v[72:75], v[88:91], v[140:143], 0\n" \
    "ds_read_b128 v[152:155], v51 offset:64\n" \
    "s_waitcnt lgkmcnt(7)\n" \
    "v_mfma_f32_16x16x32_bf16 v[68:71], v[88:91], v[136:139], 0\n" \
    "s_waitcnt lgkmcnt(6)\n" \
    "v_mfma_f32_16x16x32_bf16 v[64:67], v[88:91], v[132:135], 0\n" \
    "s_waitcnt lgkmcnt(5)\n" \
    "v_mfma_f32_16x16x32_bf16 v[60:63], v[88:91], v[128:131], 0\n" \
    "s_waitcnt lgkmcnt(4)\n" \
    "v_mfma_f32_16x16x32_bf16 v[56:59], v[88:91], v[124:127], 0\n" \
    "s_waitcnt lgkmcnt(3)\n" \
    "v_mfma_f32_16x16x32_bf16 v[52:55], v[88:91], v[120:123], 0\n" \
    "ds_read2_b32 v[88:89], v87 offset0:160 offset1:161\n" \
    "v_mfma_f32_16x16x32_bf16 v[80:83], v[92:95], v[144:147], v[80:83]\n" \
    "ds_read2_b32 v[90:91], v87 offset0:162 offset1:163\n" \
    "v_mfma_f32_16x16x32_bf16 v[76:79], v[92:95], v[140:143], v[76:79]\n" \
    "ds_read_b128 v[144:147], v51 offset:3136\n" \
    "v_mfma_f32_16x16x32_bf16 v[72:75], v[92:95], v[136:139], v[72:75]\n" \
    "v_mfma_f32_16x16x32_bf16 v[68:71], v[92:95], v[132:135], v[68:71]\n" \
    "v_mfma_f32_16x16x32_bf16 v[64:67], v[92:95], v[128:131], v[64:67]\n" \
    "v_mfma_f32_16x16x32_bf16 v[60:63], v[92:95], v[124:127], v[60:63]\n" \
    "v_mfma_f32_16x16x32_bf16 v[56:59], v[92:95], v[120:123], v[56:59]\n" \
    "ds_read2_b32 v[92:93], v87 offset0:16 offset1:17\n" \
    "v_mfma_f32_16x16x32_bf16 v[80:83], v[96:99], v[140:143], v[80:83]\n" \
    "ds_read_b128 v[140:143], v51 offset:2624\n" \
    "v_mfma_f32_16x16x32_bf16 v[76:79], v[96:99], v[136:139], v[76:79]\n" \
    "ds_read2_b32 v[94:95], v87 offset0:18 offset1:19\n" \
    "v_mfma_f32_16x16x32_bf16 v[72:75], v[96:99], v[132:135], v[72:75]\n" \
    "v_mfma_f32_16x16x32_bf16 v[68:71], v[96:99], v[128:131], v[68:71]\n" \
    "v_mfma_f32_16x16x32_bf16 v[64:67], v[96:99], v[124:127], v[64:67]\n" \
    "v_mfma_f32_16x16x32_bf16 v[60:63], v[96:99], v[120:123], v[60:63]\n" \
    "ds_read2_b32 v[96:97], v86 offset0:160 offset1:161\n" \
    "v_mfma_f32_16x16x32_bf16 v[80:83], v[100:103], v[136:139], v[80:83]\n" \
    "ds_read_b128 v[136:139], v51 offset:2112\n" \
    "v_mfma_f32_16x16x32_bf16 v[76:79], v[100:103], v[132:135], v[76:79]\n" \
    "ds_read2_b32 v[98:99], v86 offset0:162 offset1:163\n" \
    "v_mfma_f32_16x16x32_bf16 v[72:75], v[100:103], v[128:131], v[72:75]\n" \
    "v_mfma_f32_16x16x32_bf16 v[68:71], v[100:103], v[124:127], v[68:71]\n" \
    "v_mfma_f32_16x16x32_bf16 v[64:67], v[100:103], v[120:123], v[64:67]\n" \
    "ds_read2_b32 v[100:101], v86 offset0:16 offset1:17\n" \
    "v_mfma_f32_16x16x32_bf16 v[80:83], v[104:107], v[132:135], v[80:83]\n" \
    "ds_read_b128 v[132:135], v51 offset:1600\n" \
    "v_mfma_f32_16x16x32_bf16 v[76:79], v[104:107], v[128:131], v[76:79]\n" \
    "ds_read2_b32 v[102:103], v86 offset0:18 offset1:19\n" \
    "v_mfma_f32_16x16x32_bf16 v[72:75], v[104:107], v[124:127], v[72:75]\n" \
    "v_mfma_f32_16x16x32_bf16 v[68:71], v[104:107], v[120:123], v[68:71]\n" \
    "ds_read2_b32 v[104:105], v85 offset0:160 offset1:161\n" \
    "v_mfma_f32_16x16x32_bf16 v[80:83], v[108:111], v[128:131], v[80:83]\n" \
    "ds_read_b128 v[128:131], v51 offset:1088\n" \
    "v_mfma_f32_16x16x32_bf16 v[76:79], v[108:111], v[124:127], v[76:79]\n" \
    "ds_read2_b32 v[106:107], v85 offset0:162 offset1:163\n" \
    "v_mfma_f32_16x16x32_bf16 v[72:75], v[108:111], v[120:123], v[72:75]\n" \
    "ds_read2_b32 v[108:109], v85 offset0:16 offset1:17\n" \
    "v_mfma_f32_16x16x32_bf16 v[80:83], v[112:115], v[124:127], v[80:83]\n" \
    "ds_read2_b32 v[110:111], v85 offset0:18 offset1:19\n" \
    "v_mfma_f32_16x16x32_bf16 v[76:79], v[112:115], v[120:123], v[76:79]\n" \
    "ds_read2_b32 v[112:113], v84 offset0:160 offset1:161\n" \
    "v_mfma_f32_16x16x32_bf16 v[80:83], v[116:119], v[120:123], v[80:83]\n" \
    "ds_read2_b32 v[114:115], v84 offset0:162 offset1:163\n" \
    "s_waitcnt lgkmcnt(15)\n" \
    "v_mfma_f32_16x16x32_bf16 v[80:83], v[88:91], v[148:151], v[80:83]\n" \
    "ds_read2_b32 v[116:117], v84 offset0:16 offset1:17\n" \
    "v_mfma_f32_16x16x32_bf16 v[76:79], v[88:91], v[144:147], v[76:79]\n" \
    "ds_read2_b32 v[118:119], v84 offset0:18 offset1:19\n" \
    "s_waitcnt lgkmcnt(15)\n" \
    "v_mfma_f32_16x16x32_bf16 v[72:75], v[88:91], v[140:143], v[72:75]\n" \
    "s_waitcnt vmcnt(0)\n" \
    "ds_read_b128 v[148:151], %[hp] offset:3584\n" \
    "s_waitcnt lgkmcnt(14)\n" \
    "v_mfma_f32_16x16x32_bf16 v[68:71], v[88:91], v[136:139], v[68:71]\n" \
    "ds_read_b128 v[124:127], %[hp] offset:512\n" \
    "s_waitcnt lgkmcnt(12)\n" \
    "v_mfma_f32_16x16x32_bf16 v[64:67], v[88:91], v[132:135], v[64:67]\n" \
    "ds_read_b128 v[120:123], %[hp] offset:0\n" \
    "s_waitcnt lgkmcnt(10)\n" \
    "v_mfma_f32_16x16x32_bf16 v[60:63], v[88:91], v[128:131], v[60:63]\n" \
    "v_mfma_f32_16x16x32_bf16 v[56:59], v[88:91], v[156:159], v[56:59]\n" \
    "v_mfma_f32_16x16x32_bf16 v[52:55], v[88:91], v[152:155], v[52:55]\n" \
    "ds_read2_b32 v[88:89], v163 offset0:144 offset1:145\n" \
    "v_mfma_f32_16x16x32_bf16 v[80:83], v[92:95], v[144:147], v[80:83]\n" \
    "ds_read2_b32 v[90:91], v163 offset0:146 offset1:147\n" \
    "v_mfma_f32_16x16x32_bf16 v[76:79], v[92:95], v[140:143], v[76:79]\n" \
    "ds_read_b128 v[144:147], %[hp] offset:3072\n" \
    "v_mfma_f32_16x16x32_bf16 v[72:75], v[92:95], v[136:139], v[72:75]\n" \
    "v_mfma_f32_16x16x32_bf16 v[68:71], v[92:95], v[132:135], v[68:71]\n" \
    "ds_write_b128 %[scr], v[52:55] offset:0\n" \
    "v_mfma_f32_16x16x32_bf16 v[64:67], v[92:95], v[128:131], v[64:67]\n" \
    "ds_read_b64 v[86:87], %[kvr] offset:0\n" \
    "v_mfma_f32_16x16x32_bf16 v[60:63], v[92:95], v[156:159], v[60:63]\n" \
    "v_mfma_f32_16x16x32_bf16 v[56:59], v[92:95], v[152:155], v[56:59]\n" \
    "ds_read2_b32 v[92:93], v163 offset0:0 offset1:1\n" \
    "v_mfma_f32_16x16x32_bf16 v[80:83], v[96:99], v[140:143], v[80:83]\n" \
    "ds_read_b128 v[140:143], %[hp] offset:2560\n" \
    "v_mfma_f32_16x16x32_bf16 v[76:79], v[96:99], v[136:139], v[76:79]\n" \
    "ds_read2_b32 v[94:95], v163 offset0:2 offset1:3\n" \
    "v_mfma_f32_16x16x32_bf16 v[72:75], v[96:99], v[132:135], v[72:75]\n" \
    "v_mfma_f32_16x16x32_bf16 v[68:71], v[96:99], v[128:131], v[68:71]\n" \
    "ds_write_b128 %[scr], v[56:59] offset:1024\n" \
    "v_mfma_f32_16x16x32_bf16 v[64:67], v[96:99], v[156:159], v[64:67]\n" \
    "v_mfma_f32_16x16x32_bf16 v[60:63], v[96:99], v[152:155], v[60:63]\n" \
    "ds_read2_b32 v[96:97], v162 offset0:144 offset1:145\n" \
    "v_mfma_f32_16x16x32_bf16 v[80:83], v[100:103], v[136:139], v[80:83]\n" \
    "ds_read_b128 v[136:139], %[hp] offset:2048\n" \
    "v_mfma_f32_16x16x32_bf16 v[76:79], v[100:103], v[132:135], v[76:79]\n" \
    "ds_read2_b32 v[98:99], v162 offset0:146 offset1:147\n" \
    "v_mfma_f32_16x16x32_bf16 v[72:75], v[100:103], v[128:131], v[72:75]\n" \
    "v_mfma_f32_16x16x32_bf16 v[68:71], v[100:103], v[156:159], v[68:71]\n" \
    "s_waitcnt lgkmcnt(7)\n" \
    "v_lshlrev_b32 v84, 16, v86\n" \
    "v_mfma_f32_16x16x32_bf16 v[64:67], v[100:103], v[152:155], v[64:67]\n" \
    "ds_read2_b32 v[100:101], v162 offset0:0 offset1:1\n" \
    "v_mfma_f32_16x16x32_bf16 v[80:83], v[104:107], v[132:135], v[80:83]\n" \
    "ds_read_b128 v[132:135], %[hp] offset:1536\n" \
    "v_and_b32 v85, 0xffff0000, v86\n" \
    "v_mfma_f32_16x16x32_bf16 v[76:79], v[104:107], v[128:131], v[76:79]\n" \
    "ds_read2_b32 v[102:103], v162 offset0:2 offset1:3\n" \
    "v_mfma_f32_16x16x32_bf16 v[72:75], v[104:107], v[156:159], v[72:75]\n" \
    "v_lshlrev_b32 v86, 16, v87\n" \
    "v_mfma_f32_16x16x32_bf16 v[68:71], v[104:107], v[152:155], v[68:71]\n" \
    "ds_read2_b32 v[104:105], v161 offset0:144 offset1:145\n" \
    "v_mfma_f32_16x16x32_bf16 v[80:83], v[108:111], v[128:131], v[80:83]\n" \
    "ds_read_b128 v[128:131], %[hp] offset:1024\n" \
    "v_and_b32 v87, 0xffff0000, v87\n" \
    "v_mfma_f32_16x16x32_bf16 v[76:79], v[108:111], v[156:159], v[76:79]\n" \
    "ds_read2_b32 v[106:107], v161 offset0:146 offset1:147\n" \
    "v_mfma_f32_16x16x32_bf16 v[72:75], v[108:111], v[152:155], v[72:75]\n" \
    "ds_read2_b32 v[108:109], v161 offset0:0 offset1:1\n" \
    "v_mfma_f32_16x16x32_bf16 v[80:83], v[112:115], v[156:159], v[80:83]\n" \
    "ds_read2_b32 v[110:111], v161 offset0:2 offset1:3\n" \
    "v_mfma_f32_16x16x32_bf16 v[76:79], v[112:115], v[152:155], v[76:79]\n" \
    "ds_read2_b32 v[112:113], v160 offset0:144 offset1:145\n" \
    "v_mfma_f32_16x16x32_bf16 v[80:83], v[116:119], v[152:155], v[80:83]\n" \
    "ds_read2_b32 v[114:115], v160 offset0:146 offset1:147\n" \
    "v_mfma_f32_16x16x32_bf16 v[44:47], v[88:91], v[148:151], 0\n" \
    "ds_read2_b32 v[116:117], v160 offset0:0 offset1:1\n" \
    "v_mfma_f32_16x16x32_bf16 v[40:43], v[88:91], v[144:147], 0\n" \
    "ds_read2_b32 v[118:119], v160 offset0:2 offset1:3\n" \
    "s_waitcnt lgkmcnt(15)\n" \
    "v_mfma_f32_16x16x32_bf16 v[36:39], v[88:91], v[140:143], 0\n" \
    "ds_read_b128 v[148:151], %[hp] offset:3648\n" \
    "s_waitcnt lgkmcnt(14)\n" \
    "v_mfma_f32_16x16x32_bf16 v[32:35], v[88:91], v[136:139], 0\n" \
    "ds_read_b128 v[156:159], %[hp] offset:576\n" \
    "s_waitcnt lgkmcnt(12)\n" \
    "v_mfma_f32_16x16x32_bf16 v[28:31], v[88:91], v[132:135], 0\n" \
    "ds_read_b128 v[152:155], %[hp] offset:64\n" \
    "s_waitcnt lgkmcnt(10)\n" \
    "v_mfma_f32_16x16x32_bf16 v[24:27], v[88:91], v[128:131], 0\n" \
    "ds_write_b128 %[scr], v[60:63] offset:2048\n" \
    "v_mfma_f32_16x16x32_bf16 v[20:23], v[88:91], v[124:127], 0\n" \
    "v_mfma_f32_16x16x32_bf16 v[16:19], v[88:91], v[120:123], 0\n" \
    "ds_read2_b32 v[88:89], v163 offset0:160 offset1:161\n" \
    "v_mfma_f32_16x16x32_bf16 v[44:47], v[92:95], v[144:147], v[44:47]\n" \
    "ds_read2_b32 v[90:91], v163 offset0:162 offset1:163\n" \
    "v_mfma_f32_16x16x32_bf16 v[40:43], v[92:95], v[140:143], v[40:43]\n" \
    "ds_read_b128 v[144:147], %[hp] offset:3136\n" \
    "v_mfma_f32_16x16x32_bf16 v[36:39], v[92:95], v[136:139], v[36:39]\n" \
    "v_fmac_f32 v16, %[dd], v84\n" \
    "v_fmac_f32 v17, %[dd], v85\n" \
    "v_mfma_f32_16x16x32_bf16 v[32:35], v[92:95], v[132:135], v[32:35]\n" \
    "v_fmac_f32 v18, %[dd], v86\n" \
    "v_fmac_f32 v19, %[dd], v87\n" \
    "v_mfma_f32_16x16x32_bf16 v[28:31], v[92:95], v[128:131], v[28:31]\n" \
    "ds_read_b64 v[86:87], %[kvr] offset:512\n" \
    "v_mfma_f32_16x16x32_bf16 v[24:27], v[92:95], v[124:127], v[24:27]\n" \
    "ds_write_b128 %[scr], v[64:67] offset:3072\n" \
    "v_mfma_f32_16x16x32_bf16 v[20:23], v[92:95], v[120:123], v[20:23]\n" \
    "ds_read2_b32 v[92:93], v163 offset0:16 offset1:17\n" \
    "v_mfma_f32_16x16x32_bf16 v[44:47], v[96:99], v[140:143], v[44:47]\n" \
    "ds_read_b128 v[140:143], %[hp] offset:2624\n" \
    "v_mfma_f32_16x16x32_bf16 v[40:43], v[96:99], v[136:139], v[40:43]\n" \
    "ds_read2_b32 v[94:95], v163 offset0:18 offset1:19\n" \
    "v_mfma_f32_16x16x32_bf16 v[36:39], v[96:99], v[132:135], v[36:39]\n" \
    "ds_write_b128 %[scr], v[68:71] offset:4096\n" \
    "v_mfma_f32_16x16x32_bf16 v[32:35], v[96:99], v[128:131], v[32:35]\n" \
    "v_mfma_f32_16x16x32_bf16 v[28:31], v[96:99], v[124:127], v[28:31]\n" \
    "v_mfma_f32_16x16x32_bf16 v[24:27], v[96:99], v[120:123], v[24:27]\n" \
    "ds_read2_b32 v[96:97], v162 offset0:160 offset1:161\n" \
    "v_mfma_f32_16x16x32_bf16 v[44:47], v[100:103], v[136:139], v[44:47]\n" \
    "ds_read_b128 v[136:139], %[hp] offset:2112\n" \
    "v_mfma_f32_16x16x32_bf16 v[40:43], v[100:103], v[132:135], v[40:43]\n" \
    "ds_read2_b32 v[98:99], v162 offset0:162 offset1:163\n" \
    "v_mfma_f32_16x16x32_bf16 v[36:39], v[100:103], v[128:131], v[36:39]\n" \
    "ds_write_b128 %[scr], v[72:75] offset:5120\n" \
    "v_mfma_f32_16x16x32_bf16 v[32:35], v[100:103], v[124:127], v[32:35]\n" \
    "s_waitcnt lgkmcnt(9)\n" \
    "v_lshlrev_b32 v84, 16, v86\n" \
    "v_mfma_f32_16x16x32_bf16 v[28:31], v[100:103], v[120:123], v[28:31]\n" \
    "ds_read2_b32 v[100:101], v162 offset0:16 offset1:17\n" \
    "v_mfma_f32_16x16x32_bf16 v[44:47], v[104:107], v[132:135], v[44:47]\n" \
    "ds_read_b128 v[132:135], %[hp] offset:1600\n" \
    "v_and_b32 v85, 0xffff0000, v86\n" \
    "v_mfma_f32_16x16x32_bf16 v[40:43], v[104:107], v[128:131], v[40:43]\n" \
    "ds_read2_b32 v[102:103], v162 offset0:18 offset1:19\n" \
    "v_mfma_f32_16x16x32_bf16 v[36:39], v[104:107], v[124:127], v[36:39]\n" \
    "v_lshlrev_b32 v86, 16, v87\n" \
    "v_mfma_f32_16x16x32_bf16 v[32:35], v[104:107], v[120:123], v[32:35]\n" \
    "ds_read2_b32 v[104:105], v161 offset0:160 offset1:161\n" \
    "v_mfma_f32_16x16x32_bf16 v[44:47], v[108:111], v[128:131], v[44:47]\n" \
    "ds_read_b128 v[128:131], %[hp] offset:1088\n" \
    "v_and_b32 v87, 0xffff0000, v87\n" \
    "v_mfma_f32_16x16x32_bf16 v[40:43], v[108:111], v[124:127], v[40:43]\n" \
    "ds_read2_b32 v[106:107], v161 offset0:162 offset1:163\n" \
    "v_mfma_f32_16x16x32_bf16 v[36:39], v[108:111], v[120:123], v[36:39]\n" \
    "ds_read2_b32 v[108:109], v161 offset0:16 offset1:17\n" \
    "v_mfma_f32_16x16x32_bf16 v[44:47], v[112:115], v[124:127], v[44:47]\n" \
    "ds_read2_b32 v[110:111], v161 offset0:18 offset1:19\n" \
    "v_mfma_f32_16x16x32_bf16 v[40:43], v[112:115], v[120:123], v[40:43]\n" \
    "ds_read2_b32 v[112:113], v160 offset0:160 offset1:161\n" \
    "v_mfma_f32_16x16x32_bf16 v[44:47], v[116:119], v[120:123], v[44:47]\n" \
    "ds_read2_b32 v[114:115], v160 offset0:162 offset1:163\n" \
    "v_mfma_f32_16x16x32_bf16 v[44:47], v[88:91], v[148:151], v[44:47]\n" \
    "ds_read2_b32 v[116:117], v160 offset0:16 offset1:17\n" \
    "v_mfma_f32_16x16x32_bf16 v[40:43], v[88:91], v[144:147], v[40:43]\n" \
    "ds_read2_b32 v[118:119], v160 offset0:18 offset1:19\n" \
    "s_waitcnt lgkmcnt(15)\n" \
    "v_mfma_f32_16x16x32_bf16 v[36:39], v[88:91], v[140:143], v[36:39]\n" \
    "ds_read_b128 v[148:151], %[hp] offset:3712\n" \
    "s_waitcnt lgkmcnt(15)\n" \
    "v_mfma_f32_16x16x32_bf16 v[32:35], v[88:91], v[136:139], v[32:35]\n" \
    "ds_read_b128 v[124:127], %[hp] offset:640\n" \
    "s_waitcnt lgkmcnt(12)\n" \
    "v_mfma_f32_16x16x32_bf16 v[28:31], v[88:91], v[132:135], v[28:31]\n" \
    "ds_read_b128 v[120:123], %[hp] offset:128\n" \
    "s_waitcnt lgkmcnt(10)\n" \
    "v_mfma_f32_16x16x32_bf16 v[24:27], v[88:91], v[128:131], v[24:27]\n" \
    "ds_write_b128 %[scr], v[76:79] offset:6144\n" \
    "v_mfma_f32_16x16x32_bf16 v[20:23], v[88:91], v[156:159], v[20:23]\n" \
    "v_mfma_f32_16x16x32_bf16 v[16:19], v[88:91], v[152:155], v[16:19]\n" \
    "ds_read2_b32 v[88:89], v163 offset0:176 offset1:177\n" \
    "v_mfma_f32_16x16x32_bf16 v[44:47], v[92:95], v[144:147], v[44:47]\n" \
    "ds_read2_b32 v[90:91], v163 offset0:178 offset1:179\n" \
    "v_mfma_f32_16x16x32_bf16 v[40:43], v[92:95], v[140:143], v[40:43]\n" \
    "ds_read_b128 v[144:147], %[hp] offset:3200\n" \
    "v_fmac_f32 v20, %[dd], v84\n" \
    "v_fmac_f32 v21, %[dd], v85\n" \
    "v_mfma_f32_16x16x32_bf16 v[36:39], v[92:95], v[136:139], v[36:39]\n" \
    "v_fmac_f32 v22, %[dd], v86\n" \
    "v_fmac_f32 v23, %[dd], v87\n" \
    "v_mfma_f32_16x16x32_bf16 v[32:35], v[92:95], v[132:135], v[32:35]\n" \
    "ds_write_b128 %[scr], v[80:83] offset:7168\n" \
    "v_mov_b32 v50, 1\n" \
    "s_mov_b64 exec, 1\n" \
    "ds_add_u32 %[ctl], v50 offset:84\n" \
    "s_mov_b64 exec, -1\n" \
    "v_mfma_f32_16x16x32_bf16 v[28:31], v[92:95], v[128:131], v[28:31]\n" \
    "ds_read_b64 v[86:87], %[kvr] offset:1024\n" \
    "v_mfma_f32_16x16x32_bf16 v[24:27], v[92:95], v[156:159], v[24:27]\n" \
    "v_mfma_f32_16x16x32_bf16 v[20:23], v[92:95], v[152:155], v[20:23]\n" \
    "ds_read2_b32 v[92:93], v163 offset0:32 offset1:33\n" \
    "v_mfma_f32_16x16x32_bf16 v[44:47], v[96:99], v[140:143], v[44:47]\n" \
    "ds_read_b128 v[140:143], %[hp] offset:2688\n" \
    "v_mfma_f32_16x16x32_bf16 v[40:43], v[96:99], v[136:139], v[40:43]\n" \
    "ds_read2_b32 v[94:95], v163 offset0:34 offset1:35\n" \
    "v_mfma_f32_16x16x32_bf16 v[36:39], v[96:99], v[132:135], v[36:39]\n" \
    "v_mfma_f32_16x16x32_bf16 v[32:35], v[96:99], v[128:131], v[32:35]\n" \
    "v_mfma_f32_16x16x32_bf16 v[28:31], v[96:99], v[156:159], v[28:31]\n" \
    "v_mfma_f32_16x16x32_bf16 v[24:27], v[96:99], v[152:155], v[24:27]\n" \
    "ds_read2_b32 v[96:97], v162 offset0:176 offset1:177\n" \
    "v_mfma_f32_16x16x32_bf16 v[44:47], v[100:103], v[136:139], v[44:47]\n" \
    "ds_read_b128 v[136:139], %[hp] offset:2176\n" \
    "v_mfma_f32_16x16x32_bf16 v[40:43], v[100:103], v[132:135], v[40:43]\n" \
    "ds_read2_b32 v[98:99], v162 offset0:178 offset1:179\n" \
    "v_mfma_f32_16x16x32_bf16 v[36:39], v[100:103], v[128:131], v[36:39]\n" \
    "v_mfma_f32_16x16x32_bf16 v[32:35], v[100:103], v[156:159], v[32:35]\n" \
    "s_waitcnt lgkmcnt(6)\n" \
    "v_lshlrev_b32 v84, 16, v86\n" \
    "v_mfma_f32_16x16x32_bf16 v[28:31], v[100:103], v[152:155], v[28:31]\n" \
    "ds_read2_b32 v[100:101], v162 offset0:32 offset1:33\n" \
    "v_mfma_f32_16x16x32_bf16 v[44:47], v[104:107], v[132:135], v[44:47]\n" \
    "ds_read_b128 v[132:135], %[hp] offset:1664\n" \
    "v_and_b32 v85, 0xffff0000, v86\n" \
    "v_mfma_f32_16x16x32_bf16 v[40:43], v[104:107], v[128:131], v[40:43]\n" \
    "ds_read2_b32 v[102:103], v162 offset0:34 offset1:35\n" \
    "v_mfma_f32_16x16x32_bf16 v[36:39], v[104:107], v[156:159], v[36:39]\n" \
    "v_lshlrev_b32 v86, 16, v87\n" \
    "v_mfma_f32_16x16x32_bf16 v[32:35], v[104:107], v[152:155], v[32:35]\n" \
    "ds_read2_b32 v[104:105], v161 offset0:176 offset1:177\n" \
    "v_mfma_f32_16x16x32_bf16 v[44:47], v[108:111], v[128:131], v[44:47]\n" \
    "ds_read_b128 v[128:131], %[hp] offset:1152\n" \
    "v_and_b32 v87, 0xffff0000, v87\n" \
    "v_mfma_f32_16x16x32_bf16 v[40:43], v[108:111], v[156:159], v[40:43]\n" \
    "ds_read2_b32 v[106:107], v161 offset0:178 offset1:179\n" \
    "v_mfma_f32_16x16x32_bf16 v[36:39], v[108:111], v[152:155], v[36:39]\n" \
    "ds_read2_b32 v[108:109], v161 offset0:32 offset1:33\n" \
    "v_mfma_f32_16x16x32_bf16 v[44:47], v[112:115], v[156:159], v[44:47]\n" \
    "ds_read2_b32 v[110:111], v161 offset0:34 offset1:35\n" \
    "v_mfma_f32_16x16x32_bf16 v[40:43], v[112:115], v[152:155], v[40:43]\n" \
    "ds_read2_b32 v[112:113], v160 offset0:176 offset1:177\n" \
    "v_mfma_f32_16x16x32_bf16 v[44:47], v[116:119], v[152:155], v[44:47]\n" \
    "ds_read2_b32 v[114:115], v160 offset0:178 offset1:179\n" \
    "v_mfma_f32_16x16x32_bf16 v[44:47], v[88:91], v[148:151], v[44:47]\n" \
    "ds_read2_b32 v[116:117], v160 offset0:32 offset1:33\n" \
    "v_mfma_f32_16x16x32_bf16 v[40:43], v[88:91], v[144:147], v[40:43]\n" \
    "ds_read2_b32 v[118:119], v160 offset0:34 offset1:35\n" \
    "s_waitcnt lgkmcnt(15)\n" \
    "v_mfma_f32_16x16x32_bf16 v[36:39], v[88:91], v[140:143], v[36:39]\n" \
    "ds_read_b128 v[148:151], %[hp] offset:3776\n" \
    "s_waitcnt lgkmcnt(14)\n" \
    "v_mfma_f32_16x16x32_bf16 v[32:35], v[88:91], v[136:139], v[32:35]\n" \
    "ds_read_b128 v[156:159], %[hp] offset:704\n" \
    "s_waitcnt lgkmcnt(12)\n" \
    "v_mfma_f32_16x16x32_bf16 v[28:31], v[88:91], v[132:135], v[28:31]\n" \
    "ds_read_b128 v[152:155], %[hp] offset:192\n" \
    "s_waitcnt lgkmcnt(10)\n" \
    "v_mfma_f32_16x16x32_bf16 v[24:27], v[88:91], v[128:131], v[24:27]\n" \
    "v_mfma_f32_16x16x32_bf16 v[20:23], v[88:91], v[124:127], v[20:23]\n" \
    "v_mfma_f32_16x16x32_bf16 v[16:19], v[88:91], v[120:123], v[16:19]\n" \
    "ds_read2_b32 v[88:89], v163 offset0:192 offset1:193\n" \
    "v_mfma_f32_16x16x32_bf16 v[44:47], v[92:95], v[144:147], v[44:47]\n" \
    "ds_read2_b32 v[90:91], v163 offset0:194 offset1:195\n" \
    "v_fmac_f32 v24, %[dd], v84\n" \
    "v_fmac_f32 v25, %[dd], v85\n" \
    "v_mfma_f32_16x16x32_bf16 v[40:43], v[92:95], v[140:143], v[40:43]\n" \
    "ds_read_b128 v[144:147], %[hp] offset:3264\n" \
    "v_fmac_f32 v26, %[dd], v86\n" \
    "v_fmac_f32 v27, %[dd], v87\n" \
    "v_mfma_f32_16x16x32_bf16 v[36:39], v[92:95], v[136:139], v[36:39]\n" \
    "v_mfma_f32_16x16x32_bf16 v[32:35], v[92:95], v[132:135], v[32:35]\n" \
    "v_mfma_f32_16x16x32_bf16 v[28:31], v[92:95], v[128:131], v[28:31]\n" \
    "ds_read_b64 v[86:87], %[kvr] offset:1536\n" \
    "v_mfma_f32_16x16x32_bf16 v[24:27], v[92:95], v[124:127], v[24:27]\n" \
    "v_mfma_f32_16x16x32_bf16 v[20:23], v[92:95], v[120:123], v[20:23]\n" \
    "ds_read2_b32 v[92:93], v163 offset0:48 offset1:49\n" \
    "v_mfma_f32_16x16x32_bf16 v[44:47], v[96:99], v[140:143], v[44:47]\n" \
    "ds_read_b128 v[140:143], %[hp] offset:2752\n" \
    "v_mfma_f32_16x16x32_bf16 v[40:43], v[96:99], v[136:139], v[40:43]\n" \
    "ds_read2_b32 v[94:95], v163 offset0:50 offset1:51\n" \
    "v_mfma_f32_16x16x32_bf16 v[36:39], v[96:99], v[132:135], v[36:39]\n" \
    "v_mfma_f32_16x16x32_bf16 v[32:35], v[96:99], v[128:131], v[32:35]\n" \
    "v_mfma_f32_16x16x32_bf16 v[28:31], v[96:99], v[124:127], v[28:31]\n" \
    "v_mfma_f32_16x16x32_bf16 v[24:27], v[96:99], v[120:123], v[24:27]\n" \
    "ds_read2_b32 v[96:97], v162 offset0:192 offset1:193\n" \
    "v_mfma_f32_16x16x32_bf16 v[44:47], v[100:103], v[136:139], v[44:47]\n" \
    "ds_read_b128 v[136:139], %[hp] offset:2240\n" \
    "v_mfma_f32_16x16x32_bf16 v[40:43], v[100:103], v[132:135], v[40:43]\n" \
    "ds_read2_b32 v[98:99], v162 offset0:194 offset1:195\n" \
    "v_mfma_f32_16x16x32_bf16 v[36:39], v[100:103], v[128:131], v[36:39]\n" \
    "v_mfma_f32_16x16x32_bf16 v[32:35], v[100:103], v[124:127], v[32:35]\n" \
    "s_waitcnt lgkmcnt(6)\n" \
    "v_lshlrev_b32 v84, 16, v86\n" \
    "v_mfma_f32_16x16x32_bf16 v[28:31], v[100:103], v[120:123], v[28:31]\n" \
    "ds_read2_b32 v[100:101], v162 offset0:48 offset1:49\n" \
    "v_mfma_f32_16x16x32_bf16 v[44:47], v[104:107], v[132:135], v[44:47]\n" \
    "ds_read_b128 v[132:135], %[hp] offset:1728\n" \
    "v_and_b32 v85, 0xffff0000, v86\n" \
    "v_mfma_f32_16x16x32_bf16 v[40:43], v[104:107], v[128:131], v[40:43]\n" \
    "ds_read2_b32 v[102:103], v162 offset0:50 offset1:51\n" \
    "v_mfma_f32_16x16x32_bf16 v[36:39], v[104:107], v[124:127], v[36:39]\n" \
    "v_lshlrev_b32 v86, 16, v87\n" \
    "v_mfma_f32_16x16x32_bf16 v[32:35], v[104:107], v[120:123], v[32:35]\n" \
    "ds_read2_b32 v[104:105], v161 offset0:192 offset1:193\n" \
    "v_mfma_f32_16x16x32_bf16 v[44:47], v[108:111], v[128:131], v[44:47]\n" \
    "ds_read_b128 v[128:131], %[hp] offset:1216\n" \
    "v_and_b32 v87, 0xffff0000, v87\n" \
    "v_mfma_f32_16x16x32_bf16 v[40:43], v[108:111], v[124:127], v[40:43]\n" \
    "ds_read2_b32 v[106:107], v161 offset0:194 offset1:195\n" \
    "v_mfma_f32_16x16x32_bf16 v[36:39], v[108:111], v[120:123], v[36:39]\n" \
    "ds_read2_b32 v[108:109], v161 offset0:48 offset1:49\n" \
    "v_mfma_f32_16x16x32_bf16 v[44:47], v[112:115], v[124:127], v[44:47]\n" \
    "ds_read2_b32 v[110:111], v161 offset0:50 offset1:51\n" \
    "v_mfma_f32_16x16x32_bf16 v[40:43], v[112:115], v[120:123], v[40:43]\n" \
    "ds_read2_b32 v[112:113], v160 offset0:192 offset1:193\n" \
    "v_mfma_f32_16x16x32_bf16 v[44:47], v[116:119], v[120:123], v[44:47]\n" \
    "ds_read2_b32 v[114:115], v160 offset0:194 offset1:195\n" \
    "v_mfma_f32_16x16x32_bf16 v[44:47], v[88:91], v[148:151], v[44:47]\n" \
    "ds_read2_b32 v[116:117], v160 offset0:48 offset1:49\n" \
    "v_mfma_f32_16x16x32_bf16 v[40:43], v[88:91], v[144:147], v[40:43]\n" \
    "ds_read2_b32 v[118:119], v160 offset0:50 offset1:51\n" \
    "s_waitcnt lgkmcnt(15)\n" \
    "v_mfma_f32_16x16x32_bf16 v[36:39], v[88:91], v[140:143], v[36:39]\n" \
    "ds_read_b128 v[148:151], %[hp] offset:3840\n" \
    "s_waitcnt lgkmcnt(14)\n" \
    "v_mfma_f32_16x16x32_bf16 v[32:35], v[88:91], v[136:139], v[32:35]\n" \
    "ds_read_b128 v[124:127], %[hp] offset:768\n" \
    "s_waitcnt lgkmcnt(12)\n" \
    "v_mfma_f32_16x16x32_bf16 v[28:31], v[88:91], v[132:135], v[28:31]\n" \
    "ds_read_b128 v[120:123], %[hp] offset:256\n" \
    "s_waitcnt lgkmcnt(10)\n" \
    "v_mfma_f32_16x16x32_bf16 v[24:27], v[88:91], v[128:131], v[24:27]\n" \
    "v_mfma_f32_16x16x32_bf16 v[20:23], v[88:91], v[156:159], v[20:23]\n" \
    "v_mfma_f32_16x16x32_bf16 v[16:19], v[88:91], v[152:155], v[16:19]\n" \
    "ds_read2_b32 v[88:89], v163 offset0:216 offset1:217\n" \
    "v_fmac_f32 v28, %[dd], v84\n" \
    "v_fmac_f32 v29, %[dd], v85\n" \
    "v_mfma_f32_16x16x32_bf16 v[44:47], v[92:95], v[144:147], v[44:47]\n" \
    "ds_read2_b32 v[90:91], v163 offset0:218 offset1:219\n" \
    "v_fmac_f32 v30, %[dd], v86\n" \
    "v_fmac_f32 v31, %[dd], v87\n" \
    "v_mfma_f32_16x16x32_bf16 v[40:43], v[92:95], v[140:143], v[40:43]\n" \
    "ds_read_b128 v[144:147], %[hp] offset:3328\n" \
    "v_mfma_f32_16x16x32_bf16 v[36:39], v[92:95], v[136:139], v[36:39]\n" \
    "v_mfma_f32_16x16x32_bf16 v[32:35], v[92:95], v[132:135], v[32:35]\n" \
    "v_mfma_f32_16x16x32_bf16 v[28:31], v[92:95], v[128:131], v[28:31]\n" \
    "ds_read_b64 v[86:87], %[kvr] offset:2048\n" \
    "v_mfma_f32_16x16x32_bf16 v[24:27], v[92:95], v[156:159], v[24:27]\n" \
    "v_mfma_f32_16x16x32_bf16 v[20:23], v[92:95], v[152:155], v[20:23]\n" \
    "ds_read2_b32 v[92:93], v163 offset0:72 offset1:73\n" \
    "v_mfma_f32_16x16x32_bf16 v[44:47], v[96:99], v[140:143], v[44:47]\n" \
    "ds_read_b128 v[140:143], %[hp] offset:2816\n" \
    "v_mfma_f32_16x16x32_bf16 v[40:43], v[96:99], v[136:139], v[40:43]\n" \
    "ds_read2_b32 v[94:95], v163 offset0:74 offset1:75\n" \
    "v_mfma_f32_16x16x32_bf16 v[36:39], v[96:99], v[132:135], v[36:39]\n" \
    "v_mfma_f32_16x16x32_bf16 v[32:35], v[96:99], v[128:131], v[32:35]\n" \
    "v_mfma_f32_16x16x32_bf16 v[28:31], v[96:99], v[156:159], v[28:31]\n" \
    "v_mfma_f32_16x16x32_bf16 v[24:27], v[96:99], v[152:155], v[24:27]\n" \
    "ds_read2_b32 v[96:97], v162 offset0:216 offset1:217\n" \
    "v_mfma_f32_16x16x32_bf16 v[44:47], v[100:103], v[136:139], v[44:47]\n" \
    "ds_read_b128 v[136:139], %[hp] offset:2304\n" \
    "v_mfma_f32_16x16x32_bf16 v[40:43], v[100:103], v[132:135], v[40:43]\n" \
    "ds_read2_b32 v[98:99], v162 offset0:218 offset1:219\n" \
    "v_mfma_f32_16x16x32_bf16 v[36:39], v[100:103], v[128:131], v[36:39]\n" \
    "v_mfma_f32_16x16x32_bf16 v[32:35], v[100:103], v[156:159], v[32:35]\n" \
    "s_waitcnt lgkmcnt(6)\n" \
    "v_lshlrev_b32 v84, 16, v86\n" \
    "v_mfma_f32_16x16x32_bf16 v[28:31], v[100:103], v[152:155], v[28:31]\n" \
    "ds_read2_b32 v[100:101], v162 offset0:72 offset1:73\n" \
    "v_mfma_f32_16x16x32_bf16 v[44:47], v[104:107], v[132:135], v[44:47]\n" \
    "ds_read_b128 v[132:135], %[hp] offset:1792\n" \
    "v_and_b32 v85, 0xffff0000, v86\n" \
    "v_mfma_f32_16x16x32_bf16 v[40:43], v[104:107], v[128:131], v[40:43]\n" \
    "ds_read2_b32 v[102:103], v162 offset0:74 offset1:75\n" \
    "v_mfma_f32_16x16x32_bf16 v[36:39], v[104:107], v[156:159], v[36:39]\n" \
    "v_lshlrev_b32 v86, 16, v87\n" \
    "v_mfma_f32_16x16x32_bf16 v[32:35], v[104:107], v[152:155], v[32:35]\n" \
    "ds_read2_b32 v[104:105], v161 offset0:216 offset1:217\n" \
    "v_mfma_f32_16x16x32_bf16 v[44:47], v[108:111], v[128:131], v[44:47]\n" \
    "ds_read_b128 v[128:131], %[hp] offset:1280\n" \
    "v_and_b32 v87, 0xffff0000, v87\n" \
    "v_mfma_f32_16x16x32_bf16 v[40:43], v[108:111], v[156:159], v[40:43]\n" \
    "ds_read2_b32 v[106:107], v161 offset0:218 offset1:219\n" \
    "v_mfma_f32_16x16x32_bf16 v[36:39], v[108:111], v[152:155], v[36:39]\n" \
    "ds_read2_b32 v[108:109], v161 offset0:72 offset1:73\n" \
    "v_mfma_f32_16x16x32_bf16 v[44:47], v[112:115], v[156:159], v[44:47]\n" \
    "ds_read2_b32 v[110:111], v161 offset0:74 offset1:75\n" \
    "v_mfma_f32_16x16x32_bf16 v[40:43], v[112:115], v[152:155], v[40:43]\n" \
    "ds_read2_b32 v[112:113], v160 offset0:216 offset1:217\n" \
    "v_mfma_f32_16x16x32_bf16 v[44:47], v[116:119], v[152:155], v[44:47]\n" \
    "ds_read2_b32 v[114:115], v160 offset0:218 offset1:219\n" \
    "v_add_u32 v160, 288, v160\n" \
    "v_add_u32 v161, 288, v161\n" \
    "v_add_u32 v162, 288, v162\n" \
    "v_add_u32 v163, 288, v163\n" \
    "v_mfma_f32_16x16x32_bf16 v[44:47], v[88:91], v[148:151], v[44:47]\n" \
    "ds_read2_b32 v[116:117], v160 offset0:0 offset1:1\n" \
    "v_mfma_f32_16x16x32_bf16 v[40:43], v[88:91], v[144:147], v[40:43]\n" \
    "ds_read2_b32 v[118:119], v160 offset0:2 offset1:3\n" \
    "s_waitcnt lgkmcnt(15)\n" \
    "v_mfma_f32_16x16x32_bf16 v[36:39], v[88:91], v[140:143], v[36:39]\n" \
    "ds_read_b128 v[148:151], %[hp] offset:3904\n" \
    "s_waitcnt lgkmcnt(14)\n" \
    "v_mfma_f32_16x16x32_bf16 v[32:35], v[88:91], v[136:139], v[32:35]\n" \
    "ds_read_b128 v[156:159], %[hp] offset:832\n" \
    "s_waitcnt lgkmcnt(12)\n" \
    "v_mfma_f32_16x16x32_bf16 v[28:31], v[88:91], v[132:135], v[28:31]\n" \
    "ds_read_b128 v[152:155], %[hp] offset:320\n" \
    "s_waitcnt lgkmcnt(10)\n" \
    "v_mfma_f32_16x16x32_bf16 v[24:27], v[88:91], v[128:131], v[24:27]\n" \
    "v_mfma_f32_16x16x32_bf16 v[20:23], v[88:91], v[124:127], v[20:23]\n" \
    "v_fmac_f32 v32, %[dd], v84\n" \
    "v_fmac_f32 v33, %[dd], v85\n" \
    "v_mfma_f32_16x16x32_bf16 v[16:19], v[88:91], v[120:123], v[16:19]\n" \
    "ds_read2_b32 v[88:89], v163 offset0:160 offset1:161\n" \
    "v_fmac_f32 v34, %[dd], v86\n" \
    "v_fmac_f32 v35, %[dd], v87\n" \
    "v_mfma_f32_16x16x32_bf16 v[44:47], v[92:95], v[144:147], v[44:47]\n" \
    "ds_read2_b32 v[90:91], v163 offset0:162 offset1:163\n" \
    "v_mfma_f32_16x16x32_bf16 v[40:43], v[92:95], v[140:143], v[40:43]\n" \
    "ds_read_b128 v[144:147], %[hp] offset:3392\n" \
    "v_mfma_f32_16x16x32_bf16 v[36:39], v[92:95], v[136:139], v[36:39]\n" \
    "v_mfma_f32_16x16x32_bf16 v[32:35], v[92:95], v[132:135], v[32:35]\n" \
    "v_mfma_f32_16x16x32_bf16 v[28:31], v[92:95], v[128:131], v[28:31]\n" \
    "ds_read_b64 v[86:87], %[kvr] offset:2560\n" \
    "v_mfma_f32_16x16x32_bf16 v[24:27], v[92:95], v[124:127], v[24:27]\n" \
    "v_mfma_f32_16x16x32_bf16 v[20:23], v[92:95], v[120:123], v[20:23]\n" \
    "ds_read2_b32 v[92:93], v163 offset0:16 offset1:17\n" \
    "v_mfma_f32_16x16x32_bf16 v[44:47], v[96:99], v[140:143], v[44:47]\n" \
    "ds_read_b128 v[140:143], %[hp] offset:2880\n" \
    "v_mfma_f32_16x16x32_bf16 v[40:43], v[96:99], v[136:139], v[40:43]\n" \
    "ds_read2_b32 v[94:95], v163 offset0:18 offset1:19\n" \
    "v_mfma_f32_16x16x32_bf16 v[36:39], v[96:99], v[132:135], v[36:39]\n" \
    "v_mfma_f32_16x16x32_bf16 v[32:35], v[96:99], v[128:131], v[32:35]\n" \
    "v_mfma_f32_16x16x32_bf16 v[28:31], v[96:99], v[124:127], v[28:31]\n" \
    "v_mfma_f32_16x16x32_bf16 v[24:27], v[96:99], v[120:123], v[24:27]\n" \
    "ds_read2_b32 v[96:97], v162 offset0:160 offset1:161\n" \
    "v_mfma_f32_16x16x32_bf16 v[44:47], v[100:103], v[136:139], v[44:47]\n" \
    "ds_read_b128 v[136:139], %[hp] offset:2368\n" \
    "v_mfma_f32_16x16x32_bf16 v[40:43], v[100:103], v[132:135], v[40:43]\n" \
    "ds_read2_b32 v[98:99], v162 offset0:162 offset1:163\n" \
    "v_mfma_f32_16x16x32_bf16 v[36:39], v[100:103], v[128:131], v[36:39]\n" \
    "v_mfma_f32_16x16x32_bf16 v[32:35], v[100:103], v[124:127], v[32:35]\n" \
    "s_waitcnt lgkmcnt(6)\n" \
    "v_lshlrev_b32 v84, 16, v86\n" \
    "v_mfma_f32_16x16x32_bf16 v[28:31], v[100:103], v[120:123], v[28:31]\n" \
    "ds_read2_b32 v[100:101], v162 offset0:16 offset1:17\n" \
    "v_mfma_f32_16x16x32_bf16 v[44:47], v[104:107], v[132:135], v[44:47]\n" \
    "ds_read_b128 v[132:135], %[hp] offset:1856\n" \
    "v_and_b32 v85, 0xffff0000, v86\n" \
    "v_mfma_f32_16x16x32_bf16 v[40:43], v[104:107], v[128:131], v[40:43]\n" \
    "ds_read2_b32 v[102:103], v162 offset0:18 offset1:19\n" \
    "v_mfma_f32_16x16x32_bf16 v[36:39], v[104:107], v[124:127], v[36:39]\n" \
    "v_lshlrev_b32 v86, 16, v87\n" \
    "v_mfma_f32_16x16x32_bf16 v[32:35], v[104:107], v[120:123], v[32:35]\n" \
    "ds_read2_b32 v[104:105], v161 offset0:160 offset1:161\n" \
    "v_mfma_f32_16x16x32_bf16 v[44:47], v[108:111], v[128:131], v[44:47]\n" \
    "ds_read_b128 v[128:131], %[hp] offset:1344\n" \
    "v_and_b32 v87, 0xffff0000, v87\n" \
    "v_mfma_f32_16x16x32_bf16 v[40:43], v[108:111], v[124:127], v[40:43]\n" \
    "ds_read2_b32 v[106:107], v161 offset0:162 offset1:163\n" \
    "v_mfma_f32_16x16x32_bf16 v[36:39], v[108:111], v[120:123], v[36:39]\n" \
    "ds_read2_b32 v[108:109], v161 offset0:16 offset1:17\n" \
    "v_mfma_f32_16x16x32_bf16 v[44:47], v[112:115], v[124:127], v[44:47]\n" \
    "ds_read2_b32 v[110:111], v161 offset0:18 offset1:19\n" \
    "v_mfma_f32_16x16x32_bf16 v[40:43], v[112:115], v[120:123], v[40:43]\n" \
    "ds_read2_b32 v[112:113], v160 offset0:160 offset1:161\n" \
    "v_mfma_f32_16x16x32_bf16 v[44:47], v[116:119], v[120:123], v[44:47]\n" \
    "ds_read2_b32 v[114:115], v160 offset0:162 offset1:163\n" \
    "v_mfma_f32_16x16x32_bf16 v[44:47], v[88:91], v[148:151], v[44:47]\n" \
    "ds_read2_b32 v[116:117], v160 offset0:16 offset1:17\n" \
    "v_mfma_f32_16x16x32_bf16 v[40:43], v[88:91], v[144:147], v[40:43]\n" \
    "ds_read2_b32 v[118:119], v160 offset0:18 offset1:19\n" \
    "s_waitcnt lgkmcnt(15)\n" \
    "v_mfma_f32_16x16x32_bf16 v[36:39], v[88:91], v[140:143], v[36:39]\n" \
    "ds_read_b128 v[148:151], %[hp] offset:3968\n" \
    "s_waitcnt lgkmcnt(14)\n" \
    "v_mfma_f32_16x16x32_bf16 v[32:35], v[88:91], v[136:139], v[32:35]\n" \
    "ds_read_b128 v[124:127], %[hp] offset:896\n" \
    "s_waitcnt lgkmcnt(12)\n" \
    "v_mfma_f32_16x16x32_bf16 v[28:31], v[88:91], v[132:135], v[28:31]\n" \
    "ds_read_b128 v[120:123], %[hp] offset:384\n" \
    "s_waitcnt lgkmcnt(10)\n" \
    "v_mfma_f32_16x16x32_bf16 v[24:27], v[88:91], v[128:131], v[24:27]\n" \
    "v_fmac_f32 v36, %[dd], v84\n" \
    "v_fmac_f32 v37, %[dd], v85\n" \
    "v_mfma_f32_16x16x32_bf16 v[20:23], v[88:91], v[156:159], v[20:23]\n" \
    "v_fmac_f32 v38, %[dd], v86\n" \
    "v_fmac_f32 v39, %[dd], v87\n" \
    "v_mfma_f32_16x16x32_bf16 v[16:19], v[88:91], v[152:155], v[16:19]\n" \
    "ds_read2_b32 v[88:89], v163 offset0:176 offset1:177\n" \
    "v_mfma_f32_16x16x32_bf16 v[44:47], v[92:95], v[144:147], v[44:47]\n" \
    "ds_read2_b32 v[90:91], v163 offset0:178 offset1:179\n" \
    "v_mfma_f32_16x16x32_bf16 v[40:43], v[92:95], v[140:143], v[40:43]\n" \
    "ds_read_b128 v[144:147], %[hp] offset:3456\n" \
    "v_mfma_f32_16x16x32_bf16 v[36:39], v[92:95], v[136:139], v[36:39]\n" \
    "v_mfma_f32_16x16x32_bf16 v[32:35], v[92:95], v[132:135], v[32:35]\n" \
    "v_mfma_f32_16x16x32_bf16 v[28:31], v[92:95], v[128:131], v[28:31]\n" \
    "ds_read_b64 v[86:87], %[kvr] offset:3072\n" \
    "v_mfma_f32_16x16x32_bf16 v[24:27], v[92:95], v[156:159], v[24:27]\n" \
    "v_mfma_f32_16x16x32_bf16 v[20:23], v[92:95], v[152:155], v[20:23]\n" \
    "ds_read2_b32 v[92:93], v163 offset0:32 offset1:33\n" \
    "v_mfma_f32_16x16x32_bf16 v[44:47], v[96:99], v[140:143], v[44:47]\n" \
    "ds_read_b128 v[140:143], %[hp] offset:2944\n" \
    "v_mfma_f32_16x16x32_bf16 v[40:43], v[96:99], v[136:139], v[40:43]\n" \
    "ds_read2_b32 v[94:95], v163 offset0:34 offset1:35\n" \
    "v_mfma_f32_16x16x32_bf16 v[36:39], v[96:99], v[132:135], v[36:39]\n" \
    "v_mfma_f32_16x16x32_bf16 v[32:35], v[96:99], v[128:131], v[32:35]\n" \
    "v_mfma_f32_16x16x32_bf16 v[28:31], v[96:99], v[156:159], v[28:31]\n" \
    "v_mfma_f32_16x16x32_bf16 v[24:27], v[96:99], v[152:155], v[24:27]\n" \
    "ds_read2_b32 v[96:97], v162 offset0:176 offset1:177\n" \
    "v_mfma_f32_16x16x32_bf16 v[44:47], v[100:103], v[136:139], v[44:47]\n" \
    "ds_read_b128 v[136:139], %[hp] offset:2432\n" \
    "v_mfma_f32_16x16x32_bf16 v[40:43], v[100:103], v[132:135], v[40:43]\n" \
    "ds_read2_b32 v[98:99], v162 offset0:178 offset1:179\n" \
    "v_mfma_f32_16x16x32_bf16 v[36:39], v[100:103], v[128:131], v[36:39]\n" \
    "v_mfma_f32_16x16x32_bf16 v[32:35], v[100:103], v[156:159], v[32:35]\n" \
    "s_waitcnt lgkmcnt(6)\n" \
    "v_lshlrev_b32 v84, 16, v86\n" \
    "v_mfma_f32_16x16x32_bf16 v[28:31], v[100:103], v[152:155], v[28:31]\n" \
    "ds_read2_b32 v[100:101], v162 offset0:32 offset1:33\n" \
    "v_mfma_f32_16x16x32_bf16 v[44:47], v[104:107], v[132:135], v[44:47]\n" \
    "ds_read_b128 v[132:135], %[hp] offset:1920\n" \
    "v_and_b32 v85, 0xffff0000, v86\n" \
    "v_mfma_f32_16x16x32_bf16 v[40:43], v[104:107], v[128:131], v[40:43]\n" \
    "ds_read2_b32 v[102:103], v162 offset0:34 offset1:35\n" \
    "v_mfma_f32_16x16x32_bf16 v[36:39], v[104:107], v[156:159], v[36:39]\n" \
    "v_lshlrev_b32 v86, 16, v87\n" \
    "v_mfma_f32_16x16x32_bf16 v[32:35], v[104:107], v[152:155], v[32:35]\n" \
    "ds_read2_b32 v[104:105], v161 offset0:176 offset1:177\n" \
    "v_mfma_f32_16x16x32_bf16 v[44:47], v[108:111], v[128:131], v[44:47]\n" \
    "ds_read_b128 v[128:131], %[hp] offset:1408\n" \
    "v_and_b32 v87, 0xffff0000, v87\n" \
    "v_mfma_f32_16x16x32_bf16 v[40:43], v[108:111], v[156:159], v[40:43]\n" \
    "ds_read2_b32 v[106:107], v161 offset0:178 offset1:179\n" \
    "v_mfma_f32_16x16x32_bf16 v[36:39], v[108:111], v[152:155], v[36:39]\n" \
    "ds_read2_b32 v[108:109], v161 offset0:32 offset1:33\n" \
    "v_mfma_f32_16x16x32_bf16 v[44:47], v[112:115], v[156:159], v[44:47]\n" \
    "ds_read2_b32 v[110:111], v161 offset0:34 offset1:35\n" \
    "v_mfma_f32_16x16x32_bf16 v[40:43], v[112:115], v[152:155], v[40:43]\n" \
    "ds_read2_b32 v[112:113], v160 offset0:176 offset1:177\n" \
    "v_mfma_f32_16x16x32_bf16 v[44:47], v[116:119], v[152:155], v[44:47]\n" \
    "ds_read2_b32 v[114:115], v160 offset0:178 offset1:179\n" \
    "v_mfma_f32_16x16x32_bf16 v[44:47], v[88:91], v[148:151], v[44:47]\n" \
    "ds_read2_b32 v[116:117], v160 offset0:32 offset1:33\n" \
    "v_mfma_f32_16x16x32_bf16 v[40:43], v[88:91], v[144:147], v[40:43]\n" \
    "ds_read2_b32 v[118:119], v160 offset0:34 offset1:35\n" \
    "s_waitcnt lgkmcnt(15)\n" \
    "v_mfma_f32_16x16x32_bf16 v[36:39], v[88:91], v[140:143], v[36:39]\n" \
    "ds_read_b128 v[152:155], %[hp] offset:448\n" \
    "s_waitcnt lgkmcnt(14)\n" \
    "v_mfma_f32_16x16x32_bf16 v[32:35], v[88:91], v[136:139], v[32:35]\n" \
    "ds_read_b128 v[156:159], %[hp] offset:960\n" \
    "s_waitcnt lgkmcnt(12)\n" \
    "v_mfma_f32_16x16x32_bf16 v[28:31], v[88:91], v[132:135], v[28:31]\n" \
    "ds_read_b128 v[148:151], %[hp] offset:4032\n" \
    "v_fmac_f32 v40, %[dd], v84\n" \
    "v_fmac_f32 v41, %[dd], v85\n" \
    "s_waitcnt lgkmcnt(10)\n" \
    "v_mfma_f32_16x16x32_bf16 v[24:27], v[88:91], v[128:131], v[24:27]\n" \
    "v_fmac_f32 v42, %[dd], v86\n" \
    "v_fmac_f32 v43, %[dd], v87\n" \
    "v_mfma_f32_16x16x32_bf16 v[20:23], v[88:91], v[124:127], v[20:23]\n" \
    "v_mfma_f32_16x16x32_bf16 v[16:19], v[88:91], v[120:123], v[16:19]\n" \
    "ds_read2_b32 v[88:89], v163 offset0:192 offset1:193\n" \
    "v_mfma_f32_16x16x32_bf16 v[44:47], v[92:95], v[144:147], v[44:47]\n" \
    "ds_read2_b32 v[90:91], v163 offset0:194 offset1:195\n" \
    "v_mfma_f32_16x16x32_bf16 v[40:43], v[92:95], v[140:143], v[40:43]\n" \
    "ds_read_b128 v[144:147], %[hp] offset:3520\n" \
    "v_mfma_f32_16x16x32_bf16 v[36:39], v[92:95], v[136:139], v[36:39]\n" \
    "v_mfma_f32_16x16x32_bf16 v[32:35], v[92:95], v[132:135], v[32:35]\n" \
    "v_mfma_f32_16x16x32_bf16 v[28:31], v[92:95], v[128:131], v[28:31]\n" \
    "ds_read_b64 v[86:87], %[kvr] offset:3584\n" \
    "v_mfma_f32_16x16x32_bf16 v[24:27], v[92:95], v[124:127], v[24:27]\n" \
    "v_mfma_f32_16x16x32_bf16 v[20:23], v[92:95], v[120:123], v[20:23]\n" \
    "ds_read2_b32 v[92:93], v163 offset0:48 offset1:49\n" \
    "v_mfma_f32_16x16x32_bf16 v[44:47], v[96:99], v[140:143], v[44:47]\n" \
    "ds_read2_b32 v[94:95], v163 offset0:50 offset1:51\n" \
    "v_mfma_f32_16x16x32_bf16 v[40:43], v[96:99], v[136:139], v[40:43]\n" \
    "ds_read_b128 v[140:143], %[hp] offset:3008\n" \
    "v_mfma_f32_16x16x32_bf16 v[36:39], v[96:99], v[132:135], v[36:39]\n" \
    "v_mfma_f32_16x16x32_bf16 v[32:35], v[96:99], v[128:131], v[32:35]\n" \
    "v_mfma_f32_16x16x32_bf16 v[28:31], v[96:99], v[124:127], v[28:31]\n" \
    "v_mfma_f32_16x16x32_bf16 v[24:27], v[96:99], v[120:123], v[24:27]\n" \
    "ds_read2_b32 v[96:97], v162 offset0:192 offset1:193\n" \
    "v_mfma_f32_16x16x32_bf16 v[44:47], v[100:103], v[136:139], v[44:47]\n" \
    "ds_read2_b32 v[98:99], v162 offset0:194 offset1:195\n" \
    "v_mfma_f32_16x16x32_bf16 v[40:43], v[100:103], v[132:135], v[40:43]\n" \
    "ds_read_b128 v[136:139], %[hp] offset:2496\n" \
    "v_mfma_f32_16x16x32_bf16 v[36:39], v[100:103], v[128:131], v[36:39]\n" \
    "v_mfma_f32_16x16x32_bf16 v[32:35], v[100:103], v[124:127], v[32:35]\n" \
    "s_waitcnt lgkmcnt(6)\n" \
    "v_lshlrev_b32 v84, 16, v86\n" \
    "v_mfma_f32_16x16x32_bf16 v[28:31], v[100:103], v[120:123], v[28:31]\n" \
    "ds_read2_b32 v[100:101], v162 offset0:48 offset1:49\n" \
    "v_mfma_f32_16x16x32_bf16 v[44:47], v[104:107], v[132:135], v[44:47]\n" \
    "ds_read_b128 v[132:135], %[hp] offset:1984\n" \
    "v_and_b32 v85, 0xffff0000, v86\n" \
    "v_mfma_f32_16x16x32_bf16 v[40:43], v[104:107], v[128:131], v[40:43]\n" \
    "ds_read2_b32 v[102:103], v162 offset0:50 offset1:51\n" \
    "v_mfma_f32_16x16x32_bf16 v[36:39], v[104:107], v[124:127], v[36:39]\n" \
    "v_lshlrev_b32 v86, 16, v87\n" \
    "v_mfma_f32_16x16x32_bf16 v[32:35], v[104:107], v[120:123], v[32:35]\n" \
    "ds_read2_b32 v[104:105], v161 offset0:192 offset1:193\n" \
    "v_mfma_f32_16x16x32_bf16 v[44:47], v[108:111], v[128:131], v[44:47]\n" \
    "ds_read_b128 v[128:131], %[hp] offset:1472\n" \
    "v_and_b32 v87, 0xffff0000, v87\n" \
    "v_mfma_f32_16x16x32_bf16 v[40:43], v[108:111], v[124:127], v[40:43]\n" \
    "ds_read2_b32 v[106:107], v161 offset0:194 offset1:195\n" \
    "v_mfma_f32_16x16x32_bf16 v[36:39], v[108:111], v[120:123], v[36:39]\n" \
    "ds_read2_b32 v[108:109], v161 offset0:48 offset1:49\n" \
    "v_mfma_f32_16x16x32_bf16 v[44:47], v[112:115], v[124:127], v[44:47]\n" \
    "ds_read2_b32 v[110:111], v161 offset0:50 offset1:51\n" \
    "v_mfma_f32_16x16x32_bf16 v[40:43], v[112:115], v[120:123], v[40:43]\n" \
    "ds_read2_b32 v[112:113], v160 offset0:192 offset1:193\n" \
    "v_mfma_f32_16x16x32_bf16 v[44:47], v[116:119], v[120:123], v[44:47]\n" \
    "ds_read2_b32 v[114:115], v160 offset0:194 offset1:195\n" \
    "v_mfma_f32_16x16x32_bf16 v[16:19], v[88:91], v[152:155], v[16:19]\n" \
    "ds_read2_b32 v[116:117], v160 offset0:48 offset1:49\n" \
    "v_mfma_f32_16x16x32_bf16 v[20:23], v[88:91], v[156:159], v[20:23]\n" \
    "ds_read2_b32 v[118:119], v160 offset0:50 offset1:51\n" \
    "s_waitcnt lgkmcnt(15)\n" \
    "v_mfma_f32_16x16x32_bf16 v[20:23], v[92:95], v[152:155], v[20:23]\n" \
    "ds_read_b32 v50, %[ctl] offset:88\n" \
    "s_waitcnt lgkmcnt(8)\n" \
    "v_mfma_f32_16x16x32_bf16 v[24:27], v[88:91], v[128:131], v[24:27]\n" \
    "v_fmac_f32 v44, %[dd], v84\n" \
    "v_fmac_f32 v45, %[dd], v85\n" \
    "v_mfma_f32_16x16x32_bf16 v[24:27], v[92:95], v[156:159], v[24:27]\n" \
    "v_fmac_f32 v46, %[dd], v86\n" \
    "v_fmac_f32 v47, %[dd], v87\n" \
    "v_mfma_f32_16x16x32_bf16 v[24:27], v[96:99], v[152:155], v[24:27]\n" \
    "v_mfma_f32_16x16x32_bf16 v[28:31], v[88:91], v[132:135], v[28:31]\n" \
    "v_cvt_pk_bf16_f32 v52, v16, v17\n" \
    "v_cvt_pk_bf16_f32 v54, v20, v21\n" \
    "v_mfma_f32_16x16x32_bf16 v[28:31], v[92:95], v[128:131], v[28:31]\n" \
    "v_cvt_pk_bf16_f32 v53, v18, v19\n" \
    "v_cvt_pk_bf16_f32 v55, v22, v23\n" \
    "v_mfma_f32_16x16x32_bf16 v[28:31], v[96:99], v[156:159], v[28:31]\n" \
    "v_min_u32 v52, -2, v52\n" \
    "v_min_u32 v54, -2, v54\n" \
    "v_mfma_f32_16x16x32_bf16 v[28:31], v[100:103], v[152:155], v[28:31]\n" \
    "v_min_u32 v53, -2, v53\n" \
    "v_min_u32 v55, -2, v55\n" \
    "v_mfma_f32_16x16x32_bf16 v[32:35], v[88:91], v[136:139], v[32:35]\n" \
    "v_permlane16_swap_b32 v52, v54\n" \
    "v_add_u32 v68, 0, %[yo]\n" \
    "v_mfma_f32_16x16x32_bf16 v[32:35], v[92:95], v[132:135], v[32:35]\n" \
    "v_permlane16_swap_b32 v53, v55\n" \
    "v_mfma_f32_16x16x32_bf16 v[32:35], v[96:99], v[128:131], v[32:35]\n" \
    "s_waitcnt lgkmcnt(0)\n" \
    "v_readfirstlane_b32 %[tmp], v50\n" \
    "s_mov_b32 %[cnt], 0\n" \
    "s_cmp_lg_u32 %[tmp], 0\n" \
    "s_cbranch_scc1 Lok_%=\n" \
    "Ls2_%=:\n" \
    "ds_read_b32 v50, %[ctl] offset:88\n" \
    "s_add_u32 %[cnt], %[cnt], 1\n" \
    "s_waitcnt lgkmcnt(0)\n" \
    "v_readfirstlane_b32 %[tmp], v50\n" \
    "s_cmp_ge_u32 %[cnt], %[cap]\n" \
    "s_cbranch_scc1 Lok_%=\n" \
    "s_cmp_eq_u32 %[tmp], 0\n" \
    "s_cbranch_scc1 Ls2_%=\n" \
    "Lok_%=:\n" \
    "global_store_dwordx4 v68, v[52:55], %[wsp] sc1\n" \
    "v_mfma_f32_16x16x32_bf16 v[32:35], v[100:103], v[156:159], v[32:35]\n" \
    "v_cvt_pk_bf16_f32 v56, v24, v25\n" \
    "v_cvt_pk_bf16_f32 v58, v28, v29\n" \
    "v_mfma_f32_16x16x32_bf16 v[32:35], v[104:107], v[152:155], v[32:35]\n" \
    "v_cvt_pk_bf16_f32 v57, v26, v27\n" \
    "v_cvt_pk_bf16_f32 v59, v30, v31\n" \
    "v_mfma_f32_16x16x32_bf16 v[36:39], v[88:91], v[140:143], v[36:39]\n" \
    "v_min_u32 v56, -2, v56\n" \
    "v_min_u32 v58, -2, v58\n" \
    "v_mfma_f32_16x16x32_bf16 v[36:39], v[92:95], v[136:139], v[36:39]\n" \
    "v_min_u32 v57, -2, v57\n" \
    "v_min_u32 v59, -2, v59\n" \
    "v_mfma_f32_16x16x32_bf16 v[36:39], v[96:99], v[132:135], v[36:39]\n" \
    "v_permlane16_swap_b32 v56, v58\n" \
    "v_add_u32 v69, 655360, %[yo]\n" \
    "v_mfma_f32_16x16x32_bf16 v[36:39], v[100:103], v[128:131], v[36:39]\n" \
    "v_permlane16_swap_b32 v57, v59\n" \
    "v_mfma_f32_16x16x32_bf16 v[36:39], v[104:107], v[156:159], v[36:39]\n" \
    "global_store_dwordx4 v69, v[56:59], %[wsp] sc1\n" \
    "v_mfma_f32_16x16x32_bf16 v[36:39], v[108:111], v[152:155], v[36:39]\n" \
    "v_mfma_f32_16x16x32_bf16 v[40:43], v[88:91], v[144:147], v[40:43]\n" \
    "v_mfma_f32_16x16x32_bf16 v[40:43], v[92:95], v[140:143], v[40:43]\n" \
    "v_mfma_f32_16x16x32_bf16 v[40:43], v[96:99], v[136:139], v[40:43]\n" \
    "v_mfma_f32_16x16x32_bf16 v[40:43], v[100:103], v[132:135], v[40:43]\n" \
    "v_cvt_pk_bf16_f32 v60, v32, v33\n" \
    "v_cvt_pk_bf16_f32 v62, v36, v37\n" \
    "v_mfma_f32_16x16x32_bf16 v[40:43], v[104:107], v[128:131], v[40:43]\n" \
    "v_cvt_pk_bf16_f32 v61, v34, v35\n" \
    "v_cvt_pk_bf16_f32 v63, v38, v39\n" \
    "v_mfma_f32_16x16x32_bf16 v[40:43], v[108:111], v[156:159], v[40:43]\n" \
    "v_min_u32 v60, -2, v60\n" \
    "v_min_u32 v62, -2, v62\n" \
    "v_mfma_f32_16x16x32_bf16 v[40:43], v[112:115], v[152:155], v[40:43]\n" \
    "v_min_u32 v61, -2, v61\n" \
    "v_min_u32 v63, -2, v63\n" \
    "v_mfma_f32_16x16x32_bf16 v[44:47], v[88:91], v[148:151], v[44:47]\n" \
    "v_permlane16_swap_b32 v60, v62\n" \
    "v_add_u32 v70, 1310720, %[yo]\n" \
    "v_mfma_f32_16x16x32_bf16 v[44:47], v[92:95], v[144:147], v[44:47]\n" \
    "v_permlane16_swap_b32 v61, v63\n" \
    "v_mfma_f32_16x16x32_bf16 v[44:47], v[96:99], v[140:143], v[44:47]\n" \
    "global_store_dwordx4 v70, v[60:63], %[wsp] sc1\n" \
    "v_mfma_f32_16x16x32_bf16 v[44:47], v[100:103], v[136:139], v[44:47]\n" \
    "v_mfma_f32_16x16x32_bf16 v[44:47], v[104:107], v[132:135], v[44:47]\n" \
    "v_mfma_f32_16x16x32_bf16 v[44:47], v[108:111], v[128:131], v[44:47]\n" \
    "v_mfma_f32_16x16x32_bf16 v[44:47], v[112:115], v[156:159], v[44:47]\n" \
    "v_mfma_f32_16x16x32_bf16 v[44:47], v[116:119], v[152:155], v[44:47]\n" \
    "s_nop 7\n" \
    "v_cvt_pk_bf16_f32 v64, v40, v41\n" \
    "v_cvt_pk_bf16_f32 v66, v44, v45\n" \
    "v_cvt_pk_bf16_f32 v65, v42, v43\n" \
    "v_cvt_pk_bf16_f32 v67, v46, v47\n" \
    "v_min_u32 v64, -2, v64\n" \
    "v_min_u32 v66, -2, v66\n" \
    "v_min_u32 v65, -2, v65\n" \
    "v_min_u32 v67, -2, v67\n" \
    "v_permlane16_swap_b32 v64, v66\n" \
    "v_add_u32 v71, 1966080, %[yo]\n" \
    "v_permlane16_swap_b32 v65, v67\n" \
    "global_store_dwordx4 v71, v[64:67], %[wsp] sc1\n" \
    "s_nop 1\n"
#define CONV_CLOB "v16", "v17", "v18", "v19", "v20", "v21", "v22", "v23", "v24", "v25", "v26", "v27", "v28", "v29", "v30", "v31", "v32", "v33", "v34", "v35", "v36", "v37", "v38", "v39", "v40", "v41", "v42", "v43", "v44", "v45", "v46", "v47", "v50", "v51", "v52", "v53", "v54", "v55", "v56", "v57", "v58", "v59", "v60", "v61", "v62", "v63", "v64", "v65", "v66", "v67", "v68", "v69", "v70", "v71", "v72", "v73", "v74", "v75", "v76", "v77", "v78", "v79", "v80", "v81", "v82", "v83", "v84", "v85", "v86", "v87", "v88", "v89", "v90", "v91", "v92", "v93", "v94", "v95", "v96", "v97", "v98", "v99", "v100", "v101", "v102", "v103", "v104", "v105", "v106", "v107", "v108", "v109", "v110", "v111", "v112", "v113", "v114", "v115", "v116", "v117", "v118", "v119", "v120", "v121", "v122", "v123", "v124", "v125", "v126", "v127", "v128", "v129", "v130", "v131", "v132", "v133", "v134", "v135", "v136", "v137", "v138", "v139", "v140", "v141", "v142", "v143", "v144", "v145", "v146", "v147", "v148", "v149", "v150", "v151", "v152", "v153", "v154", "v155", "v156", "v157", "v158", "v159", "v160", "v161", "v162", "v163", "scc", "memory"

static_assert(C_HR - L_CTRL == 144 && C_RED - L_CTRL == 84 && C_XD - L_CTRL == 80 && C_OWN - L_CTRL == 88, "offsets in the conv stream");
typedef __attribute__((ext_vector_type(16))) unsigned u32x16;
typedef __attribute__((address_space(3))) const unsigned lds_cu32;
typedef __attribute__((address_space(3))) u32x2 lds_u32x2;
typedef __attribute__((address_space(3))) u32x4 lds_u32x4;
typedef __attribute__((address_space(3))) f32x4 lds_f32x4;
#define GPTR(T, lo, hi) ((T*)(__attribute__((address_space(1))) T*)(((u64)(hi) << 32) | (u64)(lo)))
#if defined(__HIP_DEVICE_COMPILE__)
#define NOPK __attribute__((target("no-packed-fp32-ops")))
#else
#define NOPK
#endif
__global__ NOPK __launch_bounds__(768, 3) void fused_kernel(const u16* x_, const u16* wqkv_, const u16* kin_, const u16* vin_, const u16* shift_k_,
                                                       const u16* ssm_k_, const u16* ssm_D_, const u16* wo_,
                                                       const u16* gw_, char* ws_, u16* out_) {
    u32x16 ka0;
    u32x4 ka1;
    u32x2 ka2;
    asm volatile("s_load_dwordx16 %0, %3, 0\n\ts_load_dwordx4 %1, %3, 64\n\ts_load_dwordx2 %2, %3, 80"
                 : "=&s"(ka0), "=&s"(ka1), "=&s"(ka2) : "s"(__builtin_amdgcn_kernarg_segment_ptr()));
#define x GPTR(const u16, ka0[0], ka0[1])
#define wqkv GPTR(const u16, ka0[2], ka0[3])
#define kin GPTR(const u16, ka0[4], ka0[5])
#define vin GPTR(const u16, ka0[6], ka0[7])
#define shift_k GPTR(const u16, ka0[8], ka0[9])
#define ssm_k GPTR(const u16, ka0[10], ka0[11])
#define ssm_D GPTR(const u16, ka0[12], ka0[13])
#define wo GPTR(const u16, ka0[14], ka0[15])
#define gw GPTR(const u16, ka1[0], ka1[1])
#define ws GPTR(char, ka1[2], ka1[3])
#define out GPTR(u16, ka2[0], ka2[1])
#define wsr __builtin_amdgcn_make_buffer_rsrc(ws, 0, 0x4000000, 0x00020000)
    __shared__ __attribute__((aligned(16))) char smem[LDS_TOTAL];
    const int bid = __builtin_amdgcn_workgroup_id_x();
    const int tid = __builtin_amdgcn_workitem_id_x();
    const int lane = tid & 63;
    const int wave = __builtin_amdgcn_readfirstlane(tid >> 6);
    const int xcd = bid & 7, r5 = bid >> 3, r = r5 & 15;
    const bool isG = r5 >= 16;
    const int hg = (xcd & 1) * 4 + (r & 3);
    const int rb2 = (xcd >> 1) * 4 + (r >> 2);
    const int cq = (xcd & 1) * 2 + (r & 1), rbv = (xcd >> 1) * 8 + (r >> 1);
    const int vid = (cq ^ CQ_X) * 32 + rbv;
    const int sid = rbv * 4 + cq;
    const int rbo = ((xcd + X_SHIFT) & 7) * 4 + (r >> 3) * 2 + (r5 >> 4), oc = r5 & 7;
    const int rto = (rbo >> 1) * 8 + (rbo & 1) * 2;
#define ROWT(i) (rto + ((i) & 1) + ((i) >> 1) * 4)
#define DUE_H ph
    const int w = wave & 3;
    const int g = lane >> 4, n16 = lane & 15, q4 = n16 >> 2, p4 = lane & 3;
    const unsigned lb = (unsigned)(size_t)(lds_char*)smem;
    u64 tag;
    {
        u64 id = llvm_dispatch_id();
        u64 dp = (u64)(size_t)__builtin_amdgcn_dispatch_ptr();
        tag = ((u64)((unsigned)id ^ 0x5bd1e995u) << 32) | (u64)((unsigned)(dp >> 6) ^ (unsigned)(id >> 32) ^ 0x9e3779b9u);
    }
    const unsigned tlo = (unsigned)tag, thi = (unsigned)(tag >> 32);
    u64 code0;
    asm volatile("s_getpc_b64 %0" : "=s"(code0));
    code0 &= ~255ull;
    if (wave == 0) {
        const unsigned cl = lane * 32 + r5;
        if (cl < CODE_LINES) GLT(cl * 128, code0);
    }
    const u32x4 ones = {0xffffffffu, 0xffffffffu, 0xffffffffu, 0xffffffffu};

#define WO_OFF(c) ({ const int rho_ = (c) / 10, ch_ = (c) - rho_ * 10; (unsigned)((rho_ ^ ((rho_ & 8) >> 1)) * (D_DIM * 2) + oc * 160 + ch_ * 16); })
    const int kr = KROT;
#define T_C(i) __builtin_amdgcn_readfirstlane((kr + (i)) >= 10 ? kr + (i) - 10 : kr + (i))
#define T_WV() ({ const int j_ = min(n16, 14); (unsigned)((r * 4 + g) * (NQKV * 2) + j_ * 128 + ((j_ >= 5) + (j_ >= 10) + (xcd & 1)) * 640); })
#define T_XV() ((unsigned)(((xcd >> 1) * 512 + r * 32 + (lane & 31)) * (D_DIM * 2)))
#define T_W(wv, i) GLT(wv, (const char*)wqkv + (size_t)T_C(i) * (64 * NQKV * 2))
#define T_X(xv, i) { const int c0_ = T_C(i), c1_ = T_C((i) + 1); GLT((xv) + (lane < 32 ? c0_ : c1_) * 128, x); }
#define T_PAIR(i) { T_W(wv, i); T_X(xv_, i); T_W(wv, (i) + 1); }

    unsigned e0;
    if (isG) {
        const unsigned t0_ = T_WV() + (kr + (w >> 1)) * (64 * NQKV * 2);
        const unsigned t1_ = ((xcd >> 1) * 512 + r * 32 + (lane & 31)) * (D_DIM * 2) + (kr + (lane >> 5)) * 128;
        e0 = (w & 1) ? t1_ : t0_;
    } else {
        const unsigned c = (wave >= 5 ? wave - 5 : wave) * 64 + lane, row = (c * 3277) >> 16;
        e0 = rbv * (64 * D_DIM * 2) + cq * 320 + row * (D_DIM * 2 - 320) + c * 16;
    }
    asm volatile("" : "+v"(e0));
    asm volatile("s_waitcnt lgkmcnt(0)" : "+s"(ka0), "+s"(ka1), "+s"(ka2) :: "memory");
    if (isG) {
        if (wave >= 4 && wave < 7) GLT(e0, (w & 1) ? x : wqkv);
        BARRIER();
    }
    u32x2 skw;
    if (!isG && wave < 10) {
        const int tens = wave >= 5, i5 = wave - 5 * tens;
        const unsigned lw = lb + (tens ? V_VST : V_KST) + i5 * 1024;
        const char* kb = (const char*)(tens ? vin : kin);
        __builtin_amdgcn_s_sleep(V_DELAY);
        GLS(e0, kb, lw, 0);
        GLS(e0, kb + 16 * D_DIM * 2, lw, 5152);
        GLS(e0, kb + 32 * D_DIM * 2, lw, 2 * 5152);
        GLS(e0, kb + 48 * D_DIM * 2, lw, 3 * 5152);
        asm volatile("global_load_dwordx2 %0, %1, %2" : "=v"(skw) : "v"((cq * 160 + wave * 16 + n16) * 8), "s"(shift_k) : "memory");
        if (wave == 0) { if (lane < 60) GLS(e0, kb - (rbv ? 3 * D_DIM * 2 : 0), lb, V_KST - 32 - 960); }
    }
    if (wave == 11) {
        if (lane < 16) *(u32x4*)(smem + L_CTRL + lane * 16) = (u32x4){0u, 0u, 0u, 0u};
        asm volatile("s_waitcnt lgkmcnt(0)" ::: "memory");
    }
    STAMP(1);
#define CF_OFF() ((unsigned)(lane < 16 ? WS_HF + ROWT(lane & 3) * 128 : lane < 32 ? WS_SF + (oc * 16 + (lane & 15)) * 128 : WS_HN + ((rbo >> 1) * 8 + (lane & 7)) * 128))
#define NOW() ((unsigned)__builtin_amdgcn_s_memrealtime())
#define TICKS_PER_CYCLE(now, c0, r0) ((float)((now) - (r0)) * __builtin_amdgcn_rcpf((float)((unsigned)__builtin_amdgcn_s_memtime() - (c0))))
#define CF_LOOK(v) { const bool k_ = v[0] == tlo && v[1] == thi; cfm |= __builtin_amdgcn_ballot_w64(k_); \
            if (k_) { tv = v[2] | 1; tu = v[3] | 1; } \
            if (!(~cfm & 0xffff) && lane >= 16 && lane < 40 && (lane < 32 ? !(~cfm & 0xffff0000u) : ((cfm >> (lane & ~1)) & 3) == 3)) \
                asm volatile("ds_max_u32 %0, %1\n\tds_max_u32 %0, %2 offset:16" ::"v"(lb + (lane < 32 ? C_TY : C_TM + (lane & 6) * 2)), "v"(tv), "v"(lane < 32 ? 0u : tu) : "memory"); }
#define CF_LOOP(off, need) _Pragma("nounroll") for (int it = 0; it < SPIN_CAP && (~cfm & (need)); ++it) { \
            if (it) __builtin_amdgcn_s_sleep(CF_SLEEP); \
            asm volatile("" ::: "memory"); \
            u32x4 v = {0u, 0u, 0u, 0u}; \
            if (!((cfm >> lane) & 1)) v = __builtin_amdgcn_raw_buffer_load_b128(wsr, off, 0, 16); \
            CF_LOOK(v); }
#define G_LD(n) __builtin_amdgcn_raw_buffer_load_b32(wsr, (n) * 4, WS_G + ROWT(w) * 64, 16)
#define G_CHK(v, n) { if (__builtin_expect(__any(v == 0xffffffffu), 0)) { _Pragma("nounroll") for (int it = 0; it < SPIN_CAP; ++it) { \
            asm volatile("" ::: "memory"); v = G_LD(n); if (!__any(v == 0xffffffffu)) break; __builtin_amdgcn_s_sleep(4); } } }
#define XCH_AT(rt) (lb + (isG ? ((rt) < 2 ? G_Q : G_XCH - 20480) : V_XCH) + (rt) * 10240)
#define Y_OFF (WS_Y + rbo * (D_DIM * 128) + oc * 10240 + w * 1024)
#define Y_LD(i) asm volatile("global_load_dwordx4 %0, %1, %2 sc1" : "=v"(yv[i]) : "v"(l16y), "s"(ws + Y_OFF + ((i) < 2 ? (i) * 4096 : w < 2 ? 8192 : 0)) : "memory")
#define Y_LD2(i) asm volatile("global_load_dwordx4 %0, %1, %2 sc1" : "+v"(yv[i]) : "v"(l16y), "s"(ws + Y_OFF + ((i) < 2 ? (i) * 4096 : w < 2 ? 8192 : 0)) : "memory")
#define P_RD() { _Pragma("unroll") for (int ct = 0; ct < 5; ++ct) { pa[ct] = *(volatile const lds_f32x4*)(size_t)(xa + ct * 1024); pb[ct] = *(volatile const lds_f32x4*)(size_t)(xa + 5120 + ct * 1024); } SB(); }
#define Y_TIME(t) { _Pragma("nounroll") for (int it = 0; it < 20000 && (int)(NOW() - (t)) < 0; ++it) {} }
#define TY_GET() ({ unsigned t_ = 0; _Pragma("nounroll") for (int it = 0; it < SPIN_CAP; ++it) { t_ = __builtin_amdgcn_readfirstlane(lds_ld32(lb + C_TY)); if (t_) break; __builtin_amdgcn_s_sleep(3); } t_; })
#define G_FIRST() unsigned g1 = G_LD(lane & 15)
#define Y_FIRST() { Y_LD(0); Y_LD(1); Y_LD(2); }
#define G_MUL() { G_CHK(g1, lane & 15); _Pragma("unroll") for (int ct = 0; ct < 5; ++ct) gq[ct] *= __uint_as_float(g1); \
        asm volatile("" : "+v"(gq[0]), "+v"(gq[1]), "+v"(gq[2]), "+v"(gq[3]), "+v"(gq[4])); }
#define RELANE() const int lane_ = opaque(lane); const int g = lane_ >> 4, n16 = lane_ & 15, q4 = n16 >> 2, p4 = lane_ & 3
#define SENT4(v) __any(max(max((v)[0], (v)[1]), max((v)[2], (v)[3])) == 0xffffffffu)
#define Q_READ(base) f32x4 gq[5]; { const int l_ = opaque(lane); _Pragma("unroll") for (int ct = 0; ct < 5; ++ct) { \
            const u32x2 q2 = *(const lds_u32x2*)(size_t)(lb + (base) + w * 2560 + (l_ & 15) * 160 + 8 * (l_ >> 4) + ct * 32); \
            gq[ct] = (f32x4){lo_f(q2[0]), hi_f(q2[0]), lo_f(q2[1]), hi_f(q2[1])}; } \
        asm volatile("" : "+v"(gq[0]), "+v"(gq[1]), "+v"(gq[2]), "+v"(gq[3]), "+v"(gq[4]) :: "memory"); }
#define P_STAGE(YE) { \
        RELANE(); \
        FLUSH_LO(); \
        const unsigned l_y = lb + (isG ? G_Y : V_Y); \
        const unsigned yw = opaque(l_y + (w * 8 + (lane_ >> 3)) * Y_PITCH + (lane_ & 7) * 16), yr = opaque(l_y + (4 * g + q4) * Y_PITCH + w * 32 + p4 * 8), xa = opaque(XCH_AT(w) + lane_ * 16); \
        u16* ob = out + (size_t)(ROWT(w) * 16 + n16) * D_DIM + oc * 80; \
        const float g0 = 1.f - __uint_as_float(g1); \
        asm volatile("" : "+v"(ob)); \
_Pragma("nounroll") \
        for (int it = 0; it < SPIN_CAP; ++it) { \
            asm volatile("s_cmp_eq_u32 %3, 0\n\ts_cbranch_scc0 1f\n\ts_waitcnt vmcnt(0)\n1:\n\ts_waitcnt vmcnt(3)" : "+v"(yv[0]), "+v"(yv[1]), "+v"(yv[2]) : "s"(__builtin_amdgcn_readfirstlane(it == 0 ? (unsigned)(YE) : 0u)) : "memory", "scc"); \
            *(lds_vu32x4*)(size_t)yw = yv[0]; \
            *(lds_vu32x4*)(size_t)(yw + 32 * Y_PITCH) = yv[1]; \
            if (w < 2) *(lds_vu32x4*)(size_t)(yw + 64 * Y_PITCH) = yv[2]; \
            SB(); \
            if (__builtin_expect(!__any(max(max(max(max(yv[0][0], yv[0][1]), max(yv[0][2], yv[0][3])), max(max(yv[1][0], yv[1][1]), max(yv[1][2], yv[1][3]))), max(max(yv[2][0], yv[2][1]), max(yv[2][2], yv[2][3]))) == 0xffffffffu), 1)) break; \
            if (it || Y_SL0) __builtin_amdgcn_s_sleep(Y_SLEEP); \
            if (SENT4(yv[0])) Y_LD2(0); \
            if (SENT4(yv[1])) Y_LD2(1); \
            if (SENT4(yv[2])) Y_LD2(2); \
        } \
        STAMP(8); \
        if (lds_add_rtn(lb + C_Y, lane_) < 3) { _Pragma("nounroll") for (int it = 0; it < SPIN_CAP; ++it) if (__builtin_amdgcn_readfirstlane(lds_ld32(lb + C_Y)) >= 4) break; } \
        u32x2 y2[5]; \
        f32x4 pa[5], pb[5]; \
_Pragma("unroll") \
        for (int ct = 0; ct < 5; ++ct) y2[ct] = __builtin_bit_cast(u32x2, TRRD((lds_char*)(size_t)(yr + ct * 16 * Y_PITCH))); \
        const unsigned xc = lds_v32(lb + C_XCH + w * 4); \
        SB(); \
        P_RD();       \
_Pragma("unroll") \
        for (int ct = 0; ct < 5; ++ct) gq[ct] *= (f32x4){lo_f(y2[ct][0]), hi_f(y2[ct][0]), lo_f(y2[ct][1]), hi_f(y2[ct][1])}; \
        asm volatile("" : "+v"(gq[0]), "+v"(gq[1]), "+v"(gq[2]), "+v"(gq[3]), "+v"(gq[4])); \
        STAMP(9); \
        if (__builtin_amdgcn_readfirstlane(xc) < 2) { \
            _Pragma("nounroll") for (int it = 0; it < SPIN_CAP; ++it) if (__builtin_amdgcn_readfirstlane(lds_ld32(lb + C_XCH + w * 4)) >= 2) break; \
            P_RD(); \
        } \
        STAMP(13); \
        unsigned pk[5][2]; \
_Pragma("unroll") \
        for (int ct = 0; ct < 5; ++ct) { \
            const f32x4 fin = gq[ct] + g0 * (pa[ct] + pb[ct]); \
            pk[ct][0] = pack2(fin[0], fin[1]); \
            pk[ct][1] = pack2(fin[2], fin[3]); \
        } \
_Pragma("unroll") \
        for (int p = 0; p < 2; ++p) { \
            const auto r0 = __builtin_amdgcn_permlane16_swap(pk[2 * p][0], pk[2 * p + 1][0], false, false); \
            const auto r1 = __builtin_amdgcn_permlane16_swap(pk[2 * p][1], pk[2 * p + 1][1], false, false); \
            asm volatile("global_store_dwordx4 %0, %1, off " OUT_POL "\n\ts_nop 1" :: "v"(ob + (2 * p + (g & 1)) * 16 + (g >> 1) * 8), "v"((u32x4){r0[0], r1[0], r0[1], r1[1]}) : "memory"); \
        } \
        asm volatile("global_store_dwordx2 %0, %1, off " OUT_POL :: "v"(ob + 64 + 4 * g), "v"((u32x2){pk[4][0], pk[4][1]}) : "memory"); \
        FLUSH(); \
        return; }

    const int ph = isG ? wave >> 2 : 1 - (wave >> 2), kh = w >> 1, trt = (1 - ph) * 2 + (w & 1);
    unsigned ofo, pc, pd, xa;
#define TAIL_ADDR() { RELANE(); const int hsw = (g & 1) * 4; \
        ofo = opaque(WS_O + (ROWT(trt) * 20 + kh * 10) * 1024 + 2048 + lane_ * 16); \
        pc = opaque(lb + (8 * g + hsw + q4) * 160 + p4 * 8 + (isG ? (kh ? G_WO1 : G_WO0) : V_WO + kh * 51200)); \
        pd = opaque(pc + (4 - 2 * hsw) * 160); \
        xa = opaque(XCH_AT(trt) + kh * 5120 + lane_ * 16); }

    if (__builtin_expect(isG, 1)) {
      if (__builtin_expect(wave >= 4, 0)) {
        const int half = (wave >> 2) - 1;
        if (half) __builtin_amdgcn_s_setprio(C1_PRIO); else __builtin_amdgcn_s_setprio(C0_PRIO);
        const int hf = 1 - half, rb = rb2 * 2 + hf;
        f32x4 acc[4][5];
        unsigned aofs[2];
#pragma unroll
        for (int sub = 0; sub < 2; ++sub) aofs[sub] = lb + L_A + hf * 8192 + n16 * 128 + (((sub * 4 + g) ^ ((n16 >> 1) & 7)) * 16);
        unsigned wofs[4];
#pragma unroll
        for (int i = 0; i < 4; ++i) {
            const int part = i < 3 ? i : (p4 < 3 ? p4 : 2);
            const int Q = i < 3 ? 5 * w + (w & 1) + p4 : 5 * w + ((w & 1) ? 0 : 4);
            const int c2 = (Q >> 1) - 2 * (hg & 1);
            const int cc = ((unsigned)c2 < 8u ? part * 8 + c2 : 24 + part * 2 + (c2 & 1)) ^ (2 * ((g & 1) * 4 + q4));
            wofs[i] = lb + L_W + (8 * g + q4) * W_PITCH + cc * 16 + (Q & 1) * 8;
        }
        bf16x8 af[5], bA[4], bB[4];
        lds_char* wp[2][4];
        lds_char* ap[2];
#pragma unroll
        for (int i = 0; i < 4; ++i) { wp[0][i] = (lds_char*)(size_t)opaque(wofs[i]); wp[1][i] = (lds_char*)(size_t)opaque(wofs[i] + 3 * W_SLOT); }
        ap[0] = (lds_char*)(size_t)opaque(aofs[0]);
        ap[1] = (lds_char*)(size_t)opaque(aofs[1]);
        const unsigned ctl = opaque(lb + C_FREE), ctf = opaque(lb + C_FREE + (wave - 4) * 4);
#define WSL(J, i) (wp[((J) % NW) / 3][i] + (((J) % NW) % 3) * W_SLOT)
#define MFMA16A(c, a, b) asm volatile("v_mfma_f32_16x16x32_bf16 %0, %1, %2, %0" : "+v"(c) : "v"(a), "v"(b))
#define MFMA16Z(c, a, b) asm volatile("v_mfma_f32_16x16x32_bf16 %0, %1, %2, 0" : "=&v"(c) : "v"(a), "v"(b))
#define MFMA16(c, a, b) do { if (first_) MFMA16Z(c, a, b); else MFMA16A(c, a, b); } while (0)
#define BORD(rt) (((rt) + 1 - R0_) & 3)
#define FR0 (JL < 16 ? JL + 1 : 16)
#define STEP(J, bc, bn, R0) { \
            const bool first_ = (J) == 0; \
            constexpr int R0_ = R0; \
            unsigned fl_ = 4, fs_ = 4; \
            _Pragma("unroll") for (int rt = R0; rt < 5; ++rt) { \
                s16x4 b0_, b1_; \
                if ((J) > JL && (J) <= 17 && rt == 2) { fl_ = lds_v32(ctl + (C_FULL - C_FREE) + ((J) - 13) * 4); SB(); } \
                if ((J) >= FR0 && (J) < 19 && rt == 4) { if ((J) > JL && (J) <= 17) fs_ = __builtin_amdgcn_readfirstlane(fl_); asm volatile("v_mov_b32 %0, %1" : "=v"(fl_) : "n"(J)); SB(); } \
                if (rt > 0) MFMA16(acc[0][rt], bc[0], af[rt]); SB(); \
                if ((J) < 19 && rt < 4 + R0) { b0_ = TRRD(WSL((J) + 1, BORD(rt - R0))); SB(); } \
                MFMA16(acc[1][rt], bc[1], af[rt]); SB(); \
                if ((J) < 19 && rt < 4 + R0) { b1_ = TRRD(WSL((J) + 1, BORD(rt - R0)) + 4 * W_PITCH); bn[BORD(rt - R0)] = cat8(b0_, b1_); SB(); } \
                MFMA16(acc[2][rt], af[rt], bc[2]); SB(); \
                bf16x8 an_; \
                if (TAILM && rt == 4 && (J) < 19) { an_ = *(lds_cbf16x8*)(ap[((J) + 1) & 1] + ((((J) + 1) >> 1) % 3) * A_SLOT + rt * 2048); SB(); \
                    if ((J) >= FR0) { lds_st32(ctf, fl_); SB(); } } \
                MFMA16(acc[3][rt], bc[3], af[rt]); SB(); \
                if (TAILM && rt == 4) { if ((J) < 19) af[rt] = an_; } \
                else if ((J) < 19) { af[rt] = *(lds_cbf16x8*)(ap[((J) + 1) & 1] + ((((J) + 1) >> 1) % 3) * A_SLOT + rt * 2048); SB(); } \
            } \
            if (!TAILM && (J) >= FR0 && (J) < 19) lds_st32(ctf, fl_);        \
            if ((J) == JL && JL < 16) asm volatile("s_waitcnt lgkmcnt(0)" ::: "memory"); \
            if ((J) <= JL) BARRIER(); \
            if ((J) > JL && (J) <= 17) { if (__builtin_expect(fs_ < 4, 0)) spin_ge(lb + C_FULL + ((J) - 13) * 4, 4); } \
            SB(); }
#define GEMM(R0) { \
        _Pragma("unroll") for (int i = 0; i < 4; ++i) bA[i] = cat8(TRRD(wp[0][i]), TRRD(wp[0][i] + 4 * W_PITCH)); \
        _Pragma("unroll") for (int rt = R0; rt < 5; ++rt) af[rt] = *(lds_cbf16x8*)(ap[0] + rt * 2048); \
        SB(); \
        GT(0) STEP(0, bA, bB, R0) GT(1) STEP(1, bB, bA, R0) STAMP(5); GT(2) STEP(2, bA, bB, R0) GT(3) STEP(3, bB, bA, R0) GT(4) STEP(4, bA, bB, R0) GT(5) STEP(5, bB, bA, R0) \
        STAMP(6); GT(6) STEP(6, bA, bB, R0) GT(7) STEP(7, bB, bA, R0) GT(8) STEP(8, bA, bB, R0) GT(9) STEP(9, bB, bA, R0) STAMP(14); GT(10) STEP(10, bA, bB, R0) GT(11) STEP(11, bB, bA, R0) \
        GT(12) STEP(12, bA, bB, R0) GT(13) STEP(13, bB, bA, R0) GT(14) STEP(14, bA, bB, R0) GT(15) STEP(15, bB, bA, R0) GT(16) STEP(16, bA, bB, R0) GT(17) STEP(17, bB, bA, R0) \
        TS(0); sent0 = lds_v32(lb + C_OWN); SB(); GT(18) STEP(18, bA, bB, R0) GT(19) }
#define GT(j)
        f32x4 cm0, cm1;
        u32x4 psel, vxa;
        unsigned scw, oo, ox;
#define ATT_CONST() { \
        const int l_ = opaque(lane), g = l_ >> 4, qi = l_ & 15, q4 = qi >> 2, p4 = l_ & 3, par = l_ & 1; \
        _Pragma("unroll") for (int e = 0; e < 4; ++e) { \
            const bool own = 4 * g + e <= qi, pm = (e & 1) == par; \
            cm0[e] = pm && !own ? 0.f : -3.0e38f; \
            cm1[e] = pm && own ? 0.f : -3.0e38f; \
            const bool ow = 4 * g + par + 2 * (e & 1) <= qi;         \
            const unsigned by = (e & 1) ? 0x0302u : 0x0100u; \
            psel[e] = (ow == (e >= 2)) ? (par ? (by << 16) | 0x0c0cu : 0x0c0c0000u | by) : 0x0c0c0c0cu; \
            vxa[e] = p4 == e ? lb + G_VX + w * 1152 + (hf * 4 + e) * 128 + (4 * g + q4) * 8 : lb + L_CTRL; \
        } \
        const int cc = (hg * 80 + w * 20 + (w & 1) * 4) / 8 + (g >> 1), colx = hg * 80 + w * 20 + ((w & 1) ? 0 : 16); \
        oo = WS_O + ((rb * 4 + (g & 1)) * 20 + (cc >> 2)) * 1024 + ((cc & 3) * 16 + qi) * 16; \
        ox = (g * 20 + (colx >> 5)) * 1024 + (((colx >> 3) & 3) * 16 + qi) * 16 + (colx & 4) * 2; \
        asm volatile("" : "+v"(cm0), "+v"(cm1), "+v"(psel), "+v"(vxa), "+v"(oo), "+v"(ox)); SB(); }
        scw = g == 2 ? lb + G_VX + w * 1152 + hf * 512 + n16 * 8 : lb + G_VXD;
        asm volatile("" : "+v"(scw));
        ATT_CONST();
        unsigned sent0;
        unsigned hxo = opaque(WS_HX + ((rb2 + hf) * 8 + hg) * 5120 + w * 1280 + lane * 16), hxp = opaque(hxo + 1024 - lane * 12);
        unsigned hxw = opaque(g >= 2 ? lb + G_VX + w * 1152 + n16 * 8 + (g & 1) * 4 : lb + G_VXD);
        BARRIER();
        STAMP(7);
        CSTAMP(0);
        GEMM(1)
        typedef __attribute__((address_space(3))) volatile unsigned lds_vu32;
#define HINT(at)
        u32x4 kop[5], qop[5];
        u32x16 vv;
        const unsigned hx = lb + G_HX + w * 1152;
        u32x4 ha0 = {0u, 0u, 0u, 0u};
        unsigned hb0 = 0;
        const unsigned far = half * rb2;
#define HX_LD() asm volatile("s_cmp_eq_u32 %5, 0\n\ts_cbranch_scc1 1f\n\tglobal_load_dwordx4 %0, %2, %4 sc1\n\tglobal_load_dword %1, %3, %4 sc1\n1:" \
            : "+v"(ha0), "+v"(hb0) : "v"(hxo), "v"(hxp), "s"(ws), "s"(far) : "memory", "scc")
        if (__builtin_expect(!__builtin_amdgcn_readfirstlane(sent0), 0)) spin_ge(lb + C_OWN, 1);
        if (half) __builtin_amdgcn_s_setprio(L19_PRIO);
        unsigned d0, d1, sq[5];
#define ML(i, rt) { if (rt >= 1) { acc[i][rt] = i == 2 ? __builtin_amdgcn_mfma_f32_16x16x32_bf16(af[rt], bB[2], acc[i][rt], 0, 0, 0) : __builtin_amdgcn_mfma_f32_16x16x32_bf16(bB[i], af[rt], acc[i][rt], 0, 0, 0); } SB(); }
#define PIN2(a, b) asm volatile("" : "+v"(a), "+v"(b)); SB()
#define PK_K(rt) { kop[rt][0] = pack2(acc[1][rt][0], acc[1][rt][1]); kop[rt][1] = pack2(acc[1][rt][2], acc[1][rt][3]); PIN2(kop[rt][0], kop[rt][1]); }
#define PK_V(rt) { vv[2 * rt] = pack2(acc[2][rt][0], acc[2][rt][1]); vv[2 * rt + 1] = pack2(acc[2][rt][2], acc[2][rt][3]); PIN2(vv[2 * rt], vv[2 * rt + 1]); }
#define PK_O(rt) { d0 = pack2(acc[3][rt][0], acc[3][rt][1]); d1 = pack2(acc[3][rt][2], acc[3][rt][3]); \
            asm volatile("ds_write2_b32 %0, %1, %2 offset0:%3 offset1:%4" ::"v"(scw), "v"(d0), "v"(d1), "n"(rt * 32), "n"(rt * 32 + 1) : "memory"); SB(); }
#define PK_S(rt) { const auto sw2 = __builtin_amdgcn_permlane16_swap(d0, d1, false, false); sq[rt] = sw2[0]; kop[rt][2] = sw2[1]; PIN2(sq[rt], kop[rt][2]); }
#define PK_Q(rt) { qop[rt][0] = pack2(acc[0][rt][0], acc[0][rt][1]); qop[rt][1] = pack2(acc[0][rt][2], acc[0][rt][3]); PIN2(qop[rt][0], qop[rt][1]); }
        ML(1, 4) ML(2, 4) ML(3, 4) ML(1, 3)
        PK_K(4) ML(2, 3) PK_V(4) ML(3, 3) PK_O(4) ML(0, 4) PK_S(4)
        if (half) {
            asm volatile("ds_write2_b64 %0, %1, %2 offset1:1" ::"v"(hx + lane * 16), "v"((u32x2){kop[4][0], kop[4][1]}), "v"((u32x2){vv[8], vv[9]}) : "memory");
            asm volatile("ds_write_b32 %0, %1" ::"v"(g < 2 ? hx + 1024 + lane * 4 : lb + L_TOUCH), "v"(kop[4][2]) : "memory");
            lds_st32(lb + C_HX + w * 4, 1);
        } else {
            if (rb2 < 15) {
                __builtin_amdgcn_raw_buffer_store_b128((u32x4){min(kop[4][0], 0xfffffffeu), min(kop[4][1], 0xfffffffeu), min(vv[8], 0xfffffffeu), min(vv[9], 0xfffffffeu)}, wsr, hxo, 0, 16);
                __builtin_amdgcn_raw_buffer_store_b32(min(g < 2 ? kop[4][2] : sq[4], 0xfffffffeu), wsr, hxp, 0, 16);
            }
            lds_add(lb + C_HS, lane);
        }
        SB();
        if (HX_AT == 4) { HX_LD(); SB(); }
        ML(0, 3) ML(1, 2) PK_K(3) ML(2, 2) PK_V(3) ML(3, 2) PK_O(3) ML(0, 2) PK_S(3)
        ML(1, 1) PK_Q(4) ML(2, 1) PK_Q(3) ML(3, 1) PK_K(2) ML(0, 1) PK_V(2)
        GT(20)
        TS(1);
        STAMP(2);
        CSTAMP(1);
        __builtin_amdgcn_s_setprio(T_PRIO);
        if (!half) __builtin_amdgcn_s_setprio(E_PRIO);
        if (HX_AT == 3) { HX_LD(); SB(); }
        PK_O(2) PK_Q(2) PK_S(2) PK_K(1) PK_V(1) PK_O(1) PK_Q(1) PK_S(1)
        if (HX_AT == 2) { HX_LD(); SB(); }
#pragma unroll
        for (int rt = 4; rt >= 1; --rt) {
            kop[rt][3] = 0u;
            qop[rt][2] = g < 2 ? sq[rt] : 0u;
            qop[rt][3] = 0u;
        }
        TS(2);
        bf16x8 vx[5];
#pragma unroll
        for (int rt = 4; rt >= 2; --rt) vx[rt] = cat8(TRRD((lds_char*)(size_t)vxa[rt - 1]), TRRD((lds_char*)(size_t)(vxa[rt - 1] + 128)));
        TS(3);
        STAMP(3);
        const float scale = 0.22360679774997896f * 1.4426950408889634f;
        const f32x4 z4 = {0.f, 0.f, 0.f, 0.f};
        float l0[5], l1[5], mx[5];
        u32x4 hl4;
        unsigned hl1, hx0;
#pragma unroll
        for (int rt = 4; rt >= 1; --rt) {
            if (rt == HL_AT) {
                SB();
                hx0 = lds_v32(lb + C_HX + w * 4);
                hl4 = lds_v128(hx + lane * 16);
                hl1 = lds_v32(hx + 1024 + (lane & 31) * 4);
                SB();
            }
            if (rt == 1) {
                SB();
                asm volatile("s_cmp_eq_u32 %2, 0\n\ts_cbranch_scc1 1f\n\ts_waitcnt vmcnt(0)\n1:" : "+v"(ha0), "+v"(hb0) : "s"(far) : "memory", "scc");
                if (half) {
                    if (__builtin_expect(__any(max(max(max(ha0[0], ha0[1]), max(ha0[2], ha0[3])), hb0) == 0xffffffffu), 0)) {
#pragma nounroll
                        for (int it = 0; it < SPIN_CAP; ++it) {
                            NHRETRY();
                            __builtin_amdgcn_s_sleep(HX_SLEEP);
                            HX_LD();
                            asm volatile("s_waitcnt vmcnt(0)" : "+v"(ha0), "+v"(hb0) :: "memory");
                            if (!__any(max(max(max(ha0[0], ha0[1]), max(ha0[2], ha0[3])), hb0) == 0xffffffffu)) break;
                        }
                    }
                    asm volatile("ds_write_b32 %0, %1" ::"v"(hxw), "v"(hb0) : "memory");
                } else {
                    if (__builtin_expect(!__builtin_amdgcn_readfirstlane(hx0), 0)) {
                        spin_ge(lb + C_HX + w * 4, 1);
                        hl4 = lds_v128(hx + lane * 16);
                        hl1 = lds_v32(hx + 1024 + (lane & 31) * 4);
                    }
                    ha0 = hl4;
                    hb0 = hl1;
                }
                kop[0] = (u32x4){ha0[0], ha0[1], hb0, 0u};
                vv[0] = ha0[2];
                vv[1] = ha0[3];
                vx[1] = cat8(TRRD((lds_char*)(size_t)vxa[0]), TRRD((lds_char*)(size_t)(vxa[0] + 128)));
                SB();
            }
            const f32x4 s0 = __builtin_amdgcn_mfma_f32_16x16x32_bf16(__builtin_bit_cast(bf16x8, kop[rt - 1]), __builtin_bit_cast(bf16x8, qop[rt]), cm0, 0, 0, 0);
            const f32x4 s1 = __builtin_amdgcn_mfma_f32_16x16x32_bf16(__builtin_bit_cast(bf16x8, kop[rt]), __builtin_bit_cast(bf16x8, qop[rt]), cm1, 0, 0, 0);
            if (rt == 1 && rb == 0) {
                l0[rt] = fmax_raw(s1[0], s1[1]);
                l1[rt] = fmax_raw(s1[2], s1[3]);
            } else {
                l0[rt] = fmax_raw(fmax3_raw(s0[0], s0[1], s1[0]), s1[1]);
                l1[rt] = fmax_raw(fmax3_raw(s0[2], s0[3], s1[2]), s1[3]);
            }
            mx[rt] = fmax_raw(l0[rt], l1[rt]);
        }
#define FU(v) __float_as_uint(v)
#define UF(v) __uint_as_float(v)
#define RED4(dst, src, OP) { \
            const auto ra_ = __builtin_amdgcn_permlane16_swap(FU(src[1]), FU(src[2]), false, false); \
            const auto rb_ = __builtin_amdgcn_permlane16_swap(FU(src[3]), FU(src[4]), false, false); \
            const auto rc_ = __builtin_amdgcn_permlane32_swap(FU(OP(UF(ra_[0]), UF(ra_[1]))), FU(OP(UF(rb_[0]), UF(rb_[1]))), false, false); \
            dst = OP(UF(rc_[0]), UF(rc_[1])); }
#define BC4(dst, src) { \
            const auto ra_ = __builtin_amdgcn_permlane32_swap(FU(src), FU(src), false, false); \
            const auto rb_ = __builtin_amdgcn_permlane16_swap(ra_[0], ra_[0], false, false); \
            const auto rc_ = __builtin_amdgcn_permlane16_swap(ra_[1], ra_[1], false, false); \
            dst[1] = UF(rb_[0]); dst[2] = UF(rb_[1]); dst[3] = UF(rc_[0]); dst[4] = UF(rc_[1]); }
#define FADD(a, b) ((a) + (b))
        SB(); TS(4); HINT(1); SB();
        float red;
        RED4(red, mx, fmax_raw);
        red *= scale;
        BC4(mx, red);
        f32x2 pp[5];
        float sm[5];
#pragma unroll
        for (int rt = 4; rt >= 1; --rt) {
            pp[rt] = (f32x2){__builtin_amdgcn_exp2f(__builtin_fmaf(l0[rt], scale, -mx[rt])), __builtin_amdgcn_exp2f(__builtin_fmaf(l1[rt], scale, -mx[rt]))};
            sm[rt] = pp[rt][0] + pp[rt][1];
        }
        RED4(red, sm, FADD);
        red = __builtin_amdgcn_rcpf(red);
        BC4(sm, red);
        SB(); TS(5);
        const unsigned ty0 = *(lds_vu32*)(size_t)(lb + C_TY), ow2 = *(lds_vu32*)(size_t)(lb + C_OW2);
        SB();
        f32x4 o1[5], o2 = z4;
#pragma unroll
        for (int rt = 4; rt >= 1; --rt) {
            const unsigned pk = pack2(pp[rt][0] * sm[rt], pp[rt][1] * sm[rt]);
            const u32x4 pop = {__builtin_amdgcn_perm(pk, pk, psel[0]), __builtin_amdgcn_perm(pk, pk, psel[1]), __builtin_amdgcn_perm(pk, pk, psel[2]), __builtin_amdgcn_perm(pk, pk, psel[3])};
            const u32x4 vop = {vv[2 * rt - 2], vv[2 * rt - 1], vv[2 * rt], vv[2 * rt + 1]};
            o1[rt] = __builtin_amdgcn_mfma_f32_16x16x32_bf16(__builtin_bit_cast(bf16x8, vop), __builtin_bit_cast(bf16x8, pop), z4, 0, 0, 0);
            o2 = __builtin_amdgcn_mfma_f32_16x16x32_bf16(vx[rt], __builtin_bit_cast(bf16x8, pop), o2, 0, 0, 0);
        }
        SB(); TS(6);
        u32x4 yv[3];
        unsigned g1;
        const unsigned l16y = lane * 16;
#define G_ASK() asm volatile("global_load_dword %0, %1, %2 sc1" : "=v"(g1) : "v"((lane & 15) * 4), "s"(ws + WS_G + ROWT(w) * 64) : "memory")
        if (__builtin_expect(!__builtin_amdgcn_readfirstlane(ow2), 0)) spin_ge(lb + C_OW2, 1);
        unsigned ty = half ? __builtin_amdgcn_readfirstlane(ty0) : 0;
        const unsigned yearly = __builtin_amdgcn_readfirstlane(YEARLY && ty && (int)(NOW() - ty) >= 0);
        if (yearly) { G_ASK(); Y_FIRST(); }
        SB();
#pragma unroll
        for (int rt = 3; rt >= 1; rt -= 2) {
            const auto r0 = __builtin_amdgcn_permlane16_swap(min(pack2(o1[rt][0], o1[rt][1]), 0xfffffffeu), min(pack2(o1[rt + 1][0], o1[rt + 1][1]), 0xfffffffeu), false, false);
            const auto r1 = __builtin_amdgcn_permlane16_swap(min(pack2(o1[rt][2], o1[rt][3]), 0xfffffffeu), min(pack2(o1[rt + 1][2], o1[rt + 1][3]), 0xfffffffeu), false, false);
            __builtin_amdgcn_raw_buffer_store_b128((u32x4){r0[0], r1[0], r0[1], r1[1]}, wsr, oo + (rt - 1) * 20480, 0, 16);
        }
        __builtin_amdgcn_raw_buffer_store_b64((u32x2){min(pack2(o2[0], o2[1]), 0xfffffffeu), min(pack2(o2[2], o2[3]), 0xfffffffeu)}, wsr, ox, WS_O + rb * 4 * 20480, 16);
        HINT(4);
        TS(7);
        TSFLUSH();
        lds_add(lb + C_OS1, lane);
        if (half) {
            STAMP(4);
            if (!yearly) {
                spin_mid(lb + C_LK, 1);
                G_ASK();
            }
            spin_all(lb + C_FREE + (lane & 7) * 4, 18);
            spin_ge(lb + C_WO, 4);
            Q_READ(G_Q);
            spin_ge(lb + C_OST, 4);
            lds_add(lb + C_QR, lane);
            asm volatile("s_cmp_eq_u32 %1, 0\n\ts_cbranch_scc0 1f\n\ts_waitcnt vmcnt(0)\n1:\n\ts_waitcnt vmcnt(6)" : "+v"(g1) : "s"(yearly) : "memory", "scc");
            G_MUL();
            if (!yearly) {
                if (w == 0 && !__builtin_amdgcn_readfirstlane(lds_ld32(lb + C_TY))) {
                    u64 cfm = ~0xffff0000ull;
                    unsigned tv = 0, tu = 0;
                    const unsigned cfo = CF_OFF();
                    CF_LOOP(cfo, ~0ull);
                }
                ty = TY_GET();
                Y_TIME(ty);
                Y_FIRST();
            }
            P_STAGE(yearly);
        }
        lds_add(lb + C_OST, lane);
        STAMP(4);
        TAIL_ADDR();
      } else {
        __builtin_amdgcn_s_setprio(L_PRIO);
        unsigned ev[9];
        {
            const int hi = lane >> 5, pos = lane & 31;
#pragma unroll
            for (int v = 0; v < 2; ++v) {
                const int row = 2 * w + hi + 8 * v;
                int L = pos ^ (2 * (v * 4 + (row & 3)));
                L = L > 29 ? L - 2 : L;
                const int part = L < 24 ? L >> 3 : (L - 24) >> 1;
                const int chunk = L < 24 ? (L & 7) + 2 * (hg & 1) : (L & 1) + ((hg & 1) ? 0 : 8);
                ev[v] = row * (NQKV * 2) + chunk * 16 + part * 1280 + hg * 160;
                ev[v + 2] = ev[v] + 16 * NQKV * 2;
            }
            const int ch = (lane & 7) ^ ((4 * (w & 1) + g) & 7);
#pragma unroll
            for (int j = 0; j < 4; ++j) ev[4 + j] = (rb2 * 128 + 8 * w + (lane >> 3) + 32 * j) * (D_DIM * 2) + ch * 16;
            ev[8] = 0;
        }
        ring_issue<4, 0, ring_cum(-1, 4)>(ev, x, wqkv, kr, lb + w * 1024);
        SB();
        waitvm<ring_cum(-1, 4) - ring_need(-1, 4)>();
        BARRIER();
        ring_issue<4, ring_cum(-1, 4), ring_cum(0, 4)>(ev, x, wqkv, kr, lb + w * 1024);
        SB();
        unsigned wf[2][3], qf[3];
#pragma unroll
        for (int v = 0; v < 2; ++v)
#pragma unroll
            for (int j = 0; j < 3; ++j) wf[v][j] = opaque(WO_OFF((((w + 2 * v) & 3) + 4 * j) * 64 + lane));
#pragma unroll
        for (int j = 0; j < 3; ++j) { const int c = min(j * 64 + lane, 159); const int row = c / 10, ch = c - row * 10; qf[j] = opaque(row * (D_DIM * 2) + ch * 16); }
        const unsigned sfo = opaque(CF_OFF());
        TAIL_ADDR();
        const unsigned fra = opaque(lb + C_FREE + (lane & 7) * 4);
        SB();
        unsigned c0, r0;
        ring_run17<4, 0>(ev, x, wqkv, kr, lb + w * 1024, c0, r0);
        STAMP(7);
#define WO_PART(chunk, v) { const char* wb_ = (const char*)wo + (size_t)(chunk) * (64 * D_DIM * 2); const unsigned sl_ = lb + ((w + 2 * (v)) & 3) * 1024; \
            constexpr int pos_ = (chunk) < 5 ? G_WO0 + (chunk) * 10240 : G_WO1 + ((chunk) - 5) * 10240; \
            GLS(wf[v][0], wb_, sl_, pos_); GLS(wf[v][1], wb_, sl_, pos_ + 4096); if (((w + 2 * (v)) & 3) < 2) GLS(wf[v][2], wb_, sl_, pos_ + 8192); }
#define FREED(J) { spin_all(fra, J); }
#define Q_PART() { _Pragma("unroll") for (int j = 0; j < 3; ++j) \
            if (j < 2 || lane < 32) GLS(qf[j], (const char*)x + (size_t)__builtin_amdgcn_readfirstlane(ROWT(w) * 16 * (D_DIM * 2) + oc * 160), lb + w * 2560, G_Q + j * 1024); }
        if (w == 2) {
            const unsigned t_ = NOW();
            const float f_ = TICKS_PER_CYCLE(t_, c0, r0);
            if (lane == 0) __builtin_amdgcn_raw_buffer_store_b128((u32x4){tlo, thi, t_ + (unsigned)(f_ * D0), t_ + (unsigned)(f_ * D1)}, wsr, WS_HN + (rb2 * 8 + hg) * 128, 0, 16);
            lds_st32(lb + C_TR + 4, t_ + (unsigned)(f_ * D0) + FB_T);
            lds_st32(lb + C_TR + 8, t_ + (unsigned)(f_ * D1) + FB_T);
        }
        u32x2 ov;
        const unsigned owo = WS_HG + (min(rb2 + (lane & 1), 15) * 8 + hg) * 128, ow8 = WS_HF + (rb2 * 8 + (lane & 7)) * 128;
        if (w == 1) asm volatile("s_mov_b64 exec, 3\n\tglobal_load_dwordx2 %0, %1, %2 sc1\n\ts_mov_b64 exec, -1" : "=v"(ov) : "v"(owo), "s"(ws) : "memory");
        WO_PART(0, 0);
        WO_PART(5, 1);
        WAIT_VM(9);
        BARRIER();
        WO_PART(1, 0);
        WO_PART(2, 1);
        asm volatile("s_waitcnt vmcnt(10)" : "+v"(ov) :: "memory");
        const bool own_ok = w != 1 || !(__builtin_amdgcn_ballot_w64(ov[0] != tlo || ov[1] != thi) & 3);
        if (w == 1 && own_ok) lds_st32(lb + C_OWN, 1);
        BARRIER();
        if (!own_ok) {
            poll_flags(wsr, owo, tlo, thi);
            lds_st32(lb + C_OWN, 1);
        }
#define OW2_LD() if (w == 0) asm volatile("s_mov_b64 exec, 0xff\n\tglobal_load_dwordx2 %0, %1, %2 sc1\n\ts_mov_b64 exec, -1" : "=v"(ov) : "v"(ow8), "s"(ws) : "memory")
#define OW2_USE() if (w == 0) { asm volatile("s_waitcnt vmcnt(5)" : "+v"(ov) :: "memory"); \
            if (__builtin_amdgcn_ballot_w64(ov[0] != tlo || ov[1] != thi) & 0xff) poll_flags(wsr, ow8, tlo, thi); \
            lds_st32(lb + C_OW2, 1); }
        if (OW2_AT == 0) OW2_LD();
        __builtin_amdgcn_s_setprio(WO_PRIO);
        STAMP(2);
        if (HS_POS == 0) spin_mid(lb + C_HS, HS_N);
        WO_PART(3, 0);
        WO_PART(4, 1);
        if (OW2_AT == 0) OW2_USE();
        if (HS_POS == 1) spin_mid(lb + C_HS, HS_N);
        if (OW2_AT == 1) OW2_LD();
        u32x4 fv;
        u64 cfm = ~0ull << 40;
        unsigned tv = 0, tu = 0;
#define LOOK_LD() if (w == 3) asm volatile("s_mov_b32 exec_hi, 0xff\n\tglobal_load_dwordx4 %0, %1, %2 sc1\n\ts_mov_b32 exec_hi, -1" : "=v"(fv) : "v"(sfo), "s"(ws) : "memory")
        if (LOOK_AT == 0) LOOK_LD();
        WO_PART(6, 0);
        WO_PART(7, 1);
        if (OW2_AT == 1) OW2_USE();
        if (LOOK_AT == 1) LOOK_LD();
        spin_all(fra, 17);
        WO_PART(8, 0);
        WO_PART(9, 1);
        Q_PART();
        if (LOOK_AT == 2) LOOK_LD();
        STAMP(3);
        spin_all(fra, 18);
        asm volatile("s_cmp_eq_u32 %0, %1\n\ts_cbranch_scc1 1f\n\ts_waitcnt vmcnt(0)\n1:\n\ts_waitcnt vmcnt(1)" :: "s"(w), "n"(LOOK_AT == 2 ? 3 : 9) : "memory", "scc");
        lds_add(lb + C_WO, lane);
        if (w == 3) {
            tv = lds_ld32(lb + C_TR + 4) | 1;
            tu = lds_ld32(lb + C_TR + 8) | 1;
            cfm |= 0xffull << 32;
            asm volatile("s_waitcnt vmcnt(0)" : "+v"(fv) :: "memory");
            CF_LOOK(fv);
            CF_LOOP(sfo, 0xffffull);
            lds_st32(lb + C_LK, 1);
        }
        STAMP(14);
        STAMP(4);
      }
    } else {
        __builtin_amdgcn_s_setprio(3);
        const int c5 = wave < 4 ? wave : 4;
        const int d = vid * 5 + c5;
        unsigned Dw = *(const __attribute__((address_space(4))) unsigned*)(size_t)(ssm_D + (d & ~1));
        unsigned hp0 = *(const __attribute__((address_space(4))) unsigned*)(size_t)(ssm_k + (vid * 5 + 4) * S_LEN + w * 512 - 2);
        BARRIER();
        if (wave <= 4) hp0 = 0;
        asm volatile("" : "+s"(Dw), "+s"(hp0));
#define WO_CHUNK(chunk, lds) { const char* wb_ = (const char*)wo + (size_t)(chunk) * (64 * D_DIM * 2); \
            _Pragma("unroll") for (int p = 0; p < 10; ++p) GLS(wof[p], wb_, lds, p * 1024); }
#define KV_DONE() do { if (lds_add_rtn(lb + C_KVD, lane) == 9) { \
            if (lane == 0) __hip_atomic_store((u64*)(ws + WS_XF + (cq * 32 + rbv) * 128), tag, __ATOMIC_RELAXED, __HIP_MEMORY_SCOPE_AGENT); lds_add(lb + C_KVD, lane); } } while (0)
        u32x4 hv[4];
        const unsigned l16 = lane * 16;
        const char* hsrc = (const char*)ssm_k + (size_t)d * (S_LEN * 2) + (wave < 4 ? 0 : w * 1024);
#define H_LD(i) asm volatile("global_load_dwordx4 %0, %1, %2 offset:%3" : "=v"(hv[i]) : "v"(l16), "s"(hsrc), "n"((i) * 1024) : "memory")
        if (wave < 10) {
            const int m0 = (g & 1) * 2 + (g >> 1);
            const unsigned kp = opaque(lb + V_KST + (8 * m0 + q4) * 320 + (m0 >> 1) * 32 + wave * 32 + p4 * 8);
            const unsigned km = opaque(kp - 1280 - ((m0 & 1) ? 0 : 32));
            const unsigned sw_ = opaque(lb + V_STG + wave * 2048 + n16 * 128 + ((m0 + n16) & 7) * 16);
            const unsigned sr_ = opaque(lb + V_STG + wave * 2048 + (lane >> 3) * 128 + ((lane + (lane >> 3)) & 7) * 16);
            const unsigned e1 = opaque(WS_KV + (cq * 160 + wave * 16 + (lane >> 3)) * KV_PITCH + rbv * 128 + (lane & 7) * 16), e2 = opaque(e1 + 8 * KV_PITCH);
            asm volatile("s_waitcnt vmcnt(0)" : "+v"(skw) :: "memory");
            lds_add(lb + C_KV, lane);
            const f32x2 s0 = {lo_f(skw[0]), lo_f(skw[0])}, s1 = {hi_f(skw[0]), hi_f(skw[0])}, s2 = {lo_f(skw[1]), lo_f(skw[1])}, s3 = {hi_f(skw[1]), hi_f(skw[1])};
            if (H_EARLY == 1 && wave < 8) { H_LD(0); if (wave < 4) { H_LD(1); H_LD(2); H_LD(3); } }
            spin_ge(lb + C_KV, 10);
            if (H_EARLY == 2 && wave < 8) { H_LD(0); if (wave < 4) { H_LD(1); H_LD(2); H_LD(3); } }
            STAMP(2);
            u32x2 kq[2][3], vq[2][2];
#pragma unroll
            for (int i = 0; i < 2; ++i) {
                kq[i][0] = __builtin_bit_cast(u32x2, TRRD((lds_char*)(size_t)(km + i * 10304)));
#pragma unroll
                for (int e = 0; e < 2; ++e) kq[i][e + 1] = __builtin_bit_cast(u32x2, TRRD((lds_char*)(size_t)(kp + i * 10304 + e * 1280)));
#pragma unroll
                for (int e = 0; e < 2; ++e) vq[i][e] = __builtin_bit_cast(u32x2, TRRD((lds_char*)(size_t)(kp + i * 10304 + (V_VST - V_KST) + e * 1280)));
            }
            if (rbv == 0 && m0 == 0) kq[0][0] = (u32x2){0u, 0u};
            f32x2 kf[12], res[8];
#pragma unroll
            for (int e = 1; e < 12; ++e) {
                const unsigned u0 = kq[0][e >> 2][(e >> 1) & 1], u1 = kq[1][e >> 2][(e >> 1) & 1];
                kf[e] = (e & 1) ? (f32x2){hi_f(u0), hi_f(u1)} : (f32x2){lo_f(u0), lo_f(u1)};
            }
#pragma unroll
            for (int e = 0; e < 8; ++e) {
                const unsigned u0 = vq[0][e >> 2][(e >> 1) & 1], u1 = vq[1][e >> 2][(e >> 1) & 1];
                f32x2 a = kf[e + 1] * s3;
                a = kf[e + 2] * s2 + a;
                a = kf[e + 3] * s1 + a;
                a = kf[e + 4] * s0 + a;
                res[e] = a * ((e & 1) ? (f32x2){hi_f(u0), hi_f(u1)} : (f32x2){lo_f(u0), lo_f(u1)});
            }
#pragma unroll
            for (int i = 0; i < 2; ++i) {
                u32x4 pk;
#pragma unroll
                for (int e = 0; e < 4; ++e) pk[e] = pack2(res[2 * e][i], res[2 * e + 1][i]);
                *(lds_vu32x4*)(size_t)(sw_ ^ (i * 64)) = pk;
            }
            const u32x4 pk0 = lds_v128(sr_), pk1 = lds_v128(sr_ + 1024);
            __builtin_amdgcn_raw_buffer_store_b128(pk0, wsr, e1, 0, 16);
            __builtin_amdgcn_raw_buffer_store_b128(pk1, wsr, e2, 0, 16);
            lds_add(lb + C_KVST, lane);
            STAMP(3);
        }
        if (wave >= 8) {
            const unsigned wv = T_WV(), xv_ = T_XV();
            unsigned wof[10], qf[3];
#define AUX_ADDR() { _Pragma("unroll") for (int p = 0; p < 10; ++p) wof[p] = opaque(WO_OFF(p * 64 + lane)); \
                _Pragma("unroll") for (int j = 0; j < 3; ++j) { const int c = min(j * 64 + lane, 159); const int row = c / 10, ch = c - row * 10; qf[j] = opaque(row * (D_DIM * 2) + ch * 16); } \
                SB(); }
            if (wave == 10) {
                T_PAIR(2);
                AUX_ADDR();
                if (T4_W) spin_ge(lb + T4_W, T4_N);
                T_PAIR(4);
                if (WO_MALL && lane < 50) GLT((unsigned)((vid * 50 + lane) * 128), wo);
            } else if (wave == 11) {
                if (lane < 56) {
#pragma unroll
                    for (int c5 = 0; c5 < 5; ++c5)
                        *(lds_u32x4*)(size_t)(lb + (lane < 16 ? V_KV(c5) + lane * 16 : V_F + c5 * XC_BYTES + 15 * XC_ROW + 256 + (lane < 36 ? XC_COPY + (lane - 16) * 16 : XC_B + 4 + (lane - 36) * 16))) = (u32x4){0u, 0u, 0u, 0u};
                }
                const unsigned x4o = opaque(WS_KV + d * KV_PITCH + lane * 16);
                AUX_ADDR();
                spin_ge(lb + C_KVD, 11);
                if (POLL_DELAY > 0) __builtin_amdgcn_s_sleep(POLL_DELAY);
                const unsigned xfo = WS_XF + ((cq ^ CQ_X) * 32 + (lane & 31)) * 128;
                STAMP(3);
                bool ok = lane >= 32;
#pragma nounroll
                for (int i = 0; i < SPIN_CAP; ++i) {
                    asm volatile("" ::: "memory");
                    if (!ok) { const u32x2 v = __builtin_amdgcn_raw_buffer_load_b64(wsr, xfo, 0, 16); ok = v[0] == tlo && v[1] == thi; }
                    if (__all(ok)) break;
                    __builtin_amdgcn_s_sleep(POLL_SLEEP);
                }
                asm volatile("s_mov_b32 m0, %2\n\ts_nop 0\n\tglobal_load_lds_dwordx4 %0, %1 sc1\n\tglobal_load_lds_dwordx4 %0, %1 offset:1024 sc1\n\t"
                             "global_load_lds_dwordx4 %0, %1 offset:2048 sc1\n\tglobal_load_lds_dwordx4 %0, %1 offset:3072 sc1" ::"v"(x4o), "s"(ws), "s"(lb + V_KV4 + 256) : "memory");
                lds_add(lb + C_KVR, lane);
                STAMP(2);
                WAIT_VM(0);
                lds_st32(lb + C_TR, NOW());
                lds_st32(lb + C_TR + 4, (unsigned)__builtin_amdgcn_s_memtime());
                lds_st32(lb + C_HR, 4);
                STAMP(12);
                spin_ge(lb + C_XD, 4);
                lds_st32(lb + C_GO, 1);
            } else {
                WAIT_VM(0);
                KV_DONE();
                STAMP(11);
                const unsigned c8 = (wave - 8) * 80 + (lane >> 3), so = opaque(WS_Y + vid * 640 + (lane & 7) * 16);
                unsigned sy[10];
#pragma unroll
                for (int i = 0; i < 10; ++i) { const unsigned c = c8 + i * 8, b = c / 5; sy[i] = opaque(so + b * (D_DIM * 128) + (c - b * 5) * 128); }
#define O_FLAG() { if (lds_add_rtn(lb + C_OSC, lane) == 1) { if (lane == 0) __hip_atomic_store((u64*)(ws + WS_HF + sid * 128), tag, __ATOMIC_RELAXED, __HIP_MEMORY_SCOPE_AGENT); } }
                spin_ge(lb + C_KVD, 11);
#pragma unroll
                for (int i = 0; i < 3; ++i) if (i < 2 || lane < 32) __builtin_amdgcn_raw_buffer_store_b128(ones, wsr, WS_HX + sid * 5120 + (wave - 8) * 2560 + lane * 16 + i * 1024, 0, 16);
                if (lane < 2) __builtin_amdgcn_raw_buffer_store_b128(ones, wsr, WS_G + sid * 64 + (wave - 8) * 32 + lane * 16, 0, 16);
#pragma unroll
                for (int i = 0; i < OS_PRE; ++i) __builtin_amdgcn_raw_buffer_store_b128(ones, wsr, WS_O + sid * 20480 + (wave - 8) * 10240 + lane * 16 + i * 1024, 0, 16);
                waitvm<OS_PRE>();
                if (lds_add_rtn(lb + C_HGC, lane) == 1) { if (lane == 0) __hip_atomic_store((u64*)(ws + WS_HG + sid * 128), tag, __ATOMIC_RELAXED, __HIP_MEMORY_SCOPE_AGENT); }
                AUX_ADDR();
                if (OS_PRE == 10) { WAIT_VM(0); O_FLAG(); STAMP(11); }
                if (PRIO_EARLY) __builtin_amdgcn_s_setprio(AUX_PRIO);
                spin_ge(lb + SENT_W, SENT_N);
#pragma unroll
                for (int i = OS_PRE; i < 10; ++i) __builtin_amdgcn_raw_buffer_store_b128(ones, wsr, WS_O + sid * 20480 + (wave - 8) * 10240 + lane * 16 + i * 1024, 0, 16);
#pragma unroll
                for (int i = 0; i < 10; ++i) {
                    if (OS_PRE < 10 && i == YS_N) { waitvm<YS_N>(); O_FLAG(); STAMP(11); }
                    __builtin_amdgcn_raw_buffer_store_b128(ones, wsr, sy[i], 0, 16);
                }
                STAMP(4);
                if (WO2_FIRST) { WO_CHUNK(2 + w, lb + V_WO + (2 + w) * 10240); WAIT_VM(10); } else WAIT_VM(0);
                if (lds_add_rtn(lb + C_OS1, lane) == 1) {
                    lds_st32(lb + C_OWN, 1);
                    spin_ge(lb + C_HR, 4);
                    const unsigned r0 = lds_ld32(lb + C_TR), c0 = lds_ld32(lb + C_TR + 4);
#pragma nounroll
                    for (int it = 0; it < 1000 && (int)(NOW() - __builtin_amdgcn_readfirstlane(r0)) < 60; ++it) __builtin_amdgcn_s_sleep(2);
                    const unsigned t_ = NOW();
                    const float f_ = TICKS_PER_CYCLE(t_, c0, r0);
                    if (lane == 0) __builtin_amdgcn_raw_buffer_store_b128((u32x4){tlo, thi, r0 + (unsigned)(f_ * DY), 0u}, wsr, WS_SF + vid * 128, 0, 16);
                }
                STAMP(12);
            }
            __builtin_amdgcn_s_setprio(AUX_PRIO);
            if (wave == 10) {
                if (!X_TALL) spin_ge(lb + GO_W, GO_N);
                T_PAIR(6);
                if (T8_AT == 1) WAIT_VM(1); else if (T8_AT == 2) spin_ge(lb + C_XD, 4);
                T_PAIR(8);
                spin_mid(lb + WOT_W, WOT_N);
#pragma unroll
                for (int i = 0; i < 4; ++i) {
                    const int l0 = r * 240 + i * 60 + lane;
                    waitvm<WOT_PACE>();
                    if (lane < 60) GLT((unsigned)((l0 < 1280 ? l0 : l0 + 2560) * 128), wo);
                }
                {
                    const unsigned cl = lane * 16 + r;
                    if (cl < CODE_LINES) GLT(cl * 128, code0);
                }
                WAIT_VM(0);
                lds_st32(lb + C_WT, 1);
                STAMP(4);
                WO_CHUNK(4, lb + V_WO + 4 * 10240);
            }
            if (wave == 11 || (wave < 10 && !WO2_FIRST)) {
                spin_mid(lb + WO2_W, WO2_N);
                WO_CHUNK(2 + w, lb + V_WO + (2 + w) * 10240);
            }
#pragma unroll
            for (int j = 0; j < 3; ++j)
                if (j < 2 || lane < 32) GLS(qf[j], (const char*)x + (size_t)(ROWT(w) * 16) * (D_DIM * 2) + oc * 160, lb + w * 2560, V_QS + j * 1024);
            WAIT_VM(0);
            STAMP(5);
            Q_READ(V_QS);
            lds_add(lb + C_QR, lane);
            if (w < 2) {
                spin_mid(lb + (w ? C_QR : C_RED2), 4);
                spin_mid(lb + C_WT, 1);
                WO_CHUNK(w, lb + V_WO + w * 10240);
            }
            WAIT_VM(0);
            lds_add(lb + C_WO, lane);
            STAMP(6);
            if (wave == 10) {
                u64 cfm = ~0ull << 40;
                unsigned tv = 0, tu = 0;
                const unsigned cfo = CF_OFF();
                if (VL_DELAY > 0) __builtin_amdgcn_s_sleep(VL_DELAY);
                CF_LOOP(cfo, ~0ull);
                STAMP(10);
            }
            const unsigned ty = TY_GET();
            G_FIRST();
            G_MUL();
            spin_mid(lb + C_CONVN, 8);
            __builtin_amdgcn_s_setprio(3);
            u32x4 yv[3];
            const unsigned l16y = lane * 16;
            Y_TIME(ty);
            Y_FIRST();
            STAMP(7);
            P_STAGE(false);
        } else {
            const unsigned xb = lb + V_F + c5 * XC_BYTES;
            const unsigned hb = lb + V_KV(c5) + 256;
            const float Dd = (d & 1) ? hi_f(Dw) : lo_f(Dw);
            u32x4 gx[5], gv[5];
            if (H_EARLY) WAIT_VM(0);
            else {
                spin_ge(lb + C_KVST, 10);
                H_LD(0);
                if (wave < 4) { H_LD(1); H_LD(2); H_LD(3); WAIT_VM(4); }
                else if (GATE_EARLY) {
                    const unsigned go = (sid * 16 + w * 4 + g) * (D_DIM * 2) + n16 * 16;
#pragma unroll
                    for (int i = 0; i < 5; ++i) {
                        asm volatile("global_load_dwordx4 %0, %1, %2 offset:%3" : "=v"(gx[i]) : "v"(go), "s"(x), "n"(i * 256) : "memory");
                        asm volatile("global_load_dwordx4 %0, %1, %2 offset:%3" : "=v"(gv[i]) : "v"(n16 * 16), "s"(gw), "n"(i * 256) : "memory");
                    }
                    WAIT_VM(11);
                } else WAIT_VM(1);
            }
            KV_DONE();
            STAMP(4);
            {
                const unsigned da = xb + (3 - g) * XC_ROW + (15 - n16) * 16 + (wave < 4 ? 0 : -w * (4 * XC_ROW));
                u32x4 ra[4], rb[4];
                asm volatile("s_cmp_eq_u32 %4, 0\n\ts_cbranch_scc0 1f\n\ts_waitcnt vmcnt(0)\n1:\n\ts_waitcnt vmcnt(10)" : "+v"(hv[0]), "+v"(hv[1]), "+v"(hv[2]), "+v"(hv[3]) : "s"(GATE_EARLY * (wave >> 2)) : "memory", "scc");
#define H_PREV(i) __builtin_amdgcn_update_dpp((i) == 0 ? hp0 : __builtin_amdgcn_readlane(hv[(i) == 0 ? 0 : (i) - 1][3], 63), hv[i][3], 0x138, 0xf, 0xf, false)
#define BFI16(lo, hi) (((lo) & 0xffffu) | ((hi) & 0xffff0000u))
#define H_PREP(i) { ra[i] = (u32x4){__builtin_amdgcn_alignbit(hv[i][3], hv[i][3], 16), __builtin_amdgcn_alignbit(hv[i][2], hv[i][2], 16), __builtin_amdgcn_alignbit(hv[i][1], hv[i][1], 16), __builtin_amdgcn_alignbit(hv[i][0], hv[i][0], 16)}; \
                    rb[i] = (u32x4){BFI16(hv[i][3], hv[i][2]), BFI16(hv[i][2], hv[i][1]), BFI16(hv[i][1], hv[i][0]), BFI16(hv[i][0], H_PREV(i))}; \
                    *(lds_u32x4*)(size_t)(da + (3 - (i)) * (4 * XC_ROW) + XC_COPY) = ra[i]; *(lds_u32x4*)(size_t)(da + (3 - (i)) * (4 * XC_ROW) + XC_B + 4) = rb[i]; }
#define H_DUP(i) { *(lds_u32x4*)(size_t)(da + (3 - (i)) * (4 * XC_ROW) + XC_COPY - 32) = ra[i]; *(lds_u32x4*)(size_t)(da + (3 - (i)) * (4 * XC_ROW) + XC_B + 4 - 32) = rb[i]; }
                H_PREP(0)
                if (wave < 4) {
                    H_PREP(1) H_PREP(2) H_PREP(3)
                    if (n16 == 15) { H_DUP(0) H_DUP(1) H_DUP(2) if (g < 3) H_DUP(3) }
                } else {
                    if (n16 == 15 && (w < 3 || g < 3)) H_DUP(0)
                }
            }
            STAMP(9);
            const int j0 = wave >= 4 ? 2 * w : 0;
            const int m16 = HYB ? n16 : (n16 & 3) | ((n16 & 4) << 1) | ((n16 & 8) >> 1);
            const unsigned kp = hb + 2 * (256 * j0 + 128 * (g >> 1) + 8 * m16 + 4 * (g & 1));
            const int ts = 256 * (j0 + (g & 1)) + 128 * (g >> 1) + 8 * m16;
            const unsigned yo = WS_Y + d * 128 + (HYB ? (ts >> 6) * (D_DIM * 128) + (ts & 63) * 2 : ((ts >> 7) * 2 + ((ts >> 5) & 1)) * (D_DIM * 128) + (((ts >> 6) & 1) * 32 + (ts & 31)) * 2);
            if (wave < 4) {
                const int b1 = n16 >> 3, phi = n16 & 7;
                const unsigned xp = xb + ((phi & 1) ? XC_COPY - 4 : XC_B) + (1 - b1) * XC_ROW + 2 * (8 - 2 * (phi >> 1) + 8 * g);
                const unsigned hp = hb + 2 * (8 * (m16 + g) - 120);
                const int sx4 = (4 - w) * XC_BYTES + (w >> 1) * XC_ROW + (w & 1) * 128, sh4 = V_KV4 - w * KV_BYTES + w * 128;
                unsigned c0_, c1_;
                __builtin_amdgcn_s_setprio(CONV_PRIO);
                spin_ge(lb + C_KVR, 5);
                STAMP(6);
                CSTAMP(0);
                TS(11);
                asm volatile(CONV_ASM
                             : [cnt] "=&s"(c0_), [tmp] "=&s"(c1_)
                             : [xp] "v"(xp), [hp] "v"(hp), [sx4] "s"(sx4), [sh4] "s"(sh4), [kvl] "s"(hb), [kvr] "v"(kp), [scr] "v"(lb + V_SCR + w * 8192 + l16),
                               [ctl] "v"(lb + L_CTRL), [dd] "s"(Dd), [cap] "s"(SPIN_CAP), [xo] "v"(WS_KV + d * KV_PITCH + l16), [wsp] "s"(ws), [yo] "v"(yo)
                             : CONV_CLOB);
                __builtin_amdgcn_s_setprio(3);
                TAIL_ADDR();
                CSTAMP(1);
            } else {
                lds_add(lb + C_KVR, lane);
                const int grow = sid * 16 + w * 4 + g;
                const unsigned go = opaque(grow * (D_DIM * 2) + n16 * 16);
                if (!GATE_EARLY) {
#pragma unroll
                    for (int i = 0; i < 5; ++i) gv[i] = *(const u32x4*)((const char*)gw + n16 * 16 + i * 256);
                }
                unsigned wof[10];
#pragma unroll
                for (int p = 0; p < 10; ++p) wof[p] = opaque(WO_OFF(p * 64 + lane));
                TAIL_ADDR();
                SB();
                __builtin_amdgcn_s_setprio(AUX_PRIO);
                {
                    if (GATE_LATE) spin_mid(lb + GATE_W, GATE_N);
                    if (GATE_EARLY) asm volatile("s_waitcnt vmcnt(0)" : "+v"(gx[0]), "+v"(gx[1]), "+v"(gx[2]), "+v"(gx[3]), "+v"(gx[4]), "+v"(gv[0]), "+v"(gv[1]), "+v"(gv[2]), "+v"(gv[3]), "+v"(gv[4]) :: "memory");
                    else {
#pragma unroll
                        for (int i = 0; i < 5; ++i) gx[i] = *(const u32x4*)((const char*)x + go + i * 256);
                    }
                    float sa[4] = {0.f, 0.f, 0.f, 0.f};
#pragma unroll
                    for (int i = 0; i < 5; ++i)
#pragma unroll
                        for (int e = 0; e < 4; ++e) sa[e] = __builtin_fmaf(hi_f(gx[i][e]), hi_f(gv[i][e]), __builtin_fmaf(lo_f(gx[i][e]), lo_f(gv[i][e]), sa[e]));
                    float sacc = (sa[0] + sa[1]) + (sa[2] + sa[3]);
                    sacc += __builtin_amdgcn_update_dpp(0.f, sacc, 0x128, 0xf, 0xf, true);
                    sacc += __builtin_amdgcn_update_dpp(0.f, sacc, 0x124, 0xf, 0xf, true);
                    sacc += __builtin_amdgcn_update_dpp(0.f, sacc, 0x122, 0xf, 0xf, true);
                    sacc += __builtin_amdgcn_update_dpp(0.f, sacc, 0x121, 0xf, 0xf, true);
                    const float gres = rbf(__builtin_amdgcn_rcpf(1.f + __expf(-rbf(sacc))));
                    spin_ge(lb + C_HGC, 2);
                    if (n16 == 0) __builtin_amdgcn_raw_buffer_store_b32(__float_as_uint(gres), wsr, WS_G + grow * 4, 0, 16);
                    STAMP(6);
                }
                spin_ge(lb + C_RED, 4);
                STAMP(5);
                f32x4 acc[2];
#pragma unroll
                for (int e = 0; e < 2; ++e) {
                    const u32x2 kk = *(const lds_u32x2*)(size_t)(kp + e * 512);
                    acc[e] = (f32x4){lo_f(kk[0]) * Dd, hi_f(kk[0]) * Dd, lo_f(kk[1]) * Dd, hi_f(kk[1]) * Dd};
#pragma unroll
                    for (int o = 0; o < 4; ++o) acc[e] += *(const lds_f32x4*)(size_t)(lb + V_SCR + o * 8192 + (2 * w + e) * 1024 + l16);
                }
                lds_add(lb + C_RED2, lane);
                const auto r0 = __builtin_amdgcn_permlane16_swap(min(pack2(acc[0][0], acc[0][1]), 0xfffffffeu), min(pack2(acc[1][0], acc[1][1]), 0xfffffffeu), false, false);
                const auto r1 = __builtin_amdgcn_permlane16_swap(min(pack2(acc[0][2], acc[0][3]), 0xfffffffeu), min(pack2(acc[1][2], acc[1][3]), 0xfffffffeu), false, false);
                spin_mid(lb + C_OWN, 1);
                __builtin_amdgcn_raw_buffer_store_b128((u32x4){r0[0], r1[0], r0[1], r1[1]}, wsr, yo, 0, 16);
                STAMP(7);
                spin_ge(lb + C_RED2, 4);
                spin_mid(lb + C_WT, 1);
                WO_CHUNK(6 + w, lb + V_WO + (6 + w) * 10240);
                WAIT_VM(0);
                lds_add(lb + C_WO, lane);
            }
            lds_add(lb + C_CONVN, lane);
            STAMP(12);

        }
    }

#define NPOLL(ph, it)
#define NRETRY()
#define V_WAITC 4
#define W0_DELAY 0
#define W1_DELAY 0
#define H_DELAY 0
#define WA0 2
#define WA1 7
#define PRE_PRIO 0
#define PASS1_PRIO 2
    {
        FLUSH_LO();
        const f32x4 z4 = {0.f, 0.f, 0.f, 0.f};
        STAMP(12);
        __builtin_amdgcn_s_setprio(ENT_PRIO);
        unsigned ta, tb;
#define TM_RD() { u32x2 t_; asm volatile("ds_read_b64 %0, %1\n\ts_waitcnt lgkmcnt(0)" : "=v"(t_) : "v"(lb + C_TM + DUE_H * 16 + kh * 8) : "memory"); \
            ta = __builtin_amdgcn_readfirstlane(t_[0]); tb = __builtin_amdgcn_readfirstlane(t_[1]); }
        spin_mid(lb + C_WO, isG ? 4 : 8);
        TM_RD();
        const bool sw = (int)(ta - tb) > 0;
        const int k0 = sw * 5120, k1 = 5120 - k0;
        lds_char* bc[2] = {(lds_char*)(size_t)(pc + k0 * 5), (lds_char*)(size_t)(pc + k1 * 5)};
        lds_char* bd[2] = {(lds_char*)(size_t)(pd + k0 * 5), (lds_char*)(size_t)(pd + k1 * 5)};
#define WO_RD(s, ct) cat8(TRRD(bc[(s) / 5] + ((s) % 5) * 5120 + (ct) * 32), TRRD(bd[(s) / 5] + ((s) % 5) * 5120 + (ct) * 32))
#define WO_PRE(s0) { _Pragma("unroll") for (int s = s0; s < s0 + NBUF; ++s) _Pragma("unroll") for (int ct = 0; ct < 5; ++ct) wB[s % NBUF][ct] = WO_RD(s, ct); SB(); }
        bf16x8 wB[NBUF][5];
        WO_PRE(0);
        STAMP(14);
#pragma nounroll
        for (int it = 0; it < SPIN_CAP && !(ta && tb); ++it) {
            __builtin_amdgcn_s_sleep(1);
            TM_RD();
        }
        const unsigned t0 = (sw ? tb : ta) - (isG ? G_LEAD : 0), t1 = (sw ? ta : tb) - (isG ? G_LEAD : 0);
#define T_WAIT(t) { _Pragma("nounroll") for (int it = 0; it < 20000 && (int)(NOW() - (t)) < 0; ++it) {} }
        T_WAIT(t0);
        if (isG) spin_ge(lb + C_OS1, 8);
        __builtin_amdgcn_s_setprio(T_PRIO);
        const unsigned m = __builtin_amdgcn_readfirstlane((int)(NOW() + T_JOIN - t1) >= 0 ? 15 : 3);
        STAMP(8);
        u32x4 of[10];
        const char *ws0 = ws + k0, *ws1 = ws + k1;
#define O_LD(s) asm volatile("global_load_dwordx4 %0, %1, %2 offset:%3 sc1" : "=v"(of[s]) : "v"(ofo), "s"((s) < 5 ? ws0 : ws1), "n"(((s) % 5) * 1024 - 2048) : "memory")
#define O_WAIT(s) { if ((s) >= 5) asm volatile("s_waitcnt vmcnt(%1)" : "+v"(of[s]) : "n"(9 - (s)) : "memory"); \
            else asm volatile("s_cmp_eq_u32 %1, 15\n\ts_cbranch_scc1 1f\n\ts_waitcnt vmcnt(%2)\n1:\n\ts_waitcnt vmcnt(%3)" : "+v"(of[s]) : "s"(m), "n"(4 - (s) % 5), "n"(9 - (s)) : "memory", "scc"); }
#pragma unroll
        for (int s = 0; s < 5; ++s) O_LD(s);
        if (m == 15) {
#pragma unroll
            for (int s = 5; s < 10; ++s) O_LD(s);
        }
        __builtin_amdgcn_s_waitcnt(0xc07f);
        SB();
        if (ph) __builtin_amdgcn_s_setprio(PASS1_PRIO); else __builtin_amdgcn_s_setprio(PASS_PRIO);
        STAMP(9);
        f32x4 acc[2][5];
#define GROUP(i) { _Pragma("unroll") for (int s = 5 * i; s < 5 * i + 5; ++s) _Pragma("unroll") for (int ct = 0; ct < 5; ++ct) { \
                if (ct == 0) O_WAIT(s); \
                acc[i][ct] = __builtin_amdgcn_mfma_f32_16x16x32_bf16(wB[s % NBUF][ct], __builtin_bit_cast(bf16x8, of[s]), s % 5 ? acc[i][ct] : z4, 0, 0, 0); SB(); \
                if (s + NBUF < 10) { wB[s % NBUF][ct] = WO_RD(s + NBUF, ct); SB(); } } }
#define AGAIN(i) if (__builtin_expect(__any(acc[i][0][0] != acc[i][0][0]), 0)) { \
            NRETRY(); \
            _Pragma("unroll") for (int s = 5 * i; s < 5 * i + 5; ++s) O_LD(s); \
            WO_PRE(5 * i); \
            _Pragma("nounroll") \
            for (int it = 0; it < SPIN_CAP; ++it) { \
                asm volatile("s_waitcnt vmcnt(0)" : "+v"(of[5 * i]), "+v"(of[5 * i + 1]), "+v"(of[5 * i + 2]), "+v"(of[5 * i + 3]), "+v"(of[5 * i + 4]) :: "memory"); \
                bool any_ = false; \
                _Pragma("unroll") for (int s = 5 * i; s < 5 * i + 5; ++s) if (SENT4(of[s])) { \
                    asm volatile("global_load_dwordx4 %0, %1, %2 offset:%3 sc1" : "+v"(of[s]) : "v"(ofo), "s"((s) < 5 ? ws0 : ws1), "n"(((s) % 5) * 1024 - 2048) : "memory"); any_ = true; } \
                if (!any_) break; \
            } \
            _Pragma("unroll") for (int s = 5 * i; s < 5 * i + 5; ++s) _Pragma("unroll") for (int ct = 0; ct < 5; ++ct) { \
                acc[i][ct] = __builtin_amdgcn_mfma_f32_16x16x32_bf16(wB[s % NBUF][ct], __builtin_bit_cast(bf16x8, of[s]), s % 5 ? acc[i][ct] : z4, 0, 0, 0); SB(); \
                if (s + NBUF < 5 * i + 5) { wB[s % NBUF][ct] = WO_RD(s + NBUF, ct); SB(); } } }
        GROUP(0);
        if (m != 15) {
            T_WAIT(t1);
#pragma unroll
            for (int s = 5; s < 10; ++s) O_LD(s);
            __builtin_amdgcn_s_waitcnt(0xc07f);
            SB();
        }
        STAMP(10);
        unsigned fr = 4;
        if (!isG || ph) fr = lds_v32(lb + (isG ? C_QR : C_CONVN));
        SB();
        GROUP(1);
        AGAIN(0);
        AGAIN(1);
        __builtin_amdgcn_s_setprio(T_PRIO);
        STAMP(11);
        if (__builtin_amdgcn_readfirstlane(fr) < (isG ? 4 : 8)) spin_ge(lb + (isG ? C_QR : C_CONVN), isG ? 4 : 8);
#pragma unroll
        for (int ct = 0; ct < 5; ++ct) *(lds_f32x4*)(size_t)(xa + ct * 1024) = acc[0][ct] + acc[1][ct];
        lds_add(lb + C_XCH + trt * 4, lane);
        FLUSH();
    }
}

#undef x
#undef kin
#undef vin
#undef shift_k
#undef ssm_k
#undef ssm_D
#undef wqkv
#undef wo
#undef gw
#undef ws
#undef out
#undef wsr
extern "C" void kernel_launch(void* const* d_in, const int* in_sizes, int n_in, void* d_out, int out_size,
                              void* d_ws, size_t ws_size, hipStream_t stream) {
    const u16* q = (const u16*)d_in[0];
    const u16* k = (const u16*)d_in[1];
    const u16* v = (const u16*)d_in[2];
    const u16* shift_k = (const u16*)d_in[3];
    const u16* ssm_k = (const u16*)d_in[4];
    const u16* ssm_D = (const u16*)d_in[5];
    const u16* wqkv = (const u16*)d_in[7];
    const u16* wo = (const u16*)d_in[8];
    const u16* gw = (const u16*)d_in[9];
    fused_kernel<<<256, 768, 0, stream>>>(q, wqkv, k, v, shift_k, ssm_k, ssm_D, wo, gw, (char*)d_ws, (u16*)d_out);
}
```
